```python
import torch

def _softmax(x, axis=-1):
    return torch.softmax(x, dim=axis)
def _gelu(x):
    return torch.nn.functional.gelu(x, approximate="tanh")
def _silu(x):
    return torch.nn.functional.silu(x)
def _sigmoid(x):
    return torch.sigmoid(x)
def _layernorm(x, axis=-1, eps=1e-5):
    m = x.mean(dim=axis, keepdim=True)
    v = x.var(dim=axis, keepdim=True, unbiased=False)
    return (x - m) / torch.sqrt(v + eps)
def _scatter_add(tgt, idx, src, axis=0):
    return tgt.clone().index_add_(axis, idx, src)
def _segment_sum(src, seg, n):
    out = torch.zeros((n,) + src.shape[1:], dtype=src.dtype)
    return out.index_add_(0, seg, src)
def _pad(x, pw):
    flat = tuple(v for pair in reversed(pw) for v in pair)
    return torch.nn.functional.pad(x, flat)

kernel_name = 'concat_heads__mha_block_cached__mha_block_cached__near_ridge'

def setup_inputs():

    _w = (lambda a: (lambda t: t.to(torch.bfloat16) if t.is_floating_point() else t)(torch.as_tensor(a)))
    def _A_setup():
        B, Sq, D = 8, 144, 640
        H, Sk, Dh = 32, 512, 128
        return {
            "x": _w((lambda *s: torch.randn(*s, dtype=torch.bfloat16) * 0.8)(B, Sq, D)),
            "k_cache": _w((lambda *s: torch.randn(*s, dtype=torch.bfloat16) * 0.8)(B, H, Sk, Dh)),
            "v_cache": _w((lambda *s: torch.randn(*s, dtype=torch.bfloat16) * 0.05)(B, H, Sk, Dh)),
            "wq": _w((lambda *s: torch.randn(*s, dtype=torch.bfloat16) * 0.8)(D, H * Dh) * (D ** -0.5)),
            "wo": _w((lambda *s: torch.randn(*s, dtype=torch.bfloat16) * 0.05)(H * Dh, D) * ((H * Dh) ** -0.5)),
        }
    _A_in = _A_setup()

    def _B_setup():
        B, Sq, D = 8, 144, 640
        H, Sk, Dh = 32, 512, 128
        return {
            "x": _w((lambda *s: torch.randn(*s, dtype=torch.bfloat16) * 0.8)(B, Sq, D)),
            "k_cache": _w((lambda *s: torch.randn(*s, dtype=torch.bfloat16) * 0.8)(B, H, Sk, Dh)),
            "v_cache": _w((lambda *s: torch.randn(*s, dtype=torch.bfloat16) * 0.05)(B, H, Sk, Dh)),
            "wq": _w((lambda *s: torch.randn(*s, dtype=torch.bfloat16) * 0.8)(D, H * Dh) * (D ** -0.5)),
            "wo": _w((lambda *s: torch.randn(*s, dtype=torch.bfloat16) * 0.05)(H * Dh, D) * ((H * Dh) ** -0.5)),
        }
    _B_in = _B_setup()

    return {
        "A_x": _A_in["x"],
        "A_k_cache": _A_in["k_cache"],
        "A_v_cache": _A_in["v_cache"],
        "A_wq": _A_in["wq"],
        "A_wo": _A_in["wo"],
        "B_x": _B_in["x"],
        "B_k_cache": _B_in["k_cache"],
        "B_v_cache": _B_in["v_cache"],
        "B_wq": _B_in["wq"],
        "B_wo": _B_in["wo"],
    }

def reference(A_x, A_k_cache, A_v_cache, A_wq, A_wo, B_x, B_k_cache, B_v_cache, B_wq, B_wo):
    def _A_ref(x, k_cache, v_cache, wq, wo):
        B, Sq, D = x.shape
        H, Dh = 32, 128
        q = (lambda x, *d: x.permute(*d))((x @ wq).reshape(B, Sq, H, Dh), 0, 2, 1, 3)
        scores = torch.einsum("bhqd,bhkd->bhqk", q, k_cache) * (Dh ** -0.5)
        p = _softmax(scores, axis=-1)
        o = torch.einsum("bhqk,bhkd->bhqd", p, v_cache)
        return (lambda x, *d: x.permute(*d))(o, 0, 2, 1, 3).reshape(B, Sq, H * Dh) @ wo

    def _B_ref(x, k_cache, v_cache, wq, wo):
        B, Sq, D = x.shape
        H, Dh = 32, 128
        q = (lambda x, *d: x.permute(*d))((x @ wq).reshape(B, Sq, H, Dh), 0, 2, 1, 3)
        scores = torch.einsum("bhqd,bhkd->bhqk", q, k_cache) * (Dh ** -0.5)
        p = _softmax(scores, axis=-1)
        o = torch.einsum("bhqk,bhkd->bhqd", p, v_cache)
        return (lambda x, *d: x.permute(*d))(o, 0, 2, 1, 3).reshape(B, Sq, H * Dh) @ wo

    a = _A_ref(A_x, A_k_cache, A_v_cache, A_wq, A_wo)
    b = _B_ref(B_x, B_k_cache, B_v_cache, B_wq, B_wo)
    return torch.concatenate([a, b], axis=-1)
```

```cpp
#include <hip/hip_runtime.h>
#include <hip/hip_bf16.h>
#include <stdint.h>
#define PROBE 1
typedef short bf16x8 __attribute__((ext_vector_type(8)));
typedef short s16x4 __attribute__((ext_vector_type(4)));
typedef float f32x4 __attribute__((ext_vector_type(4)));
typedef unsigned u32x4 __attribute__((ext_vector_type(4)));
typedef unsigned u32x2 __attribute__((ext_vector_type(2)));
typedef float f32x2 __attribute__((ext_vector_type(2)));
typedef __bf16 bf16x2_t __attribute__((ext_vector_type(2)));
constexpr int NB = 8, SQ = 144, DM = 640, NH = 32, SK = 512, DH = 128;
__device__ __forceinline__ unsigned cvtpk(float lo, float hi) { f32x2 v = {lo, hi}; bf16x2_t b = __builtin_convertvector(v, bf16x2_t); return __builtin_bit_cast(unsigned, b); }
__device__ __forceinline__ unsigned off_b(unsigned row, unsigned ch) { return 256u * row + 16u * (ch ^ (((row & 3) << 2) | ((row >> 2) & 3))); }
__device__ __forceinline__ s16x4 tr_read(unsigned addr) { s16x4 r; asm volatile("ds_read_b64_tr_b16 %0, %1" : "=v"(r) : "v"(addr) : "memory"); return r; }
#define WAITL() do { asm volatile("s_waitcnt lgkmcnt(0)" ::: "memory"); __builtin_amdgcn_sched_barrier(0); } while (0)

__global__ __launch_bounds__(256) void k_qproj(const uint16_t* __restrict__ xa, const uint16_t* __restrict__ wqa, const uint16_t* __restrict__ xb, const uint16_t* __restrict__ wqb, uint16_t* __restrict__ Q) {
  __shared__ __attribute__((aligned(16))) char lds[8192];
  int h = blockIdx.x, b = blockIdx.y, br = blockIdx.z;
  const uint16_t* x = (br ? xb : xa) + (size_t)b * SQ * DM;
  const uint16_t* w = (br ? wqb : wqa) + h * DH;
  int tid = threadIdx.x, wid = tid >> 6, l = tid & 63;
  f32x4 acc[2][9];
  for (int i = 0; i < 2; ++i) for (int j = 0; j < 9; ++j) acc[i][j] = f32x4{0, 0, 0, 0};
  unsigned base = (unsigned)(size_t)lds;
  int g = l >> 4, qq = (l & 15) >> 2, p = l & 3;
  for (int k0 = 0; k0 < DM; k0 += 32) {
    __syncthreads();
    for (int i = 0; i < 2; ++i) { int n = tid + 256 * i, row = n >> 4, ch = n & 15;
      *(u32x4*)(lds + off_b(row, ch)) = *(const u32x4*)(w + (size_t)(k0 + row) * (NH * DH) + ch * 8); }
    __syncthreads();
    bf16x8 a[2];
    for (int i = 0; i < 2; ++i) { int c = 2 * wid + i;
      s16x4 lo = tr_read(base + off_b(8 * g + qq, 2 * c + (p >> 1)) + 8 * (p & 1));
      s16x4 hi = tr_read(base + off_b(8 * g + 4 + qq, 2 * c + (p >> 1)) + 8 * (p & 1));
      WAITL();
      a[i] = bf16x8{lo[0], lo[1], lo[2], lo[3], hi[0], hi[1], hi[2], hi[3]}; }
#pragma unroll
    for (int j = 0; j < 9; ++j) {
      bf16x8 bb = *(const bf16x8*)(x + (size_t)(16 * j + (l & 15)) * DM + k0 + 8 * g);
      acc[0][j] = __builtin_amdgcn_mfma_f32_16x16x32_bf16(a[0], bb, acc[0][j], 0, 0, 0);
      acc[1][j] = __builtin_amdgcn_mfma_f32_16x16x32_bf16(a[1], bb, acc[1][j], 0, 0, 0);
    }
  }
  uint16_t* q = Q + (size_t)((br * NB + b) * NH + h) * SQ * DH;
  for (int i = 0; i < 2; ++i) for (int j = 0; j < 9; ++j) {
    u32x2 v = {cvtpk(acc[i][j][0], acc[i][j][1]), cvtpk(acc[i][j][2], acc[i][j][3])};
    *(u32x2*)(q + (size_t)(16 * j + (l & 15)) * DH + (2 * wid + i) * 16 + 4 * g) = v;
  }
}

__global__ __launch_bounds__(192) void k_attn(const uint16_t* __restrict__ Q, const uint16_t* __restrict__ ka, const uint16_t* __restrict__ va, const uint16_t* __restrict__ kb, const uint16_t* __restrict__ vb, uint16_t* __restrict__ O) {
  __shared__ __attribute__((aligned(16))) char lds[16384];
  int h = blockIdx.x, b = blockIdx.y, br = blockIdx.z;
  const uint16_t* K = (br ? kb : ka) + (size_t)(b * NH + h) * SK * DH;
  const uint16_t* V = (br ? vb : va) + (size_t)(b * NH + h) * SK * DH;
  const uint16_t* q = Q + (size_t)((br * NB + b) * NH + h) * SQ * DH;
  int tid = threadIdx.x, wid = tid >> 6, l = tid & 63;
  int g = l >> 4, qq = (l & 15) >> 2, p = l & 3;
  bf16x8 qf[3][4];
  for (int j = 0; j < 3; ++j) for (int s = 0; s < 4; ++s) qf[j][s] = *(const bf16x8*)(q + (size_t)(48 * wid + 16 * j + (l & 15)) * DH + 32 * s + 8 * g);
  f32x4 o[3][8];
  for (int j = 0; j < 3; ++j) for (int d = 0; d < 8; ++d) o[j][d] = f32x4{0, 0, 0, 0};
  float m[3] = {-1e30f, -1e30f, -1e30f}, ls[3] = {0, 0, 0};
  const float C = 0.08838834764831845f * 1.4426950408889634f;
  unsigned base = (unsigned)(size_t)lds;
  for (int k0 = 0; k0 < SK; k0 += 32) {
    __syncthreads();
    for (int n = tid; n < 1024; n += 192) { int t = n >> 9, r = (n >> 4) & 31, ch = n & 15;
      *(u32x4*)(lds + t * 8192 + off_b(r, ch)) = *(const u32x4*)((t ? V : K) + (size_t)(k0 + r) * DH + ch * 8); }
    __syncthreads();
    f32x4 s[3][2];
    for (int j = 0; j < 3; ++j) for (int kb2 = 0; kb2 < 2; ++kb2) s[j][kb2] = f32x4{0, 0, 0, 0};
#pragma unroll
    for (int kb2 = 0; kb2 < 2; ++kb2)
#pragma unroll
      for (int st = 0; st < 4; ++st) {
        bf16x8 kf = *(const bf16x8*)(lds + off_b(16 * kb2 + (l & 15), 4 * st + g));
#pragma unroll
        for (int j = 0; j < 3; ++j) s[j][kb2] = __builtin_amdgcn_mfma_f32_16x16x32_bf16(kf, qf[j][st], s[j][kb2], 0, 0, 0);
      }
    bf16x8 pf[3];
#pragma unroll
    for (int j = 0; j < 3; ++j) {
      float mx = -1e30f;
      for (int kb2 = 0; kb2 < 2; ++kb2) for (int r = 0; r < 4; ++r) mx = fmaxf(mx, s[j][kb2][r]);
      mx = fmaxf(mx, __shfl_xor(mx, 16)); mx = fmaxf(mx, __shfl_xor(mx, 32));
      float mn = fmaxf(m[j], mx); float al = __builtin_amdgcn_exp2f((m[j] - mn) * C); m[j] = mn;
      float ps = 0; float pv[8];
      for (int kb2 = 0; kb2 < 2; ++kb2) for (int r = 0; r < 4; ++r) { float e = __builtin_amdgcn_exp2f((s[j][kb2][r] - mn) * C); pv[kb2 * 4 + r] = e; ps += e; }
      ps += __shfl_xor(ps, 16); ps += __shfl_xor(ps, 32);
      ls[j] = ls[j] * al + ps;
      for (int d = 0; d < 8; ++d) o[j][d] *= al;
      u32x4 w = {cvtpk(pv[0], pv[1]), cvtpk(pv[2], pv[3]), cvtpk(pv[4], pv[5]), cvtpk(pv[6], pv[7])};
      pf[j] = __builtin_bit_cast(bf16x8, w);
    }
#pragma unroll
    for (int d = 0; d < 8; ++d) {
      s16x4 lo = tr_read(base + 8192 + off_b(4 * g + qq, 2 * d + (p >> 1)) + 8 * (p & 1));
      s16x4 hi = tr_read(base + 8192 + off_b(16 + 4 * g + qq, 2 * d + (p >> 1)) + 8 * (p & 1));
      WAITL();
      bf16x8 vf = bf16x8{lo[0], lo[1], lo[2], lo[3], hi[0], hi[1], hi[2], hi[3]};
#pragma unroll
      for (int j = 0; j < 3; ++j) o[j][d] = __builtin_amdgcn_mfma_f32_16x16x32_bf16(vf, pf[j], o[j][d], 0, 0, 0);
    }
  }
  uint16_t* op = O + (size_t)(br * NB + b) * SQ * (NH * DH) + h * DH;
  for (int j = 0; j < 3; ++j) { float il = 1.f / ls[j];
    for (int d = 0; d < 8; ++d) {
      u32x2 v = {cvtpk(o[j][d][0] * il, o[j][d][1] * il), cvtpk(o[j][d][2] * il, o[j][d][3] * il)};
      *(u32x2*)(op + (size_t)(48 * wid + 16 * j + (l & 15)) * (NH * DH) + 16 * d + 4 * g) = v; } }
}

__global__ __launch_bounds__(256) void k_oproj(const uint16_t* __restrict__ O, const uint16_t* __restrict__ woa, const uint16_t* __restrict__ wob, uint16_t* __restrict__ out) {
  __shared__ __attribute__((aligned(16))) char lds[64 * 128];
  int nt = blockIdx.x, bq = blockIdx.y, br = blockIdx.z;
  int b = bq / 3, rg = bq % 3;
  const uint16_t* w = (br ? wob : woa) + nt * 64;
  const uint16_t* o = O + ((size_t)(br * NB + b) * SQ + rg * 48) * (NH * DH);
  int tid = threadIdx.x, wid = tid >> 6, l = tid & 63;
  int g = l >> 4, qq = (l & 15) >> 2, p = l & 3;
  f32x4 acc[3]; for (int j = 0; j < 3; ++j) acc[j] = f32x4{0, 0, 0, 0};
  unsigned base = (unsigned)(size_t)lds;
  for (int k0 = 0; k0 < NH * DH; k0 += 64) {
    __syncthreads();
    for (int i = 0; i < 2; ++i) { int n = tid + 256 * i, row = n >> 3, ch = n & 7;
      *(u32x4*)(lds + row * 128 + ch * 16) = *(const u32x4*)(w + (size_t)(k0 + row) * DM + ch * 8); }
    __syncthreads();
#pragma unroll
    for (int ks = 0; ks < 2; ++ks) {
      s16x4 lo = tr_read(base + (32 * ks + 8 * g + qq) * 128 + wid * 32 + p * 8);
      s16x4 hi = tr_read(base + (32 * ks + 8 * g + 4 + qq) * 128 + wid * 32 + p * 8);
      WAITL();
      bf16x8 a = bf16x8{lo[0], lo[1], lo[2], lo[3], hi[0], hi[1], hi[2], hi[3]};
#pragma unroll
      for (int j = 0; j < 3; ++j) {
        bf16x8 bb = *(const bf16x8*)(o + (size_t)(16 * j + (l & 15)) * (NH * DH) + k0 + 32 * ks + 8 * g);
        acc[j] = __builtin_amdgcn_mfma_f32_16x16x32_bf16(a, bb, acc[j], 0, 0, 0);
      }
    }
  }
  for (int j = 0; j < 3; ++j) {
    u32x2 v = {cvtpk(acc[j][0], acc[j][1]), cvtpk(acc[j][2], acc[j][3])};
    *(u32x2*)(out + ((size_t)b * SQ + rg * 48 + 16 * j + (l & 15)) * (2 * DM) + br * DM + nt * 64 + wid * 16 + 4 * g) = v;
  }
}

__global__ __launch_bounds__(512) void k_stream(const u32x4* __restrict__ ka, const u32x4* __restrict__ va, const u32x4* __restrict__ kb, const u32x4* __restrict__ vb, unsigned* sink) {
  const u32x4* ptrs[4] = {ka, va, kb, vb};
  u32x4 acc = {0,0,0,0};
  int base = blockIdx.x * 8192;
#pragma unroll
  for (int t = 0; t < 4; ++t) {
    const u32x4* p = ptrs[t] + base + threadIdx.x;
#pragma unroll
    for (int i = 0; i < 16; ++i) acc ^= p[i * 512];
  }
  unsigned r = acc[0] ^ acc[1] ^ acc[2] ^ acc[3];
  if (r == 0x12345678u) sink[threadIdx.x] = r;
}

extern "C" void kernel_launch(void* const* d_in, const int* in_sizes, int n_in, void* d_out, int out_size, void* d_ws, size_t ws_size, hipStream_t stream) {
  const uint16_t* xa = (const uint16_t*)d_in[0]; const uint16_t* ka = (const uint16_t*)d_in[1]; const uint16_t* va = (const uint16_t*)d_in[2];
  const uint16_t* wqa = (const uint16_t*)d_in[3]; const uint16_t* woa = (const uint16_t*)d_in[4];
  const uint16_t* xb = (const uint16_t*)d_in[5]; const uint16_t* kb = (const uint16_t*)d_in[6]; const uint16_t* vb = (const uint16_t*)d_in[7];
  const uint16_t* wqb = (const uint16_t*)d_in[8]; const uint16_t* wob = (const uint16_t*)d_in[9];
  uint16_t* Q = (uint16_t*)d_ws; uint16_t* O = Q + (size_t)2 * NB * NH * SQ * DH;
  unsigned* sink = (unsigned*)((char*)d_ws + (128u << 20));
  for (int i = 0; i < PROBE; ++i)
    k_stream<<<256, 512, 0, stream>>>((const u32x4*)ka, (const u32x4*)va, (const u32x4*)kb, (const u32x4*)vb, sink);
  k_qproj<<<dim3(NH, NB, 2), 256, 0, stream>>>(xa, wqa, xb, wqb, Q);
  k_attn<<<dim3(NH, NB, 2), 192, 0, stream>>>(Q, ka, va, kb, vb, O);
  k_oproj<<<dim3(10, 24, 2), 256, 0, stream>>>(O, woa, wob, (uint16_t*)d_out);
}
```

```cpp
#include <hip/hip_runtime.h>
#include <hip/hip_bf16.h>
#include <stdint.h>

extern "C" __global__ __launch_bounds__(256, 1) void fused_mha_block(const void* xa, const void* ka, const void* va, const void* wqa, const void* woa,
    const void* xb, const void* kb, const void* vb, const void* wqb, const void* wob, void* out, void* ws) {
  __shared__ __attribute__((aligned(1024))) char lds[163840];
  asm volatile(
"s_mov_b64 s[12:13], %0\n"
"Lcode0_%=:\n"
"s_getpc_b64 s[6:7]\n"
"s_mov_b32 s14, %1\n"
"v_mov_b32 v247, %2\n"
"s_load_dwordx16 s[48:63], s[12:13], 0\n"
"s_load_dwordx8 s[64:71], s[12:13], 64\n"
"v_lshrrev_b32 v231, 6, v247\n"
"v_and_b32 v247, 63, v247\n"
"s_nop 0\n"
"v_readfirstlane_b32 s15, v231\n"
"s_and_b32 s21, s14, 7\n"
"s_lshr_b32 s22, s14, 3\n"
"s_lshr_b32 s18, s21, 2\n"
"s_and_b32 s21, s21, 3\n"
"s_lshr_b32 s19, s22, 2\n"
"s_and_b32 s22, s22, 3\n"
"s_lshl_b32 s20, s21, 2\n"
"s_add_u32 s20, s20, s22\n"
"s_lshr_b32 s16, s15, 1\n"
"s_and_b32 s17, s15, 1\n"
"v_lshrrev_b32 v12, 2, v247\n"
"v_and_b32 v12, 0x7, v12\n"
"v_mul_u32_u24 v12, 0x2000, v12\n"
"v_lshrrev_b32 v21, 5, v247\n"
"v_lshlrev_b32 v21, 6, v21\n"
"v_add_u32 v12, v21, v12\n"
"v_and_b32 v21, 0x3, v247\n"
"v_lshlrev_b32 v21, 4, v21\n"
"v_add_u32 v12, v21, v12\n"
"v_add_u32 v12, 0x380, v12\n"
"v_subrev_u32 v13, 896, v12\n"
"v_xor_b32 v15, 32, v13\n"
"v_add_u32 v14, 896, v15\n"
"v_lshrrev_b32 v22, 2, v247\n"
"v_and_b32 v22, 0x7, v22\n"
"v_mul_u32_u24 v22, 0x500, v22\n"
"v_lshrrev_b32 v21, 5, v247\n"
"v_lshlrev_b32 v21, 6, v21\n"
"v_add_u32 v22, v21, v22\n"
"v_and_b32 v21, 0x3, v247\n"
"v_lshrrev_b32 v20, 2, v247\n"
"v_and_b32 v20, 0x7, v20\n"
"v_lshrrev_b32 v20, 2, v20\n"
"v_lshlrev_b32 v20, 1, v20\n"
"v_xor_b32 v21, v20, v21\n"
"v_lshlrev_b32 v21, 4, v21\n"
"v_add_u32 v22, v21, v22\n"
"v_add_u32 v23, 9216, v22\n"
"s_cmp_lg_u32 s15, 3\n"
"s_cbranch_scc1 Lnocpf_%=\n"
"s_lshr_b32 s8, s14, 3\n"
"s_and_b32 s8, s8, 7\n"
"s_lshl_b32 s8, s8, 13\n"
"v_lshlrev_b32 v230, 7, v247\n"
"v_add_u32 v230, s8, v230\n"
"s_mov_b32 s8, Lcode1_%=-Lcode0_%=-64\n"
"v_min_u32 v230, s8, v230\n"
"global_load_dword v230, v230, s[6:7]\n"
"Lnocpf_%=:\n"
"s_waitcnt lgkmcnt(0)\n"
"s_cmp_eq_u32 s18, 0\n"
"s_cselect_b64 s[48:49], s[48:49], s[58:59]\n"
"s_cselect_b64 s[50:51], s[50:51], s[60:61]\n"
"s_cselect_b64 s[52:53], s[52:53], s[62:63]\n"
"s_cselect_b64 s[54:55], s[54:55], s[64:65]\n"
"s_cselect_b64 s[56:57], s[56:57], s[66:67]\n"
"s_mul_i32 s21, s19, 184320\n"
"s_add_u32 s48, s48, s21\n"
"s_addc_u32 s49, s49, 0\n"
"s_mov_b32 s24, s48\n"
"s_and_b32 s25, s49, 0xffff\n"
"s_mov_b32 s26, 0x2d000\n"
"s_mov_b32 s27, 0x20000\n"
"s_lshl_b32 s21, s20, 9\n"
"s_add_u32 s54, s54, s21\n"
"s_addc_u32 s55, s55, 0\n"
"s_sub_u32 s54, s54, 896\n"
"s_subb_u32 s55, s55, 0\n"
"s_sub_u32 s22, 0x500380, s21\n"
"s_mov_b32 s28, s54\n"
"s_and_b32 s29, s55, 0xffff\n"
"s_mov_b32 s30, s22\n"
"s_mov_b32 s31, 0x20000\n"
"s_lshl_b32 s21, s19, 4\n"
"s_add_u32 s21, s21, s20\n"
"s_lshl_b32 s21, s21, 1\n"
"s_add_u32 s21, s21, s16\n"
"s_lshl_b32 s21, s21, 17\n"
"s_add_u32 s50, s50, s21\n"
"s_addc_u32 s51, s51, 0\n"
"s_add_u32 s52, s52, s21\n"
"s_addc_u32 s53, s53, 0\n"
"s_mov_b32 s32, s50\n"
"s_and_b32 s33, s51, 0xffff\n"
"s_mov_b32 s34, 0x20000\n"
"s_mov_b32 s35, 0x20000\n"
"s_mov_b32 s36, s52\n"
"s_and_b32 s37, s53, 0xffff\n"
"s_mov_b32 s38, 0x20000\n"
"s_mov_b32 s39, 0x20000\n"
"s_lshl_b32 s98, s17, 12\n"
"s_lshl_b32 s97, s16, 13\n"
"s_add_u32 s97, s97, s98\n"
"s_mov_b32 s21, s70\n"
"s_mov_b32 s22, s71\n"
"s_add_u32 s21, s21, 0x4000000\n"
"s_addc_u32 s22, s22, 0\n"
"s_mov_b32 s44, s21\n"
"s_and_b32 s45, s22, 0xffff\n"
"s_mov_b32 s46, 0x4000000\n"
"s_mov_b32 s47, 0x20000\n"
"s_mov_b32 s40, s70\n"
"s_and_b32 s41, s71, 0xffff\n"
"s_mov_b32 s42, 0x12fc000\n"
"s_mov_b32 s43, 0x20000\n"
"s_add_u32 s94, s70, 0x2000000\n"
"s_addc_u32 s95, s71, 0\n"
"s_load_dwordx2 s[92:93], s[94:95], 0x1000\n"
"s_mov_b32 s23, 0\n"
"s_mov_b32 s74, 0x8000\n"
"s_mov_b32 s75, 0xc000\n"
"s_mov_b32 s76, 0x10000\n"
"s_mov_b32 s99, 0x14000\n"
"s_mov_b32 s77, 0x18000\n"
"s_mov_b32 s78, 0x1c800\n"
"s_mov_b32 s96, 0x21000\n"
"s_cmp_eq_u32 s15, 2\n"
"s_cbranch_scc1 LroleX_%=\n"
"s_cmp_eq_u32 s15, 3\n"
"s_cbranch_scc1 LroleKV_%=\n"
"s_lshl_b32 s79, s23, 19\n"
"s_lshl_b32 s21, s15, 8\n"
"s_add_u32 s79, s79, s21\n"
"s_lshl_b32 s80, s15, 13\n"
"s_add_u32 s81, s74, s80\n"
"s_add_u32 m0, s81, 0x0\n"
"s_add_u32 s72, s79, 0x0\n"
"buffer_load_dwordx4 v12, s[28:31], s72 offen nt lds\n"
"buffer_load_dwordx4 v13, s[28:31], s72 offen offset:1024 nt lds\n"
"s_add_u32 m0, s81, 0x800\n"
"s_add_u32 s72, s79, 0x10000\n"
"buffer_load_dwordx4 v14, s[28:31], s72 offen nt lds\n"
"buffer_load_dwordx4 v15, s[28:31], s72 offen offset:1024 nt lds\n"
"s_add_u32 m0, s81, 0x1000\n"
"s_add_u32 s72, s79, 0x20000\n"
"buffer_load_dwordx4 v12, s[28:31], s72 offen nt lds\n"
"buffer_load_dwordx4 v13, s[28:31], s72 offen offset:1024 nt lds\n"
"s_add_u32 m0, s81, 0x1800\n"
"s_add_u32 s72, s79, 0x30000\n"
"buffer_load_dwordx4 v14, s[28:31], s72 offen nt lds\n"
"buffer_load_dwordx4 v15, s[28:31], s72 offen offset:1024 nt lds\n"
"s_add_u32 s79, s79, 0x40000\n"
"s_cmp_ge_u32 s79, 0x500000\n"
"s_cselect_b32 s72, 0x500000, 0\n"
"s_sub_u32 s79, s79, s72\n"
"v_mov_b32 v0, s16\n"
"v_mul_u32_u24 v0, 0x2000, v0\n"
"v_lshrrev_b32 v21, 4, v247\n"
"v_lshlrev_b32 v21, 11, v21\n"
"v_add_u32 v0, v21, v0\n"
"v_lshrrev_b32 v21, 2, v247\n"
"v_and_b32 v21, 0x3, v21\n"
"v_lshlrev_b32 v21, 6, v21\n"
"v_add_u32 v0, v21, v0\n"
"v_and_b32 v21, 0x3, v247\n"
"v_lshrrev_b32 v21, 1, v21\n"
"v_add_u32 v21, 0x0, v21\n"
"v_lshrrev_b32 v20, 4, v247\n"
"v_and_b32 v20, 0x1, v20\n"
"v_lshlrev_b32 v20, 1, v20\n"
"v_xor_b32 v21, v20, v21\n"
"v_lshlrev_b32 v21, 4, v21\n"
"v_add_u32 v0, v21, v0\n"
"v_and_b32 v21, 0x3, v247\n"
"v_and_b32 v21, 0x1, v21\n"
"v_lshlrev_b32 v21, 3, v21\n"
"v_add_u32 v0, v21, v0\n"
"v_mov_b32 v21, s17\n"
"v_mul_u32_u24 v21, 0x400, v21\n"
"v_add_u32 v0, v21, v0\n"
"v_mov_b32 v2, s16\n"
"v_mul_u32_u24 v2, 0x2000, v2\n"
"v_lshrrev_b32 v21, 4, v247\n"
"v_lshlrev_b32 v21, 11, v21\n"
"v_add_u32 v2, v21, v2\n"
"v_lshrrev_b32 v21, 2, v247\n"
"v_and_b32 v21, 0x3, v21\n"
"v_lshlrev_b32 v21, 6, v21\n"
"v_add_u32 v2, v21, v2\n"
"v_and_b32 v21, 0x3, v247\n"
"v_lshrrev_b32 v21, 1, v21\n"
"v_add_u32 v21, 0x0, v21\n"
"v_lshrrev_b32 v20, 4, v247\n"
"v_and_b32 v20, 0x1, v20\n"
"v_lshlrev_b32 v20, 1, v20\n"
"v_xor_b32 v21, v20, v21\n"
"v_lshlrev_b32 v21, 4, v21\n"
"v_add_u32 v2, v21, v2\n"
"v_and_b32 v21, 0x3, v247\n"
"v_and_b32 v21, 0x1, v21\n"
"v_lshlrev_b32 v21, 3, v21\n"
"v_add_u32 v2, v21, v2\n"
"v_mov_b32 v21, s17\n"
"v_xor_b32 v21, 0x1, v21\n"
"v_mul_u32_u24 v21, 0x400, v21\n"
"v_add_u32 v2, v21, v2\n"
"v_mov_b32 v4, s17\n"
"v_mul_u32_u24 v4, 0x40, v4\n"
"v_and_b32 v21, 0xf, v247\n"
"v_add_u32 v4, v21, v4\n"
"v_lshrrev_b32 v4, 3, v4\n"
"v_lshlrev_b32 v4, 10, v4\n"
"v_mov_b32 v21, s17\n"
"v_mul_u32_u24 v21, 0x40, v21\n"
"v_and_b32 v20, 0xf, v247\n"
"v_add_u32 v21, v20, v21\n"
"v_and_b32 v21, 0x7, v21\n"
"v_lshlrev_b32 v21, 6, v21\n"
"v_add_u32 v4, v21, v4\n"
"v_lshrrev_b32 v21, 4, v247\n"
"v_mov_b32 v20, s17\n"
"v_mul_u32_u24 v20, 0x40, v20\n"
"v_and_b32 v19, 0xf, v247\n"
"v_add_u32 v20, v19, v20\n"
"v_lshrrev_b32 v20, 2, v20\n"
"v_and_b32 v20, 0x1, v20\n"
"v_lshlrev_b32 v20, 1, v20\n"
"v_xor_b32 v21, v20, v21\n"
"v_lshlrev_b32 v21, 4, v21\n"
"v_add_u32 v4, v21, v4\n"
"v_and_b32 v5, 0xf, v247\n"
"v_add_u32 v5, 0x80, v5\n"
"v_lshrrev_b32 v5, 3, v5\n"
"v_lshlrev_b32 v5, 10, v5\n"
"v_and_b32 v21, 0xf, v247\n"
"v_add_u32 v21, 0x80, v21\n"
"v_and_b32 v21, 0x7, v21\n"
"v_lshlrev_b32 v21, 6, v21\n"
"v_add_u32 v5, v21, v5\n"
"v_lshrrev_b32 v21, 4, v247\n"
"v_and_b32 v20, 0xf, v247\n"
"v_add_u32 v20, 0x80, v20\n"
"v_lshrrev_b32 v20, 2, v20\n"
"v_and_b32 v20, 0x1, v20\n"
"v_lshlrev_b32 v20, 1, v20\n"
"v_xor_b32 v21, v20, v21\n"
"v_lshlrev_b32 v21, 4, v21\n"
"v_add_u32 v5, v21, v5\n"
"v_xor_b32 v1, 32, v0\n"
"v_xor_b32 v3, 32, v2\n"
"v_add_u32 v6, s74, v0\n"
"v_add_u32 v7, s74, v1\n"
"v_add_u32 v8, s74, v2\n"
"v_add_u32 v9, s74, v3\n"
"v_add_u32 v10, s77, v4\n"
"v_add_u32 v11, s77, v5\n"
"v_mov_b64 v[80:81], 0\n"
"v_mov_b64 v[82:83], 0\n"
"v_mov_b64 v[84:85], 0\n"
"v_mov_b64 v[86:87], 0\n"
"v_mov_b64 v[88:89], 0\n"
"v_mov_b64 v[90:91], 0\n"
"v_mov_b64 v[92:93], 0\n"
"v_mov_b64 v[94:95], 0\n"
"v_mov_b64 v[96:97], 0\n"
"v_mov_b64 v[98:99], 0\n"
"v_mov_b64 v[100:101], 0\n"
"v_mov_b64 v[102:103], 0\n"
"v_mov_b64 v[104:105], 0\n"
"v_mov_b64 v[106:107], 0\n"
"v_mov_b64 v[108:109], 0\n"
"v_mov_b64 v[110:111], 0\n"
"v_mov_b64 v[112:113], 0\n"
"v_mov_b64 v[114:115], 0\n"
"v_mov_b64 v[116:117], 0\n"
"v_mov_b64 v[118:119], 0\n"
"v_mov_b64 v[120:121], 0\n"
"v_mov_b64 v[122:123], 0\n"
"v_mov_b64 v[124:125], 0\n"
"v_mov_b64 v[126:127], 0\n"
"v_mov_b64 v[128:129], 0\n"
"v_mov_b64 v[130:131], 0\n"
"v_mov_b64 v[132:133], 0\n"
"v_mov_b64 v[134:135], 0\n"
"v_mov_b64 v[136:137], 0\n"
"v_mov_b64 v[138:139], 0\n"
"v_mov_b64 v[140:141], 0\n"
"v_mov_b64 v[142:143], 0\n"
"v_mov_b64 v[144:145], 0\n"
"v_mov_b64 v[146:147], 0\n"
"v_mov_b64 v[148:149], 0\n"
"v_mov_b64 v[150:151], 0\n"
"v_mov_b64 v[152:153], 0\n"
"v_mov_b64 v[154:155], 0\n"
"v_mov_b64 v[156:157], 0\n"
"v_mov_b64 v[158:159], 0\n"
"v_mov_b64 v[160:161], 0\n"
"v_mov_b64 v[162:163], 0\n"
"v_mov_b64 v[164:165], 0\n"
"v_mov_b64 v[166:167], 0\n"
"v_mov_b64 v[168:169], 0\n"
"v_mov_b64 v[170:171], 0\n"
"v_mov_b64 v[172:173], 0\n"
"v_mov_b64 v[174:175], 0\n"
"v_mov_b64 v[176:177], 0\n"
"v_mov_b64 v[178:179], 0\n"
"v_mov_b64 v[180:181], 0\n"
"v_mov_b64 v[182:183], 0\n"
"v_mov_b64 v[184:185], 0\n"
"v_mov_b64 v[186:187], 0\n"
"v_mov_b64 v[188:189], 0\n"
"v_mov_b64 v[190:191], 0\n"
"v_mov_b64 v[192:193], 0\n"
"v_mov_b64 v[194:195], 0\n"
"v_mov_b64 v[196:197], 0\n"
"v_mov_b64 v[198:199], 0\n"
"v_mov_b64 v[200:201], 0\n"
"v_mov_b64 v[202:203], 0\n"
"v_mov_b64 v[204:205], 0\n"
"v_mov_b64 v[206:207], 0\n"
"v_mov_b64 v[208:209], 0\n"
"v_mov_b64 v[210:211], 0\n"
"v_mov_b64 v[212:213], 0\n"
"v_mov_b64 v[214:215], 0\n"
"v_mov_b64 v[216:217], 0\n"
"v_mov_b64 v[218:219], 0\n"
"v_mov_b64 v[220:221], 0\n"
"v_mov_b64 v[222:223], 0\n"
"v_mov_b32 v238, s16\n"
"v_mul_u32_u24 v238, 0x2000, v238\n"
"v_add_u32 v238, 0x0, v238\n"
"v_and_b32 v21, 0x1f, v247\n"
"v_lshrrev_b32 v21, 3, v21\n"
"v_lshlrev_b32 v21, 11, v21\n"
"v_add_u32 v238, v21, v238\n"
"v_and_b32 v21, 0x1f, v247\n"
"v_and_b32 v21, 0x7, v21\n"
"v_lshlrev_b32 v21, 6, v21\n"
"v_add_u32 v238, v21, v238\n"
"v_lshrrev_b32 v21, 5, v247\n"
"v_add_u32 v21, 0x0, v21\n"
"v_and_b32 v20, 0x1f, v247\n"
"v_lshrrev_b32 v20, 2, v20\n"
"v_and_b32 v20, 0x3, v20\n"
"v_xor_b32 v21, v20, v21\n"
"v_lshlrev_b32 v21, 4, v21\n"
"v_add_u32 v238, v21, v238\n"
"v_xor_b32 v239, 32, v238\n"
"v_mov_b32 v240, s16\n"
"v_mul_u32_u24 v240, 0x2000, v240\n"
"v_add_u32 v240, 0x0, v240\n"
"v_mov_b32 v21, s17\n"
"v_mul_u32_u24 v21, 0x10, v21\n"
"v_and_b32 v20, 0xf, v247\n"
"v_add_u32 v21, v20, v21\n"
"v_lshrrev_b32 v21, 3, v21\n"
"v_lshlrev_b32 v21, 11, v21\n"
"v_add_u32 v240, v21, v240\n"
"v_mov_b32 v21, s17\n"
"v_mul_u32_u24 v21, 0x10, v21\n"
"v_and_b32 v20, 0xf, v247\n"
"v_add_u32 v21, v20, v21\n"
"v_and_b32 v21, 0x7, v21\n"
"v_lshlrev_b32 v21, 6, v21\n"
"v_add_u32 v240, v21, v240\n"
"v_lshrrev_b32 v21, 4, v247\n"
"v_mov_b32 v20, s17\n"
"v_mul_u32_u24 v20, 0x10, v20\n"
"v_and_b32 v19, 0xf, v247\n"
"v_add_u32 v20, v19, v20\n"
"v_lshrrev_b32 v20, 2, v20\n"
"v_and_b32 v20, 0x3, v20\n"
"v_xor_b32 v21, v20, v21\n"
"v_lshlrev_b32 v21, 4, v21\n"
"v_add_u32 v240, v21, v240\n"
"v_mov_b32 v241, s16\n"
"v_mul_u32_u24 v241, 0x2000, v241\n"
"v_add_u32 v241, 0x10000, v241\n"
"v_add_u32 v241, 0x0, v241\n"
"v_lshrrev_b32 v21, 5, v247\n"
"v_lshlrev_b32 v21, 2, v21\n"
"v_lshrrev_b32 v20, 2, v247\n"
"v_and_b32 v20, 0x3, v20\n"
"v_add_u32 v21, v20, v21\n"
"v_lshlrev_b32 v21, 6, v21\n"
"v_add_u32 v241, v21, v241\n"
"v_lshrrev_b32 v21, 4, v247\n"
"v_and_b32 v21, 0x1, v21\n"
"v_lshlrev_b32 v21, 1, v21\n"
"v_and_b32 v20, 0x3, v247\n"
"v_lshrrev_b32 v20, 1, v20\n"
"v_add_u32 v21, v20, v21\n"
"v_lshrrev_b32 v20, 5, v247\n"
"v_add_u32 v20, 0x0, v20\n"
"v_and_b32 v20, 0x3, v20\n"
"v_xor_b32 v21, v20, v21\n"
"v_lshlrev_b32 v21, 4, v21\n"
"v_add_u32 v241, v21, v241\n"
"v_and_b32 v21, 0x3, v247\n"
"v_and_b32 v21, 0x1, v21\n"
"v_lshlrev_b32 v21, 3, v21\n"
"v_add_u32 v241, v21, v241\n"
"v_add_u32 v241, 0xc000, v241\n"
"v_xor_b32 v242, 32, v241\n"
"v_add_u32 v242, 2048, v242\n"
"v_mov_b32 v243, s16\n"
"v_mul_u32_u24 v243, 0x2000, v243\n"
"v_add_u32 v243, 0x10000, v243\n"
"v_mov_b32 v21, s17\n"
"v_mul_u32_u24 v21, 0x2, v21\n"
"v_lshrrev_b32 v20, 4, v247\n"
"v_lshrrev_b32 v20, 1, v20\n"
"v_add_u32 v21, v20, v21\n"
"v_lshlrev_b32 v21, 11, v21\n"
"v_add_u32 v243, v21, v243\n"
"v_lshrrev_b32 v21, 4, v247\n"
"v_and_b32 v21, 0x1, v21\n"
"v_lshlrev_b32 v21, 2, v21\n"
"v_lshrrev_b32 v20, 2, v247\n"
"v_and_b32 v20, 0x3, v20\n"
"v_add_u32 v21, v20, v21\n"
"v_lshlrev_b32 v21, 6, v21\n"
"v_add_u32 v243, v21, v243\n"
"v_and_b32 v21, 0x3, v247\n"
"v_lshrrev_b32 v21, 1, v21\n"
"v_lshlrev_b32 v21, 1, v21\n"
"v_add_u32 v21, 0x0, v21\n"
"v_lshrrev_b32 v20, 4, v247\n"
"v_xor_b32 v21, v20, v21\n"
"v_lshlrev_b32 v21, 4, v21\n"
"v_add_u32 v243, v21, v243\n"
"v_and_b32 v21, 0x3, v247\n"
"v_and_b32 v21, 0x1, v21\n"
"v_lshlrev_b32 v21, 3, v21\n"
"v_add_u32 v243, v21, v243\n"
"v_add_u32 v243, 0xc000, v243\n"
"v_xor_b32 v244, 16, v243\n"
"v_lshrrev_b32 v245, 2, v247\n"
"v_and_b32 v245, 0x7, v245\n"
"v_mul_u32_u24 v245, 0x100, v245\n"
"v_lshrrev_b32 v21, 5, v247\n"
"v_lshlrev_b32 v21, 6, v21\n"
"v_add_u32 v245, v21, v245\n"
"v_and_b32 v21, 0x3, v247\n"
"v_lshrrev_b32 v20, 2, v247\n"
"v_and_b32 v20, 0x7, v20\n"
"v_lshrrev_b32 v20, 2, v20\n"
"v_xor_b32 v21, v20, v21\n"
"v_lshlrev_b32 v21, 4, v21\n"
"v_add_u32 v245, v21, v245\n"
"v_xor_b32 v246, 32, v245\n"
"s_add_u32 s81, s75, s80\n"
"s_add_u32 m0, s81, 0x0\n"
"s_add_u32 s72, s79, 0x0\n"
"buffer_load_dwordx4 v12, s[28:31], s72 offen nt lds\n"
"buffer_load_dwordx4 v13, s[28:31], s72 offen offset:1024 nt lds\n"
"s_add_u32 m0, s81, 0x800\n"
"s_add_u32 s72, s79, 0x10000\n"
"buffer_load_dwordx4 v14, s[28:31], s72 offen nt lds\n"
"buffer_load_dwordx4 v15, s[28:31], s72 offen offset:1024 nt lds\n"
"s_add_u32 m0, s81, 0x1000\n"
"s_add_u32 s72, s79, 0x20000\n"
"buffer_load_dwordx4 v12, s[28:31], s72 offen nt lds\n"
"buffer_load_dwordx4 v13, s[28:31], s72 offen offset:1024 nt lds\n"
"s_add_u32 m0, s81, 0x1800\n"
"s_add_u32 s72, s79, 0x30000\n"
"buffer_load_dwordx4 v14, s[28:31], s72 offen nt lds\n"
"buffer_load_dwordx4 v15, s[28:31], s72 offen offset:1024 nt lds\n"
"s_add_u32 s79, s79, 0x40000\n"
"s_cmp_ge_u32 s79, 0x500000\n"
"s_cselect_b32 s72, 0x500000, 0\n"
"s_sub_u32 s79, s79, s72\n"
"s_add_u32 s81, s76, s80\n"
"s_add_u32 m0, s81, 0x0\n"
"s_add_u32 s72, s79, 0x0\n"
"buffer_load_dwordx4 v12, s[28:31], s72 offen nt lds\n"
"buffer_load_dwordx4 v13, s[28:31], s72 offen offset:1024 nt lds\n"
"s_add_u32 m0, s81, 0x800\n"
"s_add_u32 s72, s79, 0x10000\n"
"buffer_load_dwordx4 v14, s[28:31], s72 offen nt lds\n"
"buffer_load_dwordx4 v15, s[28:31], s72 offen offset:1024 nt lds\n"
"s_add_u32 m0, s81, 0x1000\n"
"s_add_u32 s72, s79, 0x20000\n"
"buffer_load_dwordx4 v12, s[28:31], s72 offen nt lds\n"
"buffer_load_dwordx4 v13, s[28:31], s72 offen offset:1024 nt lds\n"
"s_add_u32 m0, s81, 0x1800\n"
"s_add_u32 s72, s79, 0x30000\n"
"buffer_load_dwordx4 v14, s[28:31], s72 offen nt lds\n"
"buffer_load_dwordx4 v15, s[28:31], s72 offen offset:1024 nt lds\n"
"s_add_u32 s79, s79, 0x40000\n"
"s_cmp_ge_u32 s79, 0x500000\n"
"s_cselect_b32 s72, 0x500000, 0\n"
"s_sub_u32 s79, s79, s72\n"
"s_add_u32 s81, s99, s80\n"
"s_add_u32 m0, s81, 0x0\n"
"s_add_u32 s72, s79, 0x0\n"
"buffer_load_dwordx4 v12, s[28:31], s72 offen nt lds\n"
"buffer_load_dwordx4 v13, s[28:31], s72 offen offset:1024 nt lds\n"
"s_add_u32 m0, s81, 0x800\n"
"s_add_u32 s72, s79, 0x10000\n"
"buffer_load_dwordx4 v14, s[28:31], s72 offen nt lds\n"
"buffer_load_dwordx4 v15, s[28:31], s72 offen offset:1024 nt lds\n"
"s_add_u32 m0, s81, 0x1000\n"
"s_add_u32 s72, s79, 0x20000\n"
"buffer_load_dwordx4 v12, s[28:31], s72 offen nt lds\n"
"buffer_load_dwordx4 v13, s[28:31], s72 offen offset:1024 nt lds\n"
"s_add_u32 m0, s81, 0x1800\n"
"s_add_u32 s72, s79, 0x30000\n"
"buffer_load_dwordx4 v14, s[28:31], s72 offen nt lds\n"
"buffer_load_dwordx4 v15, s[28:31], s72 offen offset:1024 nt lds\n"
"s_add_u32 s79, s79, 0x40000\n"
"s_cmp_ge_u32 s79, 0x500000\n"
"s_cselect_b32 s72, 0x500000, 0\n"
"s_sub_u32 s79, s79, s72\n"
"s_waitcnt vmcnt(24)\n"
"s_waitcnt lgkmcnt(0)\n"
"s_add_u32 s92, s92, 1\n"
"s_addc_u32 s93, s93, 0\n"
"s_barrier\n"
"ds_read_b128 a[192:195], v10 offset:0\n"
"ds_read_b64_tr_b16 a[160:161], v6 offset:0\n"
"ds_read_b64_tr_b16 a[162:163], v6 offset:256\n"
"ds_read_b128 a[196:199], v10 offset:2048\n"
"ds_read_b128 a[200:203], v10 offset:4096\n"
"ds_read_b128 a[204:207], v10 offset:6144\n"
"ds_read_b128 a[208:211], v11 offset:0\n"
"ds_read_b64_tr_b16 a[164:165], v7 offset:0\n"
"ds_read_b64_tr_b16 a[166:167], v7 offset:256\n"
"ds_read_b64_tr_b16 a[168:169], v6 offset:512\n"
"ds_read_b64_tr_b16 a[170:171], v6 offset:768\n"
"ds_read_b64_tr_b16 a[172:173], v7 offset:512\n"
"ds_read_b64_tr_b16 a[174:175], v7 offset:768\n"
"ds_read_b64_tr_b16 a[176:177], v8 offset:0\n"
"ds_read_b64_tr_b16 a[178:179], v8 offset:256\n"
"s_waitcnt lgkmcnt(14)\n"
"ds_read_b64_tr_b16 a[180:181], v9 offset:0\n"
"s_waitcnt lgkmcnt(14)\n"
"ds_read_b64_tr_b16 a[182:183], v9 offset:256\n"
"s_mov_b32 s73, 9\n"
"s_waitcnt lgkmcnt(11)\n"
"LQWa_%=:\n"
"v_mfma_f32_16x16x32_bf16 v[96:99], a[160:163], a[192:195], v[96:99]\n"
"ds_read_b64_tr_b16 a[184:185], v8 offset:512\n"
"v_mfma_f32_16x16x32_bf16 v[100:103], a[160:163], a[196:199], v[100:103]\n"
"ds_read_b64_tr_b16 a[186:187], v8 offset:768\n"
"v_mfma_f32_16x16x32_bf16 v[160:163], a[160:163], a[200:203], v[160:163]\n"
"ds_read_b64_tr_b16 a[188:189], v9 offset:512\n"
"v_mfma_f32_16x16x32_bf16 v[164:167], a[160:163], a[204:207], v[164:167]\n"
"ds_read_b64_tr_b16 a[190:191], v9 offset:768\n"
"s_waitcnt lgkmcnt(8)\n"
"v_mfma_f32_16x16x32_bf16 v[80:83], a[160:163], a[208:211], v[80:83]\n"
"v_mfma_f32_16x16x32_bf16 v[104:107], a[164:167], a[192:195], v[104:107]\n"
"v_mfma_f32_16x16x32_bf16 v[108:111], a[164:167], a[196:199], v[108:111]\n"
"v_mfma_f32_16x16x32_bf16 v[168:171], a[164:167], a[200:203], v[168:171]\n"
"v_mfma_f32_16x16x32_bf16 v[172:175], a[164:167], a[204:207], v[172:175]\n"
"v_mfma_f32_16x16x32_bf16 v[84:87], a[164:167], a[208:211], v[84:87]\n"
"v_mfma_f32_16x16x32_bf16 v[112:115], a[168:171], a[192:195], v[112:115]\n"
"v_mfma_f32_16x16x32_bf16 v[116:119], a[168:171], a[196:199], v[116:119]\n"
"v_mfma_f32_16x16x32_bf16 v[176:179], a[168:171], a[200:203], v[176:179]\n"
"v_mfma_f32_16x16x32_bf16 v[180:183], a[168:171], a[204:207], v[180:183]\n"
"v_mfma_f32_16x16x32_bf16 v[88:91], a[168:171], a[208:211], v[88:91]\n"
"v_mfma_f32_16x16x32_bf16 v[120:123], a[172:175], a[192:195], v[120:123]\n"
"v_mfma_f32_16x16x32_bf16 v[124:127], a[172:175], a[196:199], v[124:127]\n"
"v_mfma_f32_16x16x32_bf16 v[184:187], a[172:175], a[200:203], v[184:187]\n"
"v_mfma_f32_16x16x32_bf16 v[188:191], a[172:175], a[204:207], v[188:191]\n"
"v_mfma_f32_16x16x32_bf16 v[92:95], a[172:175], a[208:211], v[92:95]\n"
"s_waitcnt vmcnt(16) lgkmcnt(0)\n"
"s_barrier\n"
"v_add_u32 v6, s75, v0\n"
"ds_read_b128 a[212:215], v10 offset:512\n"
"s_add_u32 s81, s74, s80\n"
"s_cmp_eq_u32 s73, 1\n"
"s_cselect_b32 s22, 0x7ff00000, s79\n"
"v_mfma_f32_16x16x32_bf16 v[128:131], a[176:179], a[192:195], v[128:131]\n"
"ds_read_b64_tr_b16 a[160:161], v6 offset:0\n"
"s_add_u32 m0, s81, 0x0\n"
"s_add_u32 s72, s22, 0x0\n"
"ds_read_b64_tr_b16 a[162:163], v6 offset:256\n"
"v_mfma_f32_16x16x32_bf16 v[132:135], a[176:179], a[196:199], v[132:135]\n"
"ds_read_b128 a[216:219], v10 offset:2560\n"
"buffer_load_dwordx4 v12, s[28:31], s72 offen nt lds\n"
"v_mfma_f32_16x16x32_bf16 v[192:195], a[176:179], a[200:203], v[192:195]\n"
"ds_read_b128 a[220:223], v10 offset:4608\n"
"buffer_load_dwordx4 v13, s[28:31], s72 offen offset:1024 nt lds\n"
"v_mfma_f32_16x16x32_bf16 v[196:199], a[176:179], a[204:207], v[196:199]\n"
"ds_read_b128 a[224:227], v10 offset:6656\n"
"v_add_u32 v7, s75, v1\n"
"v_mfma_f32_16x16x32_bf16 v[136:139], a[180:183], a[192:195], v[136:139]\n"
"ds_read_b128 a[228:231], v11 offset:512\n"
"s_add_u32 m0, s81, 0x800\n"
"s_add_u32 s72, s22, 0x10000\n"
"v_mfma_f32_16x16x32_bf16 v[140:143], a[180:183], a[196:199], v[140:143]\n"
"ds_read_b64_tr_b16 a[164:165], v7 offset:0\n"
"buffer_load_dwordx4 v14, s[28:31], s72 offen nt lds\n"
"v_mfma_f32_16x16x32_bf16 v[200:203], a[180:183], a[200:203], v[200:203]\n"
"ds_read_b64_tr_b16 a[166:167], v7 offset:256\n"
"buffer_load_dwordx4 v15, s[28:31], s72 offen offset:1024 nt lds\n"
"v_mfma_f32_16x16x32_bf16 v[204:207], a[180:183], a[204:207], v[204:207]\n"
"ds_read_b64_tr_b16 a[168:169], v6 offset:512\n"
"v_add_u32 v8, s75, v2\n"
"v_mfma_f32_16x16x32_bf16 v[144:147], a[184:187], a[192:195], v[144:147]\n"
"ds_read_b64_tr_b16 a[170:171], v6 offset:768\n"
"s_add_u32 m0, s81, 0x1000\n"
"s_add_u32 s72, s22, 0x20000\n"
"v_add_u32 v9, s75, v3\n"
"v_mfma_f32_16x16x32_bf16 v[148:151], a[184:187], a[196:199], v[148:151]\n"
"ds_read_b64_tr_b16 a[172:173], v7 offset:512\n"
"buffer_load_dwordx4 v12, s[28:31], s72 offen nt lds\n"
"v_mfma_f32_16x16x32_bf16 v[208:211], a[184:187], a[200:203], v[208:211]\n"
"ds_read_b64_tr_b16 a[174:175], v7 offset:768\n"
"buffer_load_dwordx4 v13, s[28:31], s72 offen offset:1024 nt lds\n"
"v_mfma_f32_16x16x32_bf16 v[212:215], a[184:187], a[204:207], v[212:215]\n"
"ds_read_b64_tr_b16 a[176:177], v8 offset:0\n"
"v_mfma_f32_16x16x32_bf16 v[152:155], a[188:191], a[192:195], v[152:155]\n"
"s_add_u32 m0, s81, 0x1800\n"
"s_add_u32 s72, s22, 0x30000\n"
"ds_read_b64_tr_b16 a[178:179], v8 offset:256\n"
"v_mfma_f32_16x16x32_bf16 v[156:159], a[188:191], a[196:199], v[156:159]\n"
"buffer_load_dwordx4 v14, s[28:31], s72 offen nt lds\n"
"s_waitcnt lgkmcnt(9)\n"
"ds_read_b64_tr_b16 a[180:181], v9 offset:0\n"
"v_mfma_f32_16x16x32_bf16 v[216:219], a[188:191], a[200:203], v[216:219]\n"
"buffer_load_dwordx4 v15, s[28:31], s72 offen offset:1024 nt lds\n"
"ds_read_b64_tr_b16 a[182:183], v9 offset:256\n"
"s_add_u32 s79, s79, 0x40000\n"
"s_cmp_ge_u32 s79, 0x500000\n"
"s_cselect_b32 s72, 0x500000, 0\n"
"s_sub_u32 s79, s79, s72\n"
"v_mfma_f32_16x16x32_bf16 v[220:223], a[188:191], a[204:207], v[220:223]\n"
"v_mfma_f32_16x16x32_bf16 v[96:99], a[160:163], a[212:215], v[96:99]\n"
"ds_read_b64_tr_b16 a[184:185], v8 offset:512\n"
"v_mfma_f32_16x16x32_bf16 v[100:103], a[160:163], a[216:219], v[100:103]\n"
"ds_read_b64_tr_b16 a[186:187], v8 offset:768\n"
"v_mfma_f32_16x16x32_bf16 v[160:163], a[160:163], a[220:223], v[160:163]\n"
"ds_read_b64_tr_b16 a[188:189], v9 offset:512\n"
"v_mfma_f32_16x16x32_bf16 v[164:167], a[160:163], a[224:227], v[164:167]\n"
"ds_read_b64_tr_b16 a[190:191], v9 offset:768\n"
"s_waitcnt lgkmcnt(9)\n"
"v_mfma_f32_16x16x32_bf16 v[80:83], a[160:163], a[228:231], v[80:83]\n"
"v_mfma_f32_16x16x32_bf16 v[104:107], a[164:167], a[212:215], v[104:107]\n"
"v_mfma_f32_16x16x32_bf16 v[108:111], a[164:167], a[216:219], v[108:111]\n"
"v_mfma_f32_16x16x32_bf16 v[168:171], a[164:167], a[220:223], v[168:171]\n"
"v_mfma_f32_16x16x32_bf16 v[172:175], a[164:167], a[224:227], v[172:175]\n"
"v_mfma_f32_16x16x32_bf16 v[84:87], a[164:167], a[228:231], v[84:87]\n"
"v_mfma_f32_16x16x32_bf16 v[112:115], a[168:171], a[212:215], v[112:115]\n"
"v_mfma_f32_16x16x32_bf16 v[116:119], a[168:171], a[216:219], v[116:119]\n"
"v_mfma_f32_16x16x32_bf16 v[176:179], a[168:171], a[220:223], v[176:179]\n"
"v_mfma_f32_16x16x32_bf16 v[180:183], a[168:171], a[224:227], v[180:183]\n"
"v_mfma_f32_16x16x32_bf16 v[88:91], a[168:171], a[228:231], v[88:91]\n"
"s_waitcnt lgkmcnt(2)\n"
"v_mfma_f32_16x16x32_bf16 v[120:123], a[172:175], a[212:215], v[120:123]\n"
"v_mfma_f32_16x16x32_bf16 v[124:127], a[172:175], a[216:219], v[124:127]\n"
"v_mfma_f32_16x16x32_bf16 v[184:187], a[172:175], a[220:223], v[184:187]\n"
"v_mfma_f32_16x16x32_bf16 v[188:191], a[172:175], a[224:227], v[188:191]\n"
"v_mfma_f32_16x16x32_bf16 v[92:95], a[172:175], a[228:231], v[92:95]\n"
"s_waitcnt vmcnt(16) lgkmcnt(0)\n"
"s_barrier\n"
"v_add_u32 v10, s78, v4\n"
"v_add_u32 v6, s76, v0\n"
"ds_read_b128 a[192:195], v10 offset:0\n"
"s_add_u32 s81, s75, s80\n"
"s_cmp_eq_u32 s73, 1\n"
"s_cselect_b32 s22, 0x7ff00000, s79\n"
"v_mfma_f32_16x16x32_bf16 v[128:131], a[176:179], a[212:215], v[128:131]\n"
"ds_read_b64_tr_b16 a[160:161], v6 offset:0\n"
"s_add_u32 m0, s81, 0x0\n"
"s_add_u32 s72, s22, 0x0\n"
"ds_read_b64_tr_b16 a[162:163], v6 offset:256\n"
"v_mfma_f32_16x16x32_bf16 v[132:135], a[176:179], a[216:219], v[132:135]\n"
"ds_read_b128 a[196:199], v10 offset:2048\n"
"buffer_load_dwordx4 v12, s[28:31], s72 offen nt lds\n"
"v_mfma_f32_16x16x32_bf16 v[192:195], a[176:179], a[220:223], v[192:195]\n"
"ds_read_b128 a[200:203], v10 offset:4096\n"
"buffer_load_dwordx4 v13, s[28:31], s72 offen offset:1024 nt lds\n"
"v_add_u32 v11, s78, v5\n"
"v_mfma_f32_16x16x32_bf16 v[196:199], a[176:179], a[224:227], v[196:199]\n"
"ds_read_b128 a[204:207], v10 offset:6144\n"
"v_add_u32 v7, s76, v1\n"
"v_mfma_f32_16x16x32_bf16 v[136:139], a[180:183], a[212:215], v[136:139]\n"
"ds_read_b128 a[208:211], v11 offset:0\n"
"s_add_u32 m0, s81, 0x800\n"
"s_add_u32 s72, s22, 0x10000\n"
"v_mfma_f32_16x16x32_bf16 v[140:143], a[180:183], a[216:219], v[140:143]\n"
"ds_read_b64_tr_b16 a[164:165], v7 offset:0\n"
"buffer_load_dwordx4 v14, s[28:31], s72 offen nt lds\n"
"v_mfma_f32_16x16x32_bf16 v[200:203], a[180:183], a[220:223], v[200:203]\n"
"ds_read_b64_tr_b16 a[166:167], v7 offset:256\n"
"buffer_load_dwordx4 v15, s[28:31], s72 offen offset:1024 nt lds\n"
"v_mfma_f32_16x16x32_bf16 v[204:207], a[180:183], a[224:227], v[204:207]\n"
"ds_read_b64_tr_b16 a[168:169], v6 offset:512\n"
"v_add_u32 v8, s76, v2\n"
"v_mfma_f32_16x16x32_bf16 v[144:147], a[184:187], a[212:215], v[144:147]\n"
"ds_read_b64_tr_b16 a[170:171], v6 offset:768\n"
"s_add_u32 m0, s81, 0x1000\n"
"s_add_u32 s72, s22, 0x20000\n"
"v_add_u32 v9, s76, v3\n"
"v_mfma_f32_16x16x32_bf16 v[148:151], a[184:187], a[216:219], v[148:151]\n"
"ds_read_b64_tr_b16 a[172:173], v7 offset:512\n"
"buffer_load_dwordx4 v12, s[28:31], s72 offen nt lds\n"
"v_mfma_f32_16x16x32_bf16 v[208:211], a[184:187], a[220:223], v[208:211]\n"
"ds_read_b64_tr_b16 a[174:175], v7 offset:768\n"
"buffer_load_dwordx4 v13, s[28:31], s72 offen offset:1024 nt lds\n"
"v_mfma_f32_16x16x32_bf16 v[212:215], a[184:187], a[224:227], v[212:215]\n"
"ds_read_b64_tr_b16 a[176:177], v8 offset:0\n"
"v_mfma_f32_16x16x32_bf16 v[152:155], a[188:191], a[212:215], v[152:155]\n"
"s_add_u32 m0, s81, 0x1800\n"
"s_add_u32 s72, s22, 0x30000\n"
"ds_read_b64_tr_b16 a[178:179], v8 offset:256\n"
"v_mfma_f32_16x16x32_bf16 v[156:159], a[188:191], a[216:219], v[156:159]\n"
"buffer_load_dwordx4 v14, s[28:31], s72 offen nt lds\n"
"s_waitcnt lgkmcnt(9)\n"
"ds_read_b64_tr_b16 a[180:181], v9 offset:0\n"
"v_mfma_f32_16x16x32_bf16 v[216:219], a[188:191], a[220:223], v[216:219]\n"
"buffer_load_dwordx4 v15, s[28:31], s72 offen offset:1024 nt lds\n"
"ds_read_b64_tr_b16 a[182:183], v9 offset:256\n"
"s_add_u32 s79, s79, 0x40000\n"
"s_cmp_ge_u32 s79, 0x500000\n"
"s_cselect_b32 s72, 0x500000, 0\n"
"s_sub_u32 s79, s79, s72\n"
"s_mov_b32 s72, s74\n"
"s_mov_b32 s74, s76\n"
"s_mov_b32 s76, s72\n"
"v_mfma_f32_16x16x32_bf16 v[220:223], a[188:191], a[224:227], v[220:223]\n"
"s_mov_b32 s72, s75\n"
"s_mov_b32 s75, s99\n"
"s_mov_b32 s99, s72\n"
"s_mov_b32 s72, s77\n"
"s_mov_b32 s77, s78\n"
"s_mov_b32 s78, s96\n"
"s_mov_b32 s96, s72\n"
"s_sub_u32 s73, s73, 1\n"
"s_cmp_lg_u32 s73, 0\n"
"s_cbranch_scc1 LQWa_%=\n"
"v_mfma_f32_16x16x32_bf16 v[96:99], a[160:163], a[192:195], v[96:99]\n"
"ds_read_b64_tr_b16 a[184:185], v8 offset:512\n"
"v_mfma_f32_16x16x32_bf16 v[100:103], a[160:163], a[196:199], v[100:103]\n"
"ds_read_b64_tr_b16 a[186:187], v8 offset:768\n"
"v_mfma_f32_16x16x32_bf16 v[160:163], a[160:163], a[200:203], v[160:163]\n"
"ds_read_b64_tr_b16 a[188:189], v9 offset:512\n"
"v_mfma_f32_16x16x32_bf16 v[164:167], a[160:163], a[204:207], v[164:167]\n"
"ds_read_b64_tr_b16 a[190:191], v9 offset:768\n"
"s_waitcnt lgkmcnt(8)\n"
"v_mfma_f32_16x16x32_bf16 v[80:83], a[160:163], a[208:211], v[80:83]\n"
"s_add_u32 m0, s97, 0x0\n"
"s_add_u32 s72, s98, 0x0\n"
"v_mfma_f32_16x16x32_bf16 v[104:107], a[164:167], a[192:195], v[104:107]\n"
"buffer_load_dwordx4 v245, s[32:35], s72 offen nt lds\n"
"v_mfma_f32_16x16x32_bf16 v[108:111], a[164:167], a[196:199], v[108:111]\n"
"buffer_load_dwordx4 v246, s[32:35], s72 offen offset:2048 nt lds\n"
"v_mfma_f32_16x16x32_bf16 v[168:171], a[164:167], a[200:203], v[168:171]\n"
"v_mfma_f32_16x16x32_bf16 v[172:175], a[164:167], a[204:207], v[172:175]\n"
"v_mfma_f32_16x16x32_bf16 v[84:87], a[164:167], a[208:211], v[84:87]\n"
"v_mfma_f32_16x16x32_bf16 v[112:115], a[168:171], a[192:195], v[112:115]\n"
"s_add_u32 m0, s97, 0x400\n"
"s_add_u32 s72, s98, 0x80\n"
"v_mfma_f32_16x16x32_bf16 v[116:119], a[168:171], a[196:199], v[116:119]\n"
"buffer_load_dwordx4 v245, s[32:35], s72 offen nt lds\n"
"v_mfma_f32_16x16x32_bf16 v[176:179], a[168:171], a[200:203], v[176:179]\n"
"buffer_load_dwordx4 v246, s[32:35], s72 offen offset:2048 nt lds\n"
"v_mfma_f32_16x16x32_bf16 v[180:183], a[168:171], a[204:207], v[180:183]\n"
"v_mfma_f32_16x16x32_bf16 v[88:91], a[168:171], a[208:211], v[88:91]\n"
"v_mfma_f32_16x16x32_bf16 v[120:123], a[172:175], a[192:195], v[120:123]\n"
"v_mfma_f32_16x16x32_bf16 v[124:127], a[172:175], a[196:199], v[124:127]\n"
"v_mfma_f32_16x16x32_bf16 v[184:187], a[172:175], a[200:203], v[184:187]\n"
"v_mfma_f32_16x16x32_bf16 v[188:191], a[172:175], a[204:207], v[188:191]\n"
"v_mfma_f32_16x16x32_bf16 v[92:95], a[172:175], a[208:211], v[92:95]\n"
"s_waitcnt vmcnt(4) lgkmcnt(0)\n"
"s_barrier\n"
"v_add_u32 v6, s75, v0\n"
"ds_read_b128 a[212:215], v10 offset:512\n"
"v_mfma_f32_16x16x32_bf16 v[128:131], a[176:179], a[192:195], v[128:131]\n"
"ds_read_b64_tr_b16 a[160:161], v6 offset:0\n"
"ds_read_b64_tr_b16 a[162:163], v6 offset:256\n"
"v_mfma_f32_16x16x32_bf16 v[132:135], a[176:179], a[196:199], v[132:135]\n"
"ds_read_b128 a[216:219], v10 offset:2560\n"
"s_add_u32 m0, s97, 0x4000\n"
"s_add_u32 s72, s98, 0x2000\n"
"v_mfma_f32_16x16x32_bf16 v[192:195], a[176:179], a[200:203], v[192:195]\n"
"ds_read_b128 a[220:223], v10 offset:4608\n"
"buffer_load_dwordx4 v245, s[32:35], s72 offen nt lds\n"
"v_mfma_f32_16x16x32_bf16 v[196:199], a[176:179], a[204:207], v[196:199]\n"
"ds_read_b128 a[224:227], v10 offset:6656\n"
"buffer_load_dwordx4 v246, s[32:35], s72 offen offset:2048 nt lds\n"
"v_add_u32 v7, s75, v1\n"
"v_mfma_f32_16x16x32_bf16 v[136:139], a[180:183], a[192:195], v[136:139]\n"
"ds_read_b128 a[228:231], v11 offset:512\n"
"v_mfma_f32_16x16x32_bf16 v[140:143], a[180:183], a[196:199], v[140:143]\n"
"ds_read_b64_tr_b16 a[164:165], v7 offset:0\n"
"v_mfma_f32_16x16x32_bf16 v[200:203], a[180:183], a[200:203], v[200:203]\n"
"ds_read_b64_tr_b16 a[166:167], v7 offset:256\n"
"v_mfma_f32_16x16x32_bf16 v[204:207], a[180:183], a[204:207], v[204:207]\n"
"ds_read_b64_tr_b16 a[168:169], v6 offset:512\n"
"v_add_u32 v8, s75, v2\n"
"v_mfma_f32_16x16x32_bf16 v[144:147], a[184:187], a[192:195], v[144:147]\n"
"ds_read_b64_tr_b16 a[170:171], v6 offset:768\n"
"s_add_u32 m0, s97, 0x4400\n"
"s_add_u32 s72, s98, 0x2080\n"
"v_add_u32 v9, s75, v3\n"
"v_mfma_f32_16x16x32_bf16 v[148:151], a[184:187], a[196:199], v[148:151]\n"
"ds_read_b64_tr_b16 a[172:173], v7 offset:512\n"
"buffer_load_dwordx4 v245, s[32:35], s72 offen nt lds\n"
"v_mfma_f32_16x16x32_bf16 v[208:211], a[184:187], a[200:203], v[208:211]\n"
"ds_read_b64_tr_b16 a[174:175], v7 offset:768\n"
"buffer_load_dwordx4 v246, s[32:35], s72 offen offset:2048 nt lds\n"
"v_mfma_f32_16x16x32_bf16 v[212:215], a[184:187], a[204:207], v[212:215]\n"
"ds_read_b64_tr_b16 a[176:177], v8 offset:0\n"
"v_mfma_f32_16x16x32_bf16 v[152:155], a[188:191], a[192:195], v[152:155]\n"
"ds_read_b64_tr_b16 a[178:179], v8 offset:256\n"
"v_mfma_f32_16x16x32_bf16 v[156:159], a[188:191], a[196:199], v[156:159]\n"
"s_waitcnt lgkmcnt(10)\n"
"ds_read_b64_tr_b16 a[180:181], v9 offset:0\n"
"v_mfma_f32_16x16x32_bf16 v[216:219], a[188:191], a[200:203], v[216:219]\n"
"ds_read_b64_tr_b16 a[182:183], v9 offset:256\n"
"v_mfma_f32_16x16x32_bf16 v[220:223], a[188:191], a[204:207], v[220:223]\n"
"v_mfma_f32_16x16x32_bf16 v[96:99], a[160:163], a[212:215], v[96:99]\n"
"ds_read_b64_tr_b16 a[184:185], v8 offset:512\n"
"v_mfma_f32_16x16x32_bf16 v[100:103], a[160:163], a[216:219], v[100:103]\n"
"ds_read_b64_tr_b16 a[186:187], v8 offset:768\n"
"v_mfma_f32_16x16x32_bf16 v[160:163], a[160:163], a[220:223], v[160:163]\n"
"ds_read_b64_tr_b16 a[188:189], v9 offset:512\n"
"s_waitcnt lgkmcnt(10)\n"
"v_mfma_f32_16x16x32_bf16 v[164:167], a[160:163], a[224:227], v[164:167]\n"
"ds_read_b64_tr_b16 a[190:191], v9 offset:768\n"
"v_mfma_f32_16x16x32_bf16 v[80:83], a[160:163], a[228:231], v[80:83]\n"
"v_mfma_f32_16x16x32_bf16 v[104:107], a[164:167], a[212:215], v[104:107]\n"
"v_mfma_f32_16x16x32_bf16 v[108:111], a[164:167], a[216:219], v[108:111]\n"
"v_mfma_f32_16x16x32_bf16 v[168:171], a[164:167], a[220:223], v[168:171]\n"
"v_mfma_f32_16x16x32_bf16 v[172:175], a[164:167], a[224:227], v[172:175]\n"
"v_mfma_f32_16x16x32_bf16 v[84:87], a[164:167], a[228:231], v[84:87]\n"
"s_waitcnt lgkmcnt(5)\n"
"v_mfma_f32_16x16x32_bf16 v[112:115], a[168:171], a[212:215], v[112:115]\n"
"v_mfma_f32_16x16x32_bf16 v[116:119], a[168:171], a[216:219], v[116:119]\n"
"v_mfma_f32_16x16x32_bf16 v[176:179], a[168:171], a[220:223], v[176:179]\n"
"v_mfma_f32_16x16x32_bf16 v[180:183], a[168:171], a[224:227], v[180:183]\n"
"v_mfma_f32_16x16x32_bf16 v[88:91], a[168:171], a[228:231], v[88:91]\n"
"v_mfma_f32_16x16x32_bf16 v[120:123], a[172:175], a[212:215], v[120:123]\n"
"v_mfma_f32_16x16x32_bf16 v[124:127], a[172:175], a[216:219], v[124:127]\n"
"v_mfma_f32_16x16x32_bf16 v[184:187], a[172:175], a[220:223], v[184:187]\n"
"v_mfma_f32_16x16x32_bf16 v[188:191], a[172:175], a[224:227], v[188:191]\n"
"v_mfma_f32_16x16x32_bf16 v[92:95], a[172:175], a[228:231], v[92:95]\n"
"s_waitcnt lgkmcnt(0)\n"
"s_barrier\n"
"v_mfma_f32_16x16x32_bf16 v[128:131], a[176:179], a[212:215], v[128:131]\n"
"v_mfma_f32_16x16x32_bf16 v[132:135], a[176:179], a[216:219], v[132:135]\n"
"v_mfma_f32_16x16x32_bf16 v[192:195], a[176:179], a[220:223], v[192:195]\n"
"v_mfma_f32_16x16x32_bf16 v[196:199], a[176:179], a[224:227], v[196:199]\n"
"v_mfma_f32_16x16x32_bf16 v[136:139], a[180:183], a[212:215], v[136:139]\n"
"v_mfma_f32_16x16x32_bf16 v[140:143], a[180:183], a[216:219], v[140:143]\n"
"v_mfma_f32_16x16x32_bf16 v[200:203], a[180:183], a[220:223], v[200:203]\n"
"v_mfma_f32_16x16x32_bf16 v[204:207], a[180:183], a[224:227], v[204:207]\n"
"v_mfma_f32_16x16x32_bf16 v[144:147], a[184:187], a[212:215], v[144:147]\n"
"v_mfma_f32_16x16x32_bf16 v[148:151], a[184:187], a[216:219], v[148:151]\n"
"s_mov_b32 s72, s74\n"
"s_mov_b32 s74, s76\n"
"v_mfma_f32_16x16x32_bf16 v[208:211], a[184:187], a[220:223], v[208:211]\n"
"s_mov_b32 s76, s72\n"
"s_mov_b32 s72, s75\n"
"v_mfma_f32_16x16x32_bf16 v[212:215], a[184:187], a[224:227], v[212:215]\n"
"s_mov_b32 s75, s99\n"
"s_mov_b32 s99, s72\n"
"v_mfma_f32_16x16x32_bf16 v[152:155], a[188:191], a[212:215], v[152:155]\n"
"s_mov_b32 s72, s77\n"
"s_mov_b32 s77, s78\n"
"s_mov_b32 s78, s96\n"
"v_mfma_f32_16x16x32_bf16 v[156:159], a[188:191], a[216:219], v[156:159]\n"
"s_mov_b32 s96, s72\n"
"v_mfma_f32_16x16x32_bf16 v[216:219], a[188:191], a[220:223], v[216:219]\n"
"v_mfma_f32_16x16x32_bf16 v[220:223], a[188:191], a[224:227], v[220:223]\n"
"s_branch Lqdone_%=\n"
"LroleX_%=:\n"
"s_lshl_b32 s79, s23, 7\n"
"s_add_u32 m0, s77, 0x0\n"
"s_add_u32 s72, s79, 0x0\n"
"buffer_load_dwordx4 v22, s[24:27], s72 offen lds\n"
"buffer_load_dwordx4 v23, s[24:27], s72 offen offset:1024 lds\n"
"s_add_u32 m0, s77, 0x800\n"
"s_add_u32 s72, s79, 0x5000\n"
"buffer_load_dwordx4 v22, s[24:27], s72 offen lds\n"
"buffer_load_dwordx4 v23, s[24:27], s72 offen offset:1024 lds\n"
"s_add_u32 m0, s77, 0x1000\n"
"s_add_u32 s72, s79, 0xa000\n"
"buffer_load_dwordx4 v22, s[24:27], s72 offen lds\n"
"buffer_load_dwordx4 v23, s[24:27], s72 offen offset:1024 lds\n"
"s_add_u32 m0, s77, 0x1800\n"
"s_add_u32 s72, s79, 0xf000\n"
"buffer_load_dwordx4 v22, s[24:27], s72 offen lds\n"
"buffer_load_dwordx4 v23, s[24:27], s72 offen offset:1024 lds\n"
"s_add_u32 m0, s77, 0x2000\n"
"s_add_u32 s72, s79, 0x14000\n"
"buffer_load_dwordx4 v22, s[24:27], s72 offen lds\n"
"buffer_load_dwordx4 v23, s[24:27], s72 offen offset:1024 lds\n"
"s_add_u32 m0, s77, 0x2800\n"
"s_add_u32 s72, s79, 0x19000\n"
"buffer_load_dwordx4 v22, s[24:27], s72 offen lds\n"
"buffer_load_dwordx4 v23, s[24:27], s72 offen offset:1024 lds\n"
"s_add_u32 m0, s77, 0x3000\n"
"s_add_u32 s72, s79, 0x1e000\n"
"buffer_load_dwordx4 v22, s[24:27], s72 offen lds\n"
"buffer_load_dwordx4 v23, s[24:27], s72 offen offset:1024 lds\n"
"s_add_u32 m0, s77, 0x3800\n"
"s_add_u32 s72, s79, 0x23000\n"
"buffer_load_dwordx4 v22, s[24:27], s72 offen lds\n"
"buffer_load_dwordx4 v23, s[24:27], s72 offen offset:1024 lds\n"
"s_add_u32 m0, s77, 0x4000\n"
"s_add_u32 s72, s79, 0x28000\n"
"buffer_load_dwordx4 v22, s[24:27], s72 offen lds\n"
"buffer_load_dwordx4 v23, s[24:27], s72 offen offset:1024 lds\n"
"s_add_u32 s79, s79, 0x80\n"
"s_cmp_ge_u32 s79, 0x500\n"
"s_cselect_b32 s72, 0x500, 0\n"
"s_sub_u32 s79, s79, s72\n"
"v_mov_b32 v0, s16\n"
"v_mul_u32_u24 v0, 0x2000, v0\n"
"v_lshrrev_b32 v21, 4, v247\n"
"v_lshlrev_b32 v21, 11, v21\n"
"v_add_u32 v0, v21, v0\n"
"v_lshrrev_b32 v21, 2, v247\n"
"v_and_b32 v21, 0x3, v21\n"
"v_lshlrev_b32 v21, 6, v21\n"
"v_add_u32 v0, v21, v0\n"
"v_and_b32 v21, 0x3, v247\n"
"v_lshrrev_b32 v21, 1, v21\n"
"v_add_u32 v21, 0x0, v21\n"
"v_lshrrev_b32 v20, 4, v247\n"
"v_and_b32 v20, 0x1, v20\n"
"v_lshlrev_b32 v20, 1, v20\n"
"v_xor_b32 v21, v20, v21\n"
"v_lshlrev_b32 v21, 4, v21\n"
"v_add_u32 v0, v21, v0\n"
"v_and_b32 v21, 0x3, v247\n"
"v_and_b32 v21, 0x1, v21\n"
"v_lshlrev_b32 v21, 3, v21\n"
"v_add_u32 v0, v21, v0\n"
"v_mov_b32 v21, s17\n"
"v_mul_u32_u24 v21, 0x400, v21\n"
"v_add_u32 v0, v21, v0\n"
"v_mov_b32 v2, s16\n"
"v_mul_u32_u24 v2, 0x2000, v2\n"
"v_lshrrev_b32 v21, 4, v247\n"
"v_lshlrev_b32 v21, 11, v21\n"
"v_add_u32 v2, v21, v2\n"
"v_lshrrev_b32 v21, 2, v247\n"
"v_and_b32 v21, 0x3, v21\n"
"v_lshlrev_b32 v21, 6, v21\n"
"v_add_u32 v2, v21, v2\n"
"v_and_b32 v21, 0x3, v247\n"
"v_lshrrev_b32 v21, 1, v21\n"
"v_add_u32 v21, 0x0, v21\n"
"v_lshrrev_b32 v20, 4, v247\n"
"v_and_b32 v20, 0x1, v20\n"
"v_lshlrev_b32 v20, 1, v20\n"
"v_xor_b32 v21, v20, v21\n"
"v_lshlrev_b32 v21, 4, v21\n"
"v_add_u32 v2, v21, v2\n"
"v_and_b32 v21, 0x3, v247\n"
"v_and_b32 v21, 0x1, v21\n"
"v_lshlrev_b32 v21, 3, v21\n"
"v_add_u32 v2, v21, v2\n"
"v_mov_b32 v21, s17\n"
"v_xor_b32 v21, 0x1, v21\n"
"v_mul_u32_u24 v21, 0x400, v21\n"
"v_add_u32 v2, v21, v2\n"
"v_mov_b32 v4, s17\n"
"v_mul_u32_u24 v4, 0x40, v4\n"
"v_and_b32 v21, 0xf, v247\n"
"v_add_u32 v4, v21, v4\n"
"v_lshrrev_b32 v4, 3, v4\n"
"v_lshlrev_b32 v4, 10, v4\n"
"v_mov_b32 v21, s17\n"
"v_mul_u32_u24 v21, 0x40, v21\n"
"v_and_b32 v20, 0xf, v247\n"
"v_add_u32 v21, v20, v21\n"
"v_and_b32 v21, 0x7, v21\n"
"v_lshlrev_b32 v21, 6, v21\n"
"v_add_u32 v4, v21, v4\n"
"v_lshrrev_b32 v21, 4, v247\n"
"v_mov_b32 v20, s17\n"
"v_mul_u32_u24 v20, 0x40, v20\n"
"v_and_b32 v19, 0xf, v247\n"
"v_add_u32 v20, v19, v20\n"
"v_lshrrev_b32 v20, 2, v20\n"
"v_and_b32 v20, 0x1, v20\n"
"v_lshlrev_b32 v20, 1, v20\n"
"v_xor_b32 v21, v20, v21\n"
"v_lshlrev_b32 v21, 4, v21\n"
"v_add_u32 v4, v21, v4\n"
"v_and_b32 v5, 0xf, v247\n"
"v_add_u32 v5, 0x80, v5\n"
"v_lshrrev_b32 v5, 3, v5\n"
"v_lshlrev_b32 v5, 10, v5\n"
"v_and_b32 v21, 0xf, v247\n"
"v_add_u32 v21, 0x80, v21\n"
"v_and_b32 v21, 0x7, v21\n"
"v_lshlrev_b32 v21, 6, v21\n"
"v_add_u32 v5, v21, v5\n"
"v_lshrrev_b32 v21, 4, v247\n"
"v_and_b32 v20, 0xf, v247\n"
"v_add_u32 v20, 0x80, v20\n"
"v_lshrrev_b32 v20, 2, v20\n"
"v_and_b32 v20, 0x1, v20\n"
"v_lshlrev_b32 v20, 1, v20\n"
"v_xor_b32 v21, v20, v21\n"
"v_lshlrev_b32 v21, 4, v21\n"
"v_add_u32 v5, v21, v5\n"
"v_xor_b32 v1, 32, v0\n"
"v_xor_b32 v3, 32, v2\n"
"v_add_u32 v6, s74, v0\n"
"v_add_u32 v7, s74, v1\n"
"v_add_u32 v8, s74, v2\n"
"v_add_u32 v9, s74, v3\n"
"v_add_u32 v10, s77, v4\n"
"v_add_u32 v11, s77, v5\n"
"v_mov_b64 v[80:81], 0\n"
"v_mov_b64 v[82:83], 0\n"
"v_mov_b64 v[84:85], 0\n"
"v_mov_b64 v[86:87], 0\n"
"v_mov_b64 v[88:89], 0\n"
"v_mov_b64 v[90:91], 0\n"
"v_mov_b64 v[92:93], 0\n"
"v_mov_b64 v[94:95], 0\n"
"v_mov_b64 v[96:97], 0\n"
"v_mov_b64 v[98:99], 0\n"
"v_mov_b64 v[100:101], 0\n"
"v_mov_b64 v[102:103], 0\n"
"v_mov_b64 v[104:105], 0\n"
"v_mov_b64 v[106:107], 0\n"
"v_mov_b64 v[108:109], 0\n"
"v_mov_b64 v[110:111], 0\n"
"v_mov_b64 v[112:113], 0\n"
"v_mov_b64 v[114:115], 0\n"
"v_mov_b64 v[116:117], 0\n"
"v_mov_b64 v[118:119], 0\n"
"v_mov_b64 v[120:121], 0\n"
"v_mov_b64 v[122:123], 0\n"
"v_mov_b64 v[124:125], 0\n"
"v_mov_b64 v[126:127], 0\n"
"v_mov_b64 v[128:129], 0\n"
"v_mov_b64 v[130:131], 0\n"
"v_mov_b64 v[132:133], 0\n"
"v_mov_b64 v[134:135], 0\n"
"v_mov_b64 v[136:137], 0\n"
"v_mov_b64 v[138:139], 0\n"
"v_mov_b64 v[140:141], 0\n"
"v_mov_b64 v[142:143], 0\n"
"v_mov_b64 v[144:145], 0\n"
"v_mov_b64 v[146:147], 0\n"
"v_mov_b64 v[148:149], 0\n"
"v_mov_b64 v[150:151], 0\n"
"v_mov_b64 v[152:153], 0\n"
"v_mov_b64 v[154:155], 0\n"
"v_mov_b64 v[156:157], 0\n"
"v_mov_b64 v[158:159], 0\n"
"v_mov_b64 v[160:161], 0\n"
"v_mov_b64 v[162:163], 0\n"
"v_mov_b64 v[164:165], 0\n"
"v_mov_b64 v[166:167], 0\n"
"v_mov_b64 v[168:169], 0\n"
"v_mov_b64 v[170:171], 0\n"
"v_mov_b64 v[172:173], 0\n"
"v_mov_b64 v[174:175], 0\n"
"v_mov_b64 v[176:177], 0\n"
"v_mov_b64 v[178:179], 0\n"
"v_mov_b64 v[180:181], 0\n"
"v_mov_b64 v[182:183], 0\n"
"v_mov_b64 v[184:185], 0\n"
"v_mov_b64 v[186:187], 0\n"
"v_mov_b64 v[188:189], 0\n"
"v_mov_b64 v[190:191], 0\n"
"v_mov_b64 v[192:193], 0\n"
"v_mov_b64 v[194:195], 0\n"
"v_mov_b64 v[196:197], 0\n"
"v_mov_b64 v[198:199], 0\n"
"v_mov_b64 v[200:201], 0\n"
"v_mov_b64 v[202:203], 0\n"
"v_mov_b64 v[204:205], 0\n"
"v_mov_b64 v[206:207], 0\n"
"v_mov_b64 v[208:209], 0\n"
"v_mov_b64 v[210:211], 0\n"
"v_mov_b64 v[212:213], 0\n"
"v_mov_b64 v[214:215], 0\n"
"v_mov_b64 v[216:217], 0\n"
"v_mov_b64 v[218:219], 0\n"
"v_mov_b64 v[220:221], 0\n"
"v_mov_b64 v[222:223], 0\n"
"v_mov_b32 v238, s16\n"
"v_mul_u32_u24 v238, 0x2000, v238\n"
"v_add_u32 v238, 0x0, v238\n"
"v_and_b32 v21, 0x1f, v247\n"
"v_lshrrev_b32 v21, 3, v21\n"
"v_lshlrev_b32 v21, 11, v21\n"
"v_add_u32 v238, v21, v238\n"
"v_and_b32 v21, 0x1f, v247\n"
"v_and_b32 v21, 0x7, v21\n"
"v_lshlrev_b32 v21, 6, v21\n"
"v_add_u32 v238, v21, v238\n"
"v_lshrrev_b32 v21, 5, v247\n"
"v_add_u32 v21, 0x0, v21\n"
"v_and_b32 v20, 0x1f, v247\n"
"v_lshrrev_b32 v20, 2, v20\n"
"v_and_b32 v20, 0x3, v20\n"
"v_xor_b32 v21, v20, v21\n"
"v_lshlrev_b32 v21, 4, v21\n"
"v_add_u32 v238, v21, v238\n"
"v_xor_b32 v239, 32, v238\n"
"v_mov_b32 v240, s16\n"
"v_mul_u32_u24 v240, 0x2000, v240\n"
"v_add_u32 v240, 0x0, v240\n"
"v_mov_b32 v21, s17\n"
"v_mul_u32_u24 v21, 0x10, v21\n"
"v_and_b32 v20, 0xf, v247\n"
"v_add_u32 v21, v20, v21\n"
"v_lshrrev_b32 v21, 3, v21\n"
"v_lshlrev_b32 v21, 11, v21\n"
"v_add_u32 v240, v21, v240\n"
"v_mov_b32 v21, s17\n"
"v_mul_u32_u24 v21, 0x10, v21\n"
"v_and_b32 v20, 0xf, v247\n"
"v_add_u32 v21, v20, v21\n"
"v_and_b32 v21, 0x7, v21\n"
"v_lshlrev_b32 v21, 6, v21\n"
"v_add_u32 v240, v21, v240\n"
"v_lshrrev_b32 v21, 4, v247\n"
"v_mov_b32 v20, s17\n"
"v_mul_u32_u24 v20, 0x10, v20\n"
"v_and_b32 v19, 0xf, v247\n"
"v_add_u32 v20, v19, v20\n"
"v_lshrrev_b32 v20, 2, v20\n"
"v_and_b32 v20, 0x3, v20\n"
"v_xor_b32 v21, v20, v21\n"
"v_lshlrev_b32 v21, 4, v21\n"
"v_add_u32 v240, v21, v240\n"
"v_mov_b32 v241, s16\n"
"v_mul_u32_u24 v241, 0x2000, v241\n"
"v_add_u32 v241, 0x10000, v241\n"
"v_add_u32 v241, 0x0, v241\n"
"v_lshrrev_b32 v21, 5, v247\n"
"v_lshlrev_b32 v21, 2, v21\n"
"v_lshrrev_b32 v20, 2, v247\n"
"v_and_b32 v20, 0x3, v20\n"
"v_add_u32 v21, v20, v21\n"
"v_lshlrev_b32 v21, 6, v21\n"
"v_add_u32 v241, v21, v241\n"
"v_lshrrev_b32 v21, 4, v247\n"
"v_and_b32 v21, 0x1, v21\n"
"v_lshlrev_b32 v21, 1, v21\n"
"v_and_b32 v20, 0x3, v247\n"
"v_lshrrev_b32 v20, 1, v20\n"
"v_add_u32 v21, v20, v21\n"
"v_lshrrev_b32 v20, 5, v247\n"
"v_add_u32 v20, 0x0, v20\n"
"v_and_b32 v20, 0x3, v20\n"
"v_xor_b32 v21, v20, v21\n"
"v_lshlrev_b32 v21, 4, v21\n"
"v_add_u32 v241, v21, v241\n"
"v_and_b32 v21, 0x3, v247\n"
"v_and_b32 v21, 0x1, v21\n"
"v_lshlrev_b32 v21, 3, v21\n"
"v_add_u32 v241, v21, v241\n"
"v_add_u32 v241, 0xc000, v241\n"
"v_xor_b32 v242, 32, v241\n"
"v_add_u32 v242, 2048, v242\n"
"v_mov_b32 v243, s16\n"
"v_mul_u32_u24 v243, 0x2000, v243\n"
"v_add_u32 v243, 0x10000, v243\n"
"v_mov_b32 v21, s17\n"
"v_mul_u32_u24 v21, 0x2, v21\n"
"v_lshrrev_b32 v20, 4, v247\n"
"v_lshrrev_b32 v20, 1, v20\n"
"v_add_u32 v21, v20, v21\n"
"v_lshlrev_b32 v21, 11, v21\n"
"v_add_u32 v243, v21, v243\n"
"v_lshrrev_b32 v21, 4, v247\n"
"v_and_b32 v21, 0x1, v21\n"
"v_lshlrev_b32 v21, 2, v21\n"
"v_lshrrev_b32 v20, 2, v247\n"
"v_and_b32 v20, 0x3, v20\n"
"v_add_u32 v21, v20, v21\n"
"v_lshlrev_b32 v21, 6, v21\n"
"v_add_u32 v243, v21, v243\n"
"v_and_b32 v21, 0x3, v247\n"
"v_lshrrev_b32 v21, 1, v21\n"
"v_lshlrev_b32 v21, 1, v21\n"
"v_add_u32 v21, 0x0, v21\n"
"v_lshrrev_b32 v20, 4, v247\n"
"v_xor_b32 v21, v20, v21\n"
"v_lshlrev_b32 v21, 4, v21\n"
"v_add_u32 v243, v21, v243\n"
"v_and_b32 v21, 0x3, v247\n"
"v_and_b32 v21, 0x1, v21\n"
"v_lshlrev_b32 v21, 3, v21\n"
"v_add_u32 v243, v21, v243\n"
"v_add_u32 v243, 0xc000, v243\n"
"v_xor_b32 v244, 16, v243\n"
"v_lshrrev_b32 v245, 2, v247\n"
"v_and_b32 v245, 0x7, v245\n"
"v_mul_u32_u24 v245, 0x100, v245\n"
"v_lshrrev_b32 v21, 5, v247\n"
"v_lshlrev_b32 v21, 6, v21\n"
"v_add_u32 v245, v21, v245\n"
"v_and_b32 v21, 0x3, v247\n"
"v_lshrrev_b32 v20, 2, v247\n"
"v_and_b32 v20, 0x7, v20\n"
"v_lshrrev_b32 v20, 2, v20\n"
"v_xor_b32 v21, v20, v21\n"
"v_lshlrev_b32 v21, 4, v21\n"
"v_add_u32 v245, v21, v245\n"
"v_xor_b32 v246, 32, v245\n"
"s_waitcnt vmcnt(0)\n"
"s_waitcnt lgkmcnt(0)\n"
"s_add_u32 s92, s92, 1\n"
"s_addc_u32 s93, s93, 0\n"
"s_barrier\n"
"ds_read_b128 a[192:195], v10 offset:0\n"
"ds_read_b64_tr_b16 a[160:161], v6 offset:0\n"
"ds_read_b64_tr_b16 a[162:163], v6 offset:256\n"
"ds_read_b128 a[196:199], v10 offset:2048\n"
"ds_read_b128 a[200:203], v10 offset:4096\n"
"ds_read_b128 a[204:207], v10 offset:6144\n"
"ds_read_b128 a[208:211], v11 offset:0\n"
"ds_read_b64_tr_b16 a[164:165], v7 offset:0\n"
"ds_read_b64_tr_b16 a[166:167], v7 offset:256\n"
"ds_read_b64_tr_b16 a[168:169], v6 offset:512\n"
"ds_read_b64_tr_b16 a[170:171], v6 offset:768\n"
"ds_read_b64_tr_b16 a[172:173], v7 offset:512\n"
"ds_read_b64_tr_b16 a[174:175], v7 offset:768\n"
"ds_read_b64_tr_b16 a[176:177], v8 offset:0\n"
"ds_read_b64_tr_b16 a[178:179], v8 offset:256\n"
"s_waitcnt lgkmcnt(14)\n"
"ds_read_b64_tr_b16 a[180:181], v9 offset:0\n"
"s_waitcnt lgkmcnt(14)\n"
"ds_read_b64_tr_b16 a[182:183], v9 offset:256\n"
"s_add_u32 m0, s78, 0x0\n"
"s_add_u32 s72, s79, 0x0\n"
"buffer_load_dwordx4 v22, s[24:27], s72 offen lds\n"
"buffer_load_dwordx4 v23, s[24:27], s72 offen offset:1024 lds\n"
"s_add_u32 m0, s78, 0x800\n"
"s_add_u32 s72, s79, 0x5000\n"
"buffer_load_dwordx4 v22, s[24:27], s72 offen lds\n"
"buffer_load_dwordx4 v23, s[24:27], s72 offen offset:1024 lds\n"
"s_add_u32 m0, s78, 0x1000\n"
"s_add_u32 s72, s79, 0xa000\n"
"buffer_load_dwordx4 v22, s[24:27], s72 offen lds\n"
"buffer_load_dwordx4 v23, s[24:27], s72 offen offset:1024 lds\n"
"s_add_u32 m0, s78, 0x1800\n"
"s_add_u32 s72, s79, 0xf000\n"
"buffer_load_dwordx4 v22, s[24:27], s72 offen lds\n"
"buffer_load_dwordx4 v23, s[24:27], s72 offen offset:1024 lds\n"
"s_add_u32 m0, s78, 0x2000\n"
"s_add_u32 s72, s79, 0x14000\n"
"buffer_load_dwordx4 v22, s[24:27], s72 offen lds\n"
"buffer_load_dwordx4 v23, s[24:27], s72 offen offset:1024 lds\n"
"s_add_u32 m0, s78, 0x2800\n"
"s_add_u32 s72, s79, 0x19000\n"
"buffer_load_dwordx4 v22, s[24:27], s72 offen lds\n"
"buffer_load_dwordx4 v23, s[24:27], s72 offen offset:1024 lds\n"
"s_add_u32 m0, s78, 0x3000\n"
"s_add_u32 s72, s79, 0x1e000\n"
"buffer_load_dwordx4 v22, s[24:27], s72 offen lds\n"
"buffer_load_dwordx4 v23, s[24:27], s72 offen offset:1024 lds\n"
"s_add_u32 m0, s78, 0x3800\n"
"s_add_u32 s72, s79, 0x23000\n"
"buffer_load_dwordx4 v22, s[24:27], s72 offen lds\n"
"buffer_load_dwordx4 v23, s[24:27], s72 offen offset:1024 lds\n"
"s_add_u32 m0, s78, 0x4000\n"
"s_add_u32 s72, s79, 0x28000\n"
"buffer_load_dwordx4 v22, s[24:27], s72 offen lds\n"
"buffer_load_dwordx4 v23, s[24:27], s72 offen offset:1024 lds\n"
"s_add_u32 s79, s79, 0x80\n"
"s_cmp_ge_u32 s79, 0x500\n"
"s_cselect_b32 s72, 0x500, 0\n"
"s_sub_u32 s79, s79, s72\n"
"s_mov_b32 s73, 8\n"
"s_waitcnt lgkmcnt(11)\n"
"LQXa_%=:\n"
"v_mfma_f32_16x16x32_bf16 v[96:99], a[160:163], a[192:195], v[96:99]\n"
"ds_read_b64_tr_b16 a[184:185], v8 offset:512\n"
"v_mfma_f32_16x16x32_bf16 v[100:103], a[160:163], a[196:199], v[100:103]\n"
"ds_read_b64_tr_b16 a[186:187], v8 offset:768\n"
"v_mfma_f32_16x16x32_bf16 v[160:163], a[160:163], a[200:203], v[160:163]\n"
"ds_read_b64_tr_b16 a[188:189], v9 offset:512\n"
"v_mfma_f32_16x16x32_bf16 v[164:167], a[160:163], a[204:207], v[164:167]\n"
"ds_read_b64_tr_b16 a[190:191], v9 offset:768\n"
"s_add_u32 m0, s96, 0x0\n"
"s_add_u32 s72, s79, 0x0\n"
"s_waitcnt lgkmcnt(9)\n"
"v_mfma_f32_16x16x32_bf16 v[80:83], a[160:163], a[208:211], v[80:83]\n"
"buffer_load_dwordx4 v22, s[24:27], s72 offen lds\n"
"v_mfma_f32_16x16x32_bf16 v[104:107], a[164:167], a[192:195], v[104:107]\n"
"buffer_load_dwordx4 v23, s[24:27], s72 offen offset:1024 lds\n"
"v_mfma_f32_16x16x32_bf16 v[108:111], a[164:167], a[196:199], v[108:111]\n"
"v_mfma_f32_16x16x32_bf16 v[168:171], a[164:167], a[200:203], v[168:171]\n"
"v_mfma_f32_16x16x32_bf16 v[172:175], a[164:167], a[204:207], v[172:175]\n"
"s_add_u32 m0, s96, 0x800\n"
"s_add_u32 s72, s79, 0x5000\n"
"v_mfma_f32_16x16x32_bf16 v[84:87], a[164:167], a[208:211], v[84:87]\n"
"buffer_load_dwordx4 v22, s[24:27], s72 offen lds\n"
"v_mfma_f32_16x16x32_bf16 v[112:115], a[168:171], a[192:195], v[112:115]\n"
"buffer_load_dwordx4 v23, s[24:27], s72 offen offset:1024 lds\n"
"v_mfma_f32_16x16x32_bf16 v[116:119], a[168:171], a[196:199], v[116:119]\n"
"v_mfma_f32_16x16x32_bf16 v[176:179], a[168:171], a[200:203], v[176:179]\n"
"v_mfma_f32_16x16x32_bf16 v[180:183], a[168:171], a[204:207], v[180:183]\n"
"v_mfma_f32_16x16x32_bf16 v[88:91], a[168:171], a[208:211], v[88:91]\n"
"s_add_u32 m0, s96, 0x1000\n"
"s_add_u32 s72, s79, 0xa000\n"
"s_waitcnt lgkmcnt(1)\n"
"v_mfma_f32_16x16x32_bf16 v[120:123], a[172:175], a[192:195], v[120:123]\n"
"buffer_load_dwordx4 v22, s[24:27], s72 offen lds\n"
"v_mfma_f32_16x16x32_bf16 v[124:127], a[172:175], a[196:199], v[124:127]\n"
"buffer_load_dwordx4 v23, s[24:27], s72 offen offset:1024 lds\n"
"v_mfma_f32_16x16x32_bf16 v[184:187], a[172:175], a[200:203], v[184:187]\n"
"v_mfma_f32_16x16x32_bf16 v[188:191], a[172:175], a[204:207], v[188:191]\n"
"v_mfma_f32_16x16x32_bf16 v[92:95], a[172:175], a[208:211], v[92:95]\n"
"s_waitcnt lgkmcnt(0)\n"
"s_barrier\n"
"v_add_u32 v6, s75, v0\n"
"ds_read_b128 a[212:215], v10 offset:512\n"
"v_mfma_f32_16x16x32_bf16 v[128:131], a[176:179], a[192:195], v[128:131]\n"
"ds_read_b64_tr_b16 a[160:161], v6 offset:0\n"
"ds_read_b64_tr_b16 a[162:163], v6 offset:256\n"
"v_mfma_f32_16x16x32_bf16 v[132:135], a[176:179], a[196:199], v[132:135]\n"
"ds_read_b128 a[216:219], v10 offset:2560\n"
"v_mfma_f32_16x16x32_bf16 v[192:195], a[176:179], a[200:203], v[192:195]\n"
"ds_read_b128 a[220:223], v10 offset:4608\n"
"s_add_u32 m0, s96, 0x1800\n"
"s_add_u32 s72, s79, 0xf000\n"
"v_mfma_f32_16x16x32_bf16 v[196:199], a[176:179], a[204:207], v[196:199]\n"
"ds_read_b128 a[224:227], v10 offset:6656\n"
"buffer_load_dwordx4 v22, s[24:27], s72 offen lds\n"
"v_add_u32 v7, s75, v1\n"
"v_mfma_f32_16x16x32_bf16 v[136:139], a[180:183], a[192:195], v[136:139]\n"
"ds_read_b128 a[228:231], v11 offset:512\n"
"buffer_load_dwordx4 v23, s[24:27], s72 offen offset:1024 lds\n"
"v_mfma_f32_16x16x32_bf16 v[140:143], a[180:183], a[196:199], v[140:143]\n"
"ds_read_b64_tr_b16 a[164:165], v7 offset:0\n"
"v_mfma_f32_16x16x32_bf16 v[200:203], a[180:183], a[200:203], v[200:203]\n"
"ds_read_b64_tr_b16 a[166:167], v7 offset:256\n"
"v_mfma_f32_16x16x32_bf16 v[204:207], a[180:183], a[204:207], v[204:207]\n"
"ds_read_b64_tr_b16 a[168:169], v6 offset:512\n"
"v_add_u32 v8, s75, v2\n"
"v_mfma_f32_16x16x32_bf16 v[144:147], a[184:187], a[192:195], v[144:147]\n"
"ds_read_b64_tr_b16 a[170:171], v6 offset:768\n"
"v_add_u32 v9, s75, v3\n"
"v_mfma_f32_16x16x32_bf16 v[148:151], a[184:187], a[196:199], v[148:151]\n"
"ds_read_b64_tr_b16 a[172:173], v7 offset:512\n"
"s_add_u32 m0, s96, 0x2000\n"
"s_add_u32 s72, s79, 0x14000\n"
"v_mfma_f32_16x16x32_bf16 v[208:211], a[184:187], a[200:203], v[208:211]\n"
"ds_read_b64_tr_b16 a[174:175], v7 offset:768\n"
"buffer_load_dwordx4 v22, s[24:27], s72 offen lds\n"
"v_mfma_f32_16x16x32_bf16 v[212:215], a[184:187], a[204:207], v[212:215]\n"
"ds_read_b64_tr_b16 a[176:177], v8 offset:0\n"
"buffer_load_dwordx4 v23, s[24:27], s72 offen offset:1024 lds\n"
"v_mfma_f32_16x16x32_bf16 v[152:155], a[188:191], a[192:195], v[152:155]\n"
"ds_read_b64_tr_b16 a[178:179], v8 offset:256\n"
"v_mfma_f32_16x16x32_bf16 v[156:159], a[188:191], a[196:199], v[156:159]\n"
"s_waitcnt lgkmcnt(10)\n"
"ds_read_b64_tr_b16 a[180:181], v9 offset:0\n"
"v_mfma_f32_16x16x32_bf16 v[216:219], a[188:191], a[200:203], v[216:219]\n"
"ds_read_b64_tr_b16 a[182:183], v9 offset:256\n"
"v_mfma_f32_16x16x32_bf16 v[220:223], a[188:191], a[204:207], v[220:223]\n"
"v_mfma_f32_16x16x32_bf16 v[96:99], a[160:163], a[212:215], v[96:99]\n"
"ds_read_b64_tr_b16 a[184:185], v8 offset:512\n"
"v_mfma_f32_16x16x32_bf16 v[100:103], a[160:163], a[216:219], v[100:103]\n"
"ds_read_b64_tr_b16 a[186:187], v8 offset:768\n"
"v_mfma_f32_16x16x32_bf16 v[160:163], a[160:163], a[220:223], v[160:163]\n"
"ds_read_b64_tr_b16 a[188:189], v9 offset:512\n"
"s_waitcnt lgkmcnt(10)\n"
"v_mfma_f32_16x16x32_bf16 v[164:167], a[160:163], a[224:227], v[164:167]\n"
"ds_read_b64_tr_b16 a[190:191], v9 offset:768\n"
"s_add_u32 m0, s96, 0x2800\n"
"s_add_u32 s72, s79, 0x19000\n"
"v_mfma_f32_16x16x32_bf16 v[80:83], a[160:163], a[228:231], v[80:83]\n"
"buffer_load_dwordx4 v22, s[24:27], s72 offen lds\n"
"v_mfma_f32_16x16x32_bf16 v[104:107], a[164:167], a[212:215], v[104:107]\n"
"buffer_load_dwordx4 v23, s[24:27], s72 offen offset:1024 lds\n"
"v_mfma_f32_16x16x32_bf16 v[108:111], a[164:167], a[216:219], v[108:111]\n"
"v_mfma_f32_16x16x32_bf16 v[168:171], a[164:167], a[220:223], v[168:171]\n"
"v_mfma_f32_16x16x32_bf16 v[172:175], a[164:167], a[224:227], v[172:175]\n"
"s_add_u32 m0, s96, 0x3000\n"
"s_add_u32 s72, s79, 0x1e000\n"
"v_mfma_f32_16x16x32_bf16 v[84:87], a[164:167], a[228:231], v[84:87]\n"
"buffer_load_dwordx4 v22, s[24:27], s72 offen lds\n"
"s_waitcnt lgkmcnt(4)\n"
"v_mfma_f32_16x16x32_bf16 v[112:115], a[168:171], a[212:215], v[112:115]\n"
"buffer_load_dwordx4 v23, s[24:27], s72 offen offset:1024 lds\n"
"v_mfma_f32_16x16x32_bf16 v[116:119], a[168:171], a[216:219], v[116:119]\n"
"v_mfma_f32_16x16x32_bf16 v[176:179], a[168:171], a[220:223], v[176:179]\n"
"v_mfma_f32_16x16x32_bf16 v[180:183], a[168:171], a[224:227], v[180:183]\n"
"v_mfma_f32_16x16x32_bf16 v[88:91], a[168:171], a[228:231], v[88:91]\n"
"s_add_u32 m0, s96, 0x3800\n"
"s_add_u32 s72, s79, 0x23000\n"
"v_mfma_f32_16x16x32_bf16 v[120:123], a[172:175], a[212:215], v[120:123]\n"
"buffer_load_dwordx4 v22, s[24:27], s72 offen lds\n"
"v_mfma_f32_16x16x32_bf16 v[124:127], a[172:175], a[216:219], v[124:127]\n"
"buffer_load_dwordx4 v23, s[24:27], s72 offen offset:1024 lds\n"
"v_mfma_f32_16x16x32_bf16 v[184:187], a[172:175], a[220:223], v[184:187]\n"
"v_mfma_f32_16x16x32_bf16 v[188:191], a[172:175], a[224:227], v[188:191]\n"
"v_mfma_f32_16x16x32_bf16 v[92:95], a[172:175], a[228:231], v[92:95]\n"
"s_waitcnt vmcnt(16) lgkmcnt(0)\n"
"s_barrier\n"
"v_add_u32 v10, s78, v4\n"
"v_add_u32 v6, s76, v0\n"
"ds_read_b128 a[192:195], v10 offset:0\n"
"v_mfma_f32_16x16x32_bf16 v[128:131], a[176:179], a[212:215], v[128:131]\n"
"ds_read_b64_tr_b16 a[160:161], v6 offset:0\n"
"ds_read_b64_tr_b16 a[162:163], v6 offset:256\n"
"v_mfma_f32_16x16x32_bf16 v[132:135], a[176:179], a[216:219], v[132:135]\n"
"ds_read_b128 a[196:199], v10 offset:2048\n"
"v_mfma_f32_16x16x32_bf16 v[192:195], a[176:179], a[220:223], v[192:195]\n"
"ds_read_b128 a[200:203], v10 offset:4096\n"
"s_add_u32 m0, s96, 0x4000\n"
"s_add_u32 s72, s79, 0x28000\n"
"v_add_u32 v11, s78, v5\n"
"v_mfma_f32_16x16x32_bf16 v[196:199], a[176:179], a[224:227], v[196:199]\n"
"ds_read_b128 a[204:207], v10 offset:6144\n"
"buffer_load_dwordx4 v22, s[24:27], s72 offen lds\n"
"v_add_u32 v7, s76, v1\n"
"v_mfma_f32_16x16x32_bf16 v[136:139], a[180:183], a[212:215], v[136:139]\n"
"ds_read_b128 a[208:211], v11 offset:0\n"
"buffer_load_dwordx4 v23, s[24:27], s72 offen offset:1024 lds\n"
"v_mfma_f32_16x16x32_bf16 v[140:143], a[180:183], a[216:219], v[140:143]\n"
"ds_read_b64_tr_b16 a[164:165], v7 offset:0\n"
"v_mfma_f32_16x16x32_bf16 v[200:203], a[180:183], a[220:223], v[200:203]\n"
"ds_read_b64_tr_b16 a[166:167], v7 offset:256\n"
"v_mfma_f32_16x16x32_bf16 v[204:207], a[180:183], a[224:227], v[204:207]\n"
"ds_read_b64_tr_b16 a[168:169], v6 offset:512\n"
"v_add_u32 v8, s76, v2\n"
"v_mfma_f32_16x16x32_bf16 v[144:147], a[184:187], a[212:215], v[144:147]\n"
"ds_read_b64_tr_b16 a[170:171], v6 offset:768\n"
"v_add_u32 v9, s76, v3\n"
"v_mfma_f32_16x16x32_bf16 v[148:151], a[184:187], a[216:219], v[148:151]\n"
"ds_read_b64_tr_b16 a[172:173], v7 offset:512\n"
"s_add_u32 s79, s79, 0x80\n"
"s_cmp_ge_u32 s79, 0x500\n"
"v_mfma_f32_16x16x32_bf16 v[208:211], a[184:187], a[220:223], v[208:211]\n"
"ds_read_b64_tr_b16 a[174:175], v7 offset:768\n"
"s_cselect_b32 s72, 0x500, 0\n"
"s_sub_u32 s79, s79, s72\n"
"v_mfma_f32_16x16x32_bf16 v[212:215], a[184:187], a[224:227], v[212:215]\n"
"ds_read_b64_tr_b16 a[176:177], v8 offset:0\n"
"s_mov_b32 s72, s74\n"
"s_mov_b32 s74, s76\n"
"v_mfma_f32_16x16x32_bf16 v[152:155], a[188:191], a[212:215], v[152:155]\n"
"s_mov_b32 s76, s72\n"
"ds_read_b64_tr_b16 a[178:179], v8 offset:256\n"
"s_mov_b32 s72, s75\n"
"s_mov_b32 s75, s99\n"
"v_mfma_f32_16x16x32_bf16 v[156:159], a[188:191], a[216:219], v[156:159]\n"
"s_waitcnt lgkmcnt(9)\n"
"ds_read_b64_tr_b16 a[180:181], v9 offset:0\n"
"s_mov_b32 s99, s72\n"
"s_mov_b32 s72, s77\n"
"v_mfma_f32_16x16x32_bf16 v[216:219], a[188:191], a[220:223], v[216:219]\n"
"ds_read_b64_tr_b16 a[182:183], v9 offset:256\n"
"s_mov_b32 s77, s78\n"
"s_mov_b32 s78, s96\n"
"v_mfma_f32_16x16x32_bf16 v[220:223], a[188:191], a[224:227], v[220:223]\n"
"s_mov_b32 s96, s72\n"
"s_sub_u32 s73, s73, 1\n"
"s_cmp_lg_u32 s73, 0\n"
"s_cbranch_scc1 LQXa_%=\n"
"v_mfma_f32_16x16x32_bf16 v[96:99], a[160:163], a[192:195], v[96:99]\n"
"ds_read_b64_tr_b16 a[184:185], v8 offset:512\n"
"v_mfma_f32_16x16x32_bf16 v[100:103], a[160:163], a[196:199], v[100:103]\n"
"ds_read_b64_tr_b16 a[186:187], v8 offset:768\n"
"v_mfma_f32_16x16x32_bf16 v[160:163], a[160:163], a[200:203], v[160:163]\n"
"ds_read_b64_tr_b16 a[188:189], v9 offset:512\n"
"v_mfma_f32_16x16x32_bf16 v[164:167], a[160:163], a[204:207], v[164:167]\n"
"ds_read_b64_tr_b16 a[190:191], v9 offset:768\n"
"s_waitcnt lgkmcnt(9)\n"
"v_mfma_f32_16x16x32_bf16 v[80:83], a[160:163], a[208:211], v[80:83]\n"
"v_mfma_f32_16x16x32_bf16 v[104:107], a[164:167], a[192:195], v[104:107]\n"
"v_mfma_f32_16x16x32_bf16 v[108:111], a[164:167], a[196:199], v[108:111]\n"
"v_mfma_f32_16x16x32_bf16 v[168:171], a[164:167], a[200:203], v[168:171]\n"
"v_mfma_f32_16x16x32_bf16 v[172:175], a[164:167], a[204:207], v[172:175]\n"
"v_mfma_f32_16x16x32_bf16 v[84:87], a[164:167], a[208:211], v[84:87]\n"
"v_mfma_f32_16x16x32_bf16 v[112:115], a[168:171], a[192:195], v[112:115]\n"
"v_mfma_f32_16x16x32_bf16 v[116:119], a[168:171], a[196:199], v[116:119]\n"
"v_mfma_f32_16x16x32_bf16 v[176:179], a[168:171], a[200:203], v[176:179]\n"
"v_mfma_f32_16x16x32_bf16 v[180:183], a[168:171], a[204:207], v[180:183]\n"
"v_mfma_f32_16x16x32_bf16 v[88:91], a[168:171], a[208:211], v[88:91]\n"
"s_waitcnt lgkmcnt(2)\n"
"v_mfma_f32_16x16x32_bf16 v[120:123], a[172:175], a[192:195], v[120:123]\n"
"v_mfma_f32_16x16x32_bf16 v[124:127], a[172:175], a[196:199], v[124:127]\n"
"v_mfma_f32_16x16x32_bf16 v[184:187], a[172:175], a[200:203], v[184:187]\n"
"v_mfma_f32_16x16x32_bf16 v[188:191], a[172:175], a[204:207], v[188:191]\n"
"v_mfma_f32_16x16x32_bf16 v[92:95], a[172:175], a[208:211], v[92:95]\n"
"s_waitcnt lgkmcnt(0)\n"
"s_barrier\n"
"v_add_u32 v6, s75, v0\n"
"ds_read_b128 a[212:215], v10 offset:512\n"
"v_mfma_f32_16x16x32_bf16 v[128:131], a[176:179], a[192:195], v[128:131]\n"
"ds_read_b64_tr_b16 a[160:161], v6 offset:0\n"
"ds_read_b64_tr_b16 a[162:163], v6 offset:256\n"
"v_mfma_f32_16x16x32_bf16 v[132:135], a[176:179], a[196:199], v[132:135]\n"
"ds_read_b128 a[216:219], v10 offset:2560\n"
"v_mfma_f32_16x16x32_bf16 v[192:195], a[176:179], a[200:203], v[192:195]\n"
"ds_read_b128 a[220:223], v10 offset:4608\n"
"v_mfma_f32_16x16x32_bf16 v[196:199], a[176:179], a[204:207], v[196:199]\n"
"ds_read_b128 a[224:227], v10 offset:6656\n"
"v_add_u32 v7, s75, v1\n"
"v_mfma_f32_16x16x32_bf16 v[136:139], a[180:183], a[192:195], v[136:139]\n"
"ds_read_b128 a[228:231], v11 offset:512\n"
"v_mfma_f32_16x16x32_bf16 v[140:143], a[180:183], a[196:199], v[140:143]\n"
"ds_read_b64_tr_b16 a[164:165], v7 offset:0\n"
"v_mfma_f32_16x16x32_bf16 v[200:203], a[180:183], a[200:203], v[200:203]\n"
"ds_read_b64_tr_b16 a[166:167], v7 offset:256\n"
"v_mfma_f32_16x16x32_bf16 v[204:207], a[180:183], a[204:207], v[204:207]\n"
"ds_read_b64_tr_b16 a[168:169], v6 offset:512\n"
"v_add_u32 v8, s75, v2\n"
"v_mfma_f32_16x16x32_bf16 v[144:147], a[184:187], a[192:195], v[144:147]\n"
"ds_read_b64_tr_b16 a[170:171], v6 offset:768\n"
"v_add_u32 v9, s75, v3\n"
"v_mfma_f32_16x16x32_bf16 v[148:151], a[184:187], a[196:199], v[148:151]\n"
"ds_read_b64_tr_b16 a[172:173], v7 offset:512\n"
"v_mfma_f32_16x16x32_bf16 v[208:211], a[184:187], a[200:203], v[208:211]\n"
"ds_read_b64_tr_b16 a[174:175], v7 offset:768\n"
"v_mfma_f32_16x16x32_bf16 v[212:215], a[184:187], a[204:207], v[212:215]\n"
"ds_read_b64_tr_b16 a[176:177], v8 offset:0\n"
"v_mfma_f32_16x16x32_bf16 v[152:155], a[188:191], a[192:195], v[152:155]\n"
"ds_read_b64_tr_b16 a[178:179], v8 offset:256\n"
"v_mfma_f32_16x16x32_bf16 v[156:159], a[188:191], a[196:199], v[156:159]\n"
"s_waitcnt lgkmcnt(10)\n"
"ds_read_b64_tr_b16 a[180:181], v9 offset:0\n"
"v_mfma_f32_16x16x32_bf16 v[216:219], a[188:191], a[200:203], v[216:219]\n"
"ds_read_b64_tr_b16 a[182:183], v9 offset:256\n"
"v_mfma_f32_16x16x32_bf16 v[220:223], a[188:191], a[204:207], v[220:223]\n"
"v_mfma_f32_16x16x32_bf16 v[96:99], a[160:163], a[212:215], v[96:99]\n"
"ds_read_b64_tr_b16 a[184:185], v8 offset:512\n"
"v_mfma_f32_16x16x32_bf16 v[100:103], a[160:163], a[216:219], v[100:103]\n"
"ds_read_b64_tr_b16 a[186:187], v8 offset:768\n"
"v_mfma_f32_16x16x32_bf16 v[160:163], a[160:163], a[220:223], v[160:163]\n"
"ds_read_b64_tr_b16 a[188:189], v9 offset:512\n"
"s_waitcnt lgkmcnt(10)\n"
"v_mfma_f32_16x16x32_bf16 v[164:167], a[160:163], a[224:227], v[164:167]\n"
"ds_read_b64_tr_b16 a[190:191], v9 offset:768\n"
"v_mfma_f32_16x16x32_bf16 v[80:83], a[160:163], a[228:231], v[80:83]\n"
"v_mfma_f32_16x16x32_bf16 v[104:107], a[164:167], a[212:215], v[104:107]\n"
"v_mfma_f32_16x16x32_bf16 v[108:111], a[164:167], a[216:219], v[108:111]\n"
"v_mfma_f32_16x16x32_bf16 v[168:171], a[164:167], a[220:223], v[168:171]\n"
"v_mfma_f32_16x16x32_bf16 v[172:175], a[164:167], a[224:227], v[172:175]\n"
"v_mfma_f32_16x16x32_bf16 v[84:87], a[164:167], a[228:231], v[84:87]\n"
"s_waitcnt lgkmcnt(5)\n"
"v_mfma_f32_16x16x32_bf16 v[112:115], a[168:171], a[212:215], v[112:115]\n"
"v_mfma_f32_16x16x32_bf16 v[116:119], a[168:171], a[216:219], v[116:119]\n"
"v_mfma_f32_16x16x32_bf16 v[176:179], a[168:171], a[220:223], v[176:179]\n"
"v_mfma_f32_16x16x32_bf16 v[180:183], a[168:171], a[224:227], v[180:183]\n"
"v_mfma_f32_16x16x32_bf16 v[88:91], a[168:171], a[228:231], v[88:91]\n"
"v_mfma_f32_16x16x32_bf16 v[120:123], a[172:175], a[212:215], v[120:123]\n"
"v_mfma_f32_16x16x32_bf16 v[124:127], a[172:175], a[216:219], v[124:127]\n"
"v_mfma_f32_16x16x32_bf16 v[184:187], a[172:175], a[220:223], v[184:187]\n"
"v_mfma_f32_16x16x32_bf16 v[188:191], a[172:175], a[224:227], v[188:191]\n"
"v_mfma_f32_16x16x32_bf16 v[92:95], a[172:175], a[228:231], v[92:95]\n"
"s_waitcnt vmcnt(0) lgkmcnt(0)\n"
"s_barrier\n"
"v_add_u32 v10, s78, v4\n"
"v_add_u32 v6, s76, v0\n"
"ds_read_b128 a[192:195], v10 offset:0\n"
"v_mfma_f32_16x16x32_bf16 v[128:131], a[176:179], a[212:215], v[128:131]\n"
"ds_read_b64_tr_b16 a[160:161], v6 offset:0\n"
"ds_read_b64_tr_b16 a[162:163], v6 offset:256\n"
"v_mfma_f32_16x16x32_bf16 v[132:135], a[176:179], a[216:219], v[132:135]\n"
"ds_read_b128 a[196:199], v10 offset:2048\n"
"v_mfma_f32_16x16x32_bf16 v[192:195], a[176:179], a[220:223], v[192:195]\n"
"ds_read_b128 a[200:203], v10 offset:4096\n"
"v_add_u32 v11, s78, v5\n"
"v_mfma_f32_16x16x32_bf16 v[196:199], a[176:179], a[224:227], v[196:199]\n"
"ds_read_b128 a[204:207], v10 offset:6144\n"
"v_add_u32 v7, s76, v1\n"
"v_mfma_f32_16x16x32_bf16 v[136:139], a[180:183], a[212:215], v[136:139]\n"
"ds_read_b128 a[208:211], v11 offset:0\n"
"v_mfma_f32_16x16x32_bf16 v[140:143], a[180:183], a[216:219], v[140:143]\n"
"ds_read_b64_tr_b16 a[164:165], v7 offset:0\n"
"v_mfma_f32_16x16x32_bf16 v[200:203], a[180:183], a[220:223], v[200:203]\n"
"ds_read_b64_tr_b16 a[166:167], v7 offset:256\n"
"v_mfma_f32_16x16x32_bf16 v[204:207], a[180:183], a[224:227], v[204:207]\n"
"ds_read_b64_tr_b16 a[168:169], v6 offset:512\n"
"v_add_u32 v8, s76, v2\n"
"v_mfma_f32_16x16x32_bf16 v[144:147], a[184:187], a[212:215], v[144:147]\n"
"ds_read_b64_tr_b16 a[170:171], v6 offset:768\n"
"v_add_u32 v9, s76, v3\n"
"v_mfma_f32_16x16x32_bf16 v[148:151], a[184:187], a[216:219], v[148:151]\n"
"ds_read_b64_tr_b16 a[172:173], v7 offset:512\n"
"s_mov_b32 s72, s74\n"
"s_mov_b32 s74, s76\n"
"v_mfma_f32_16x16x32_bf16 v[208:211], a[184:187], a[220:223], v[208:211]\n"
"ds_read_b64_tr_b16 a[174:175], v7 offset:768\n"
"s_mov_b32 s76, s72\n"
"s_mov_b32 s72, s75\n"
"v_mfma_f32_16x16x32_bf16 v[212:215], a[184:187], a[224:227], v[212:215]\n"
"ds_read_b64_tr_b16 a[176:177], v8 offset:0\n"
"s_mov_b32 s75, s99\n"
"s_mov_b32 s99, s72\n"
"v_mfma_f32_16x16x32_bf16 v[152:155], a[188:191], a[212:215], v[152:155]\n"
"s_mov_b32 s72, s77\n"
"ds_read_b64_tr_b16 a[178:179], v8 offset:256\n"
"s_mov_b32 s77, s78\n"
"s_mov_b32 s78, s96\n"
"v_mfma_f32_16x16x32_bf16 v[156:159], a[188:191], a[216:219], v[156:159]\n"
"s_waitcnt lgkmcnt(9)\n"
"ds_read_b64_tr_b16 a[180:181], v9 offset:0\n"
"s_mov_b32 s96, s72\n"
"v_mfma_f32_16x16x32_bf16 v[216:219], a[188:191], a[220:223], v[216:219]\n"
"ds_read_b64_tr_b16 a[182:183], v9 offset:256\n"
"v_mfma_f32_16x16x32_bf16 v[220:223], a[188:191], a[224:227], v[220:223]\n"
"v_mfma_f32_16x16x32_bf16 v[96:99], a[160:163], a[192:195], v[96:99]\n"
"ds_read_b64_tr_b16 a[184:185], v8 offset:512\n"
"v_mfma_f32_16x16x32_bf16 v[100:103], a[160:163], a[196:199], v[100:103]\n"
"ds_read_b64_tr_b16 a[186:187], v8 offset:768\n"
"v_mfma_f32_16x16x32_bf16 v[160:163], a[160:163], a[200:203], v[160:163]\n"
"ds_read_b64_tr_b16 a[188:189], v9 offset:512\n"
"v_mfma_f32_16x16x32_bf16 v[164:167], a[160:163], a[204:207], v[164:167]\n"
"ds_read_b64_tr_b16 a[190:191], v9 offset:768\n"
"s_waitcnt lgkmcnt(9)\n"
"v_mfma_f32_16x16x32_bf16 v[80:83], a[160:163], a[208:211], v[80:83]\n"
"s_add_u32 m0, s97, 0x0\n"
"s_add_u32 s72, s98, 0x0\n"
"v_mfma_f32_16x16x32_bf16 v[104:107], a[164:167], a[192:195], v[104:107]\n"
"buffer_load_dwordx4 v245, s[32:35], s72 offen nt lds\n"
"v_mfma_f32_16x16x32_bf16 v[108:111], a[164:167], a[196:199], v[108:111]\n"
"buffer_load_dwordx4 v246, s[32:35], s72 offen offset:2048 nt lds\n"
"v_mfma_f32_16x16x32_bf16 v[168:171], a[164:167], a[200:203], v[168:171]\n"
"v_mfma_f32_16x16x32_bf16 v[172:175], a[164:167], a[204:207], v[172:175]\n"
"v_mfma_f32_16x16x32_bf16 v[84:87], a[164:167], a[208:211], v[84:87]\n"
"v_mfma_f32_16x16x32_bf16 v[112:115], a[168:171], a[192:195], v[112:115]\n"
"s_add_u32 m0, s97, 0x400\n"
"s_add_u32 s72, s98, 0x80\n"
"v_mfma_f32_16x16x32_bf16 v[116:119], a[168:171], a[196:199], v[116:119]\n"
"buffer_load_dwordx4 v245, s[32:35], s72 offen nt lds\n"
"v_mfma_f32_16x16x32_bf16 v[176:179], a[168:171], a[200:203], v[176:179]\n"
"buffer_load_dwordx4 v246, s[32:35], s72 offen offset:2048 nt lds\n"
"v_mfma_f32_16x16x32_bf16 v[180:183], a[168:171], a[204:207], v[180:183]\n"
"v_mfma_f32_16x16x32_bf16 v[88:91], a[168:171], a[208:211], v[88:91]\n"
"s_waitcnt lgkmcnt(1)\n"
"v_mfma_f32_16x16x32_bf16 v[120:123], a[172:175], a[192:195], v[120:123]\n"
"v_mfma_f32_16x16x32_bf16 v[124:127], a[172:175], a[196:199], v[124:127]\n"
"v_mfma_f32_16x16x32_bf16 v[184:187], a[172:175], a[200:203], v[184:187]\n"
"v_mfma_f32_16x16x32_bf16 v[188:191], a[172:175], a[204:207], v[188:191]\n"
"v_mfma_f32_16x16x32_bf16 v[92:95], a[172:175], a[208:211], v[92:95]\n"
"s_waitcnt lgkmcnt(0)\n"
"s_barrier\n"
"v_add_u32 v6, s75, v0\n"
"ds_read_b128 a[212:215], v10 offset:512\n"
"v_mfma_f32_16x16x32_bf16 v[128:131], a[176:179], a[192:195], v[128:131]\n"
"ds_read_b64_tr_b16 a[160:161], v6 offset:0\n"
"ds_read_b64_tr_b16 a[162:163], v6 offset:256\n"
"v_mfma_f32_16x16x32_bf16 v[132:135], a[176:179], a[196:199], v[132:135]\n"
"ds_read_b128 a[216:219], v10 offset:2560\n"
"s_add_u32 m0, s97, 0x4000\n"
"s_add_u32 s72, s98, 0x2000\n"
"v_mfma_f32_16x16x32_bf16 v[192:195], a[176:179], a[200:203], v[192:195]\n"
"ds_read_b128 a[220:223], v10 offset:4608\n"
"buffer_load_dwordx4 v245, s[32:35], s72 offen nt lds\n"
"v_mfma_f32_16x16x32_bf16 v[196:199], a[176:179], a[204:207], v[196:199]\n"
"ds_read_b128 a[224:227], v10 offset:6656\n"
"buffer_load_dwordx4 v246, s[32:35], s72 offen offset:2048 nt lds\n"
"v_add_u32 v7, s75, v1\n"
"v_mfma_f32_16x16x32_bf16 v[136:139], a[180:183], a[192:195], v[136:139]\n"
"ds_read_b128 a[228:231], v11 offset:512\n"
"v_mfma_f32_16x16x32_bf16 v[140:143], a[180:183], a[196:199], v[140:143]\n"
"ds_read_b64_tr_b16 a[164:165], v7 offset:0\n"
"v_mfma_f32_16x16x32_bf16 v[200:203], a[180:183], a[200:203], v[200:203]\n"
"ds_read_b64_tr_b16 a[166:167], v7 offset:256\n"
"v_mfma_f32_16x16x32_bf16 v[204:207], a[180:183], a[204:207], v[204:207]\n"
"ds_read_b64_tr_b16 a[168:169], v6 offset:512\n"
"v_add_u32 v8, s75, v2\n"
"v_mfma_f32_16x16x32_bf16 v[144:147], a[184:187], a[192:195], v[144:147]\n"
"ds_read_b64_tr_b16 a[170:171], v6 offset:768\n"
"s_add_u32 m0, s97, 0x4400\n"
"s_add_u32 s72, s98, 0x2080\n"
"v_add_u32 v9, s75, v3\n"
"v_mfma_f32_16x16x32_bf16 v[148:151], a[184:187], a[196:199], v[148:151]\n"
"ds_read_b64_tr_b16 a[172:173], v7 offset:512\n"
"buffer_load_dwordx4 v245, s[32:35], s72 offen nt lds\n"
"v_mfma_f32_16x16x32_bf16 v[208:211], a[184:187], a[200:203], v[208:211]\n"
"ds_read_b64_tr_b16 a[174:175], v7 offset:768\n"
"buffer_load_dwordx4 v246, s[32:35], s72 offen offset:2048 nt lds\n"
"v_mfma_f32_16x16x32_bf16 v[212:215], a[184:187], a[204:207], v[212:215]\n"
"ds_read_b64_tr_b16 a[176:177], v8 offset:0\n"
"v_mfma_f32_16x16x32_bf16 v[152:155], a[188:191], a[192:195], v[152:155]\n"
"ds_read_b64_tr_b16 a[178:179], v8 offset:256\n"
"v_mfma_f32_16x16x32_bf16 v[156:159], a[188:191], a[196:199], v[156:159]\n"
"s_waitcnt lgkmcnt(10)\n"
"ds_read_b64_tr_b16 a[180:181], v9 offset:0\n"
"v_mfma_f32_16x16x32_bf16 v[216:219], a[188:191], a[200:203], v[216:219]\n"
"ds_read_b64_tr_b16 a[182:183], v9 offset:256\n"
"v_mfma_f32_16x16x32_bf16 v[220:223], a[188:191], a[204:207], v[220:223]\n"
"v_mfma_f32_16x16x32_bf16 v[96:99], a[160:163], a[212:215], v[96:99]\n"
"ds_read_b64_tr_b16 a[184:185], v8 offset:512\n"
"v_mfma_f32_16x16x32_bf16 v[100:103], a[160:163], a[216:219], v[100:103]\n"
"ds_read_b64_tr_b16 a[186:187], v8 offset:768\n"
"v_mfma_f32_16x16x32_bf16 v[160:163], a[160:163], a[220:223], v[160:163]\n"
"ds_read_b64_tr_b16 a[188:189], v9 offset:512\n"
"s_waitcnt lgkmcnt(10)\n"
"v_mfma_f32_16x16x32_bf16 v[164:167], a[160:163], a[224:227], v[164:167]\n"
"ds_read_b64_tr_b16 a[190:191], v9 offset:768\n"
"v_mfma_f32_16x16x32_bf16 v[80:83], a[160:163], a[228:231], v[80:83]\n"
"v_mfma_f32_16x16x32_bf16 v[104:107], a[164:167], a[212:215], v[104:107]\n"
"v_mfma_f32_16x16x32_bf16 v[108:111], a[164:167], a[216:219], v[108:111]\n"
"v_mfma_f32_16x16x32_bf16 v[168:171], a[164:167], a[220:223], v[168:171]\n"
"v_mfma_f32_16x16x32_bf16 v[172:175], a[164:167], a[224:227], v[172:175]\n"
"v_mfma_f32_16x16x32_bf16 v[84:87], a[164:167], a[228:231], v[84:87]\n"
"s_waitcnt lgkmcnt(5)\n"
"v_mfma_f32_16x16x32_bf16 v[112:115], a[168:171], a[212:215], v[112:115]\n"
"v_mfma_f32_16x16x32_bf16 v[116:119], a[168:171], a[216:219], v[116:119]\n"
"v_mfma_f32_16x16x32_bf16 v[176:179], a[168:171], a[220:223], v[176:179]\n"
"v_mfma_f32_16x16x32_bf16 v[180:183], a[168:171], a[224:227], v[180:183]\n"
"v_mfma_f32_16x16x32_bf16 v[88:91], a[168:171], a[228:231], v[88:91]\n"
"v_mfma_f32_16x16x32_bf16 v[120:123], a[172:175], a[212:215], v[120:123]\n"
"v_mfma_f32_16x16x32_bf16 v[124:127], a[172:175], a[216:219], v[124:127]\n"
"v_mfma_f32_16x16x32_bf16 v[184:187], a[172:175], a[220:223], v[184:187]\n"
"v_mfma_f32_16x16x32_bf16 v[188:191], a[172:175], a[224:227], v[188:191]\n"
"v_mfma_f32_16x16x32_bf16 v[92:95], a[172:175], a[228:231], v[92:95]\n"
"s_waitcnt lgkmcnt(0)\n"
"s_barrier\n"
"v_mfma_f32_16x16x32_bf16 v[128:131], a[176:179], a[212:215], v[128:131]\n"
"v_mfma_f32_16x16x32_bf16 v[132:135], a[176:179], a[216:219], v[132:135]\n"
"v_mfma_f32_16x16x32_bf16 v[192:195], a[176:179], a[220:223], v[192:195]\n"
"v_mfma_f32_16x16x32_bf16 v[196:199], a[176:179], a[224:227], v[196:199]\n"
"v_mfma_f32_16x16x32_bf16 v[136:139], a[180:183], a[212:215], v[136:139]\n"
"v_mfma_f32_16x16x32_bf16 v[140:143], a[180:183], a[216:219], v[140:143]\n"
"v_mfma_f32_16x16x32_bf16 v[200:203], a[180:183], a[220:223], v[200:203]\n"
"v_mfma_f32_16x16x32_bf16 v[204:207], a[180:183], a[224:227], v[204:207]\n"
"v_mfma_f32_16x16x32_bf16 v[144:147], a[184:187], a[212:215], v[144:147]\n"
"v_mfma_f32_16x16x32_bf16 v[148:151], a[184:187], a[216:219], v[148:151]\n"
"s_mov_b32 s72, s74\n"
"s_mov_b32 s74, s76\n"
"v_mfma_f32_16x16x32_bf16 v[208:211], a[184:187], a[220:223], v[208:211]\n"
"s_mov_b32 s76, s72\n"
"s_mov_b32 s72, s75\n"
"v_mfma_f32_16x16x32_bf16 v[212:215], a[184:187], a[224:227], v[212:215]\n"
"s_mov_b32 s75, s99\n"
"s_mov_b32 s99, s72\n"
"v_mfma_f32_16x16x32_bf16 v[152:155], a[188:191], a[212:215], v[152:155]\n"
"s_mov_b32 s72, s77\n"
"s_mov_b32 s77, s78\n"
"s_mov_b32 s78, s96\n"
"v_mfma_f32_16x16x32_bf16 v[156:159], a[188:191], a[216:219], v[156:159]\n"
"s_mov_b32 s96, s72\n"
"v_mfma_f32_16x16x32_bf16 v[216:219], a[188:191], a[220:223], v[216:219]\n"
"v_mfma_f32_16x16x32_bf16 v[220:223], a[188:191], a[224:227], v[220:223]\n"
"s_branch Lqdone_%=\n"
"LroleKV_%=:\n"
"v_mov_b32 v0, s16\n"
"v_mul_u32_u24 v0, 0x2000, v0\n"
"v_lshrrev_b32 v21, 4, v247\n"
"v_lshlrev_b32 v21, 11, v21\n"
"v_add_u32 v0, v21, v0\n"
"v_lshrrev_b32 v21, 2, v247\n"
"v_and_b32 v21, 0x3, v21\n"
"v_lshlrev_b32 v21, 6, v21\n"
"v_add_u32 v0, v21, v0\n"
"v_and_b32 v21, 0x3, v247\n"
"v_lshrrev_b32 v21, 1, v21\n"
"v_add_u32 v21, 0x0, v21\n"
"v_lshrrev_b32 v20, 4, v247\n"
"v_and_b32 v20, 0x1, v20\n"
"v_lshlrev_b32 v20, 1, v20\n"
"v_xor_b32 v21, v20, v21\n"
"v_lshlrev_b32 v21, 4, v21\n"
"v_add_u32 v0, v21, v0\n"
"v_and_b32 v21, 0x3, v247\n"
"v_and_b32 v21, 0x1, v21\n"
"v_lshlrev_b32 v21, 3, v21\n"
"v_add_u32 v0, v21, v0\n"
"v_mov_b32 v21, s17\n"
"v_mul_u32_u24 v21, 0x400, v21\n"
"v_add_u32 v0, v21, v0\n"
"v_mov_b32 v2, s16\n"
"v_mul_u32_u24 v2, 0x2000, v2\n"
"v_lshrrev_b32 v21, 4, v247\n"
"v_lshlrev_b32 v21, 11, v21\n"
"v_add_u32 v2, v21, v2\n"
"v_lshrrev_b32 v21, 2, v247\n"
"v_and_b32 v21, 0x3, v21\n"
"v_lshlrev_b32 v21, 6, v21\n"
"v_add_u32 v2, v21, v2\n"
"v_and_b32 v21, 0x3, v247\n"
"v_lshrrev_b32 v21, 1, v21\n"
"v_add_u32 v21, 0x0, v21\n"
"v_lshrrev_b32 v20, 4, v247\n"
"v_and_b32 v20, 0x1, v20\n"
"v_lshlrev_b32 v20, 1, v20\n"
"v_xor_b32 v21, v20, v21\n"
"v_lshlrev_b32 v21, 4, v21\n"
"v_add_u32 v2, v21, v2\n"
"v_and_b32 v21, 0x3, v247\n"
"v_and_b32 v21, 0x1, v21\n"
"v_lshlrev_b32 v21, 3, v21\n"
"v_add_u32 v2, v21, v2\n"
"v_mov_b32 v21, s17\n"
"v_xor_b32 v21, 0x1, v21\n"
"v_mul_u32_u24 v21, 0x400, v21\n"
"v_add_u32 v2, v21, v2\n"
"v_mov_b32 v4, s17\n"
"v_mul_u32_u24 v4, 0x40, v4\n"
"v_and_b32 v21, 0xf, v247\n"
"v_add_u32 v4, v21, v4\n"
"v_lshrrev_b32 v4, 3, v4\n"
"v_lshlrev_b32 v4, 10, v4\n"
"v_mov_b32 v21, s17\n"
"v_mul_u32_u24 v21, 0x40, v21\n"
"v_and_b32 v20, 0xf, v247\n"
"v_add_u32 v21, v20, v21\n"
"v_and_b32 v21, 0x7, v21\n"
"v_lshlrev_b32 v21, 6, v21\n"
"v_add_u32 v4, v21, v4\n"
"v_lshrrev_b32 v21, 4, v247\n"
"v_mov_b32 v20, s17\n"
"v_mul_u32_u24 v20, 0x40, v20\n"
"v_and_b32 v19, 0xf, v247\n"
"v_add_u32 v20, v19, v20\n"
"v_lshrrev_b32 v20, 2, v20\n"
"v_and_b32 v20, 0x1, v20\n"
"v_lshlrev_b32 v20, 1, v20\n"
"v_xor_b32 v21, v20, v21\n"
"v_lshlrev_b32 v21, 4, v21\n"
"v_add_u32 v4, v21, v4\n"
"v_and_b32 v5, 0xf, v247\n"
"v_add_u32 v5, 0x80, v5\n"
"v_lshrrev_b32 v5, 3, v5\n"
"v_lshlrev_b32 v5, 10, v5\n"
"v_and_b32 v21, 0xf, v247\n"
"v_add_u32 v21, 0x80, v21\n"
"v_and_b32 v21, 0x7, v21\n"
"v_lshlrev_b32 v21, 6, v21\n"
"v_add_u32 v5, v21, v5\n"
"v_lshrrev_b32 v21, 4, v247\n"
"v_and_b32 v20, 0xf, v247\n"
"v_add_u32 v20, 0x80, v20\n"
"v_lshrrev_b32 v20, 2, v20\n"
"v_and_b32 v20, 0x1, v20\n"
"v_lshlrev_b32 v20, 1, v20\n"
"v_xor_b32 v21, v20, v21\n"
"v_lshlrev_b32 v21, 4, v21\n"
"v_add_u32 v5, v21, v5\n"
"v_xor_b32 v1, 32, v0\n"
"v_xor_b32 v3, 32, v2\n"
"v_add_u32 v6, s74, v0\n"
"v_add_u32 v7, s74, v1\n"
"v_add_u32 v8, s74, v2\n"
"v_add_u32 v9, s74, v3\n"
"v_add_u32 v10, s77, v4\n"
"v_add_u32 v11, s77, v5\n"
"v_mov_b64 v[80:81], 0\n"
"v_mov_b64 v[82:83], 0\n"
"v_mov_b64 v[84:85], 0\n"
"v_mov_b64 v[86:87], 0\n"
"v_mov_b64 v[88:89], 0\n"
"v_mov_b64 v[90:91], 0\n"
"v_mov_b64 v[92:93], 0\n"
"v_mov_b64 v[94:95], 0\n"
"v_mov_b64 v[96:97], 0\n"
"v_mov_b64 v[98:99], 0\n"
"v_mov_b64 v[100:101], 0\n"
"v_mov_b64 v[102:103], 0\n"
"v_mov_b64 v[104:105], 0\n"
"v_mov_b64 v[106:107], 0\n"
"v_mov_b64 v[108:109], 0\n"
"v_mov_b64 v[110:111], 0\n"
"v_mov_b64 v[112:113], 0\n"
"v_mov_b64 v[114:115], 0\n"
"v_mov_b64 v[116:117], 0\n"
"v_mov_b64 v[118:119], 0\n"
"v_mov_b64 v[120:121], 0\n"
"v_mov_b64 v[122:123], 0\n"
"v_mov_b64 v[124:125], 0\n"
"v_mov_b64 v[126:127], 0\n"
"v_mov_b64 v[128:129], 0\n"
"v_mov_b64 v[130:131], 0\n"
"v_mov_b64 v[132:133], 0\n"
"v_mov_b64 v[134:135], 0\n"
"v_mov_b64 v[136:137], 0\n"
"v_mov_b64 v[138:139], 0\n"
"v_mov_b64 v[140:141], 0\n"
"v_mov_b64 v[142:143], 0\n"
"v_mov_b64 v[144:145], 0\n"
"v_mov_b64 v[146:147], 0\n"
"v_mov_b64 v[148:149], 0\n"
"v_mov_b64 v[150:151], 0\n"
"v_mov_b64 v[152:153], 0\n"
"v_mov_b64 v[154:155], 0\n"
"v_mov_b64 v[156:157], 0\n"
"v_mov_b64 v[158:159], 0\n"
"v_mov_b64 v[160:161], 0\n"
"v_mov_b64 v[162:163], 0\n"
"v_mov_b64 v[164:165], 0\n"
"v_mov_b64 v[166:167], 0\n"
"v_mov_b64 v[168:169], 0\n"
"v_mov_b64 v[170:171], 0\n"
"v_mov_b64 v[172:173], 0\n"
"v_mov_b64 v[174:175], 0\n"
"v_mov_b64 v[176:177], 0\n"
"v_mov_b64 v[178:179], 0\n"
"v_mov_b64 v[180:181], 0\n"
"v_mov_b64 v[182:183], 0\n"
"v_mov_b64 v[184:185], 0\n"
"v_mov_b64 v[186:187], 0\n"
"v_mov_b64 v[188:189], 0\n"
"v_mov_b64 v[190:191], 0\n"
"v_mov_b64 v[192:193], 0\n"
"v_mov_b64 v[194:195], 0\n"
"v_mov_b64 v[196:197], 0\n"
"v_mov_b64 v[198:199], 0\n"
"v_mov_b64 v[200:201], 0\n"
"v_mov_b64 v[202:203], 0\n"
"v_mov_b64 v[204:205], 0\n"
"v_mov_b64 v[206:207], 0\n"
"v_mov_b64 v[208:209], 0\n"
"v_mov_b64 v[210:211], 0\n"
"v_mov_b64 v[212:213], 0\n"
"v_mov_b64 v[214:215], 0\n"
"v_mov_b64 v[216:217], 0\n"
"v_mov_b64 v[218:219], 0\n"
"v_mov_b64 v[220:221], 0\n"
"v_mov_b64 v[222:223], 0\n"
"v_mov_b32 v238, s16\n"
"v_mul_u32_u24 v238, 0x2000, v238\n"
"v_add_u32 v238, 0x0, v238\n"
"v_and_b32 v21, 0x1f, v247\n"
"v_lshrrev_b32 v21, 3, v21\n"
"v_lshlrev_b32 v21, 11, v21\n"
"v_add_u32 v238, v21, v238\n"
"v_and_b32 v21, 0x1f, v247\n"
"v_and_b32 v21, 0x7, v21\n"
"v_lshlrev_b32 v21, 6, v21\n"
"v_add_u32 v238, v21, v238\n"
"v_lshrrev_b32 v21, 5, v247\n"
"v_add_u32 v21, 0x0, v21\n"
"v_and_b32 v20, 0x1f, v247\n"
"v_lshrrev_b32 v20, 2, v20\n"
"v_and_b32 v20, 0x3, v20\n"
"v_xor_b32 v21, v20, v21\n"
"v_lshlrev_b32 v21, 4, v21\n"
"v_add_u32 v238, v21, v238\n"
"v_xor_b32 v239, 32, v238\n"
"v_mov_b32 v240, s16\n"
"v_mul_u32_u24 v240, 0x2000, v240\n"
"v_add_u32 v240, 0x0, v240\n"
"v_mov_b32 v21, s17\n"
"v_mul_u32_u24 v21, 0x10, v21\n"
"v_and_b32 v20, 0xf, v247\n"
"v_add_u32 v21, v20, v21\n"
"v_lshrrev_b32 v21, 3, v21\n"
"v_lshlrev_b32 v21, 11, v21\n"
"v_add_u32 v240, v21, v240\n"
"v_mov_b32 v21, s17\n"
"v_mul_u32_u24 v21, 0x10, v21\n"
"v_and_b32 v20, 0xf, v247\n"
"v_add_u32 v21, v20, v21\n"
"v_and_b32 v21, 0x7, v21\n"
"v_lshlrev_b32 v21, 6, v21\n"
"v_add_u32 v240, v21, v240\n"
"v_lshrrev_b32 v21, 4, v247\n"
"v_mov_b32 v20, s17\n"
"v_mul_u32_u24 v20, 0x10, v20\n"
"v_and_b32 v19, 0xf, v247\n"
"v_add_u32 v20, v19, v20\n"
"v_lshrrev_b32 v20, 2, v20\n"
"v_and_b32 v20, 0x3, v20\n"
"v_xor_b32 v21, v20, v21\n"
"v_lshlrev_b32 v21, 4, v21\n"
"v_add_u32 v240, v21, v240\n"
"v_mov_b32 v241, s16\n"
"v_mul_u32_u24 v241, 0x2000, v241\n"
"v_add_u32 v241, 0x10000, v241\n"
"v_add_u32 v241, 0x0, v241\n"
"v_lshrrev_b32 v21, 5, v247\n"
"v_lshlrev_b32 v21, 2, v21\n"
"v_lshrrev_b32 v20, 2, v247\n"
"v_and_b32 v20, 0x3, v20\n"
"v_add_u32 v21, v20, v21\n"
"v_lshlrev_b32 v21, 6, v21\n"
"v_add_u32 v241, v21, v241\n"
"v_lshrrev_b32 v21, 4, v247\n"
"v_and_b32 v21, 0x1, v21\n"
"v_lshlrev_b32 v21, 1, v21\n"
"v_and_b32 v20, 0x3, v247\n"
"v_lshrrev_b32 v20, 1, v20\n"
"v_add_u32 v21, v20, v21\n"
"v_lshrrev_b32 v20, 5, v247\n"
"v_add_u32 v20, 0x0, v20\n"
"v_and_b32 v20, 0x3, v20\n"
"v_xor_b32 v21, v20, v21\n"
"v_lshlrev_b32 v21, 4, v21\n"
"v_add_u32 v241, v21, v241\n"
"v_and_b32 v21, 0x3, v247\n"
"v_and_b32 v21, 0x1, v21\n"
"v_lshlrev_b32 v21, 3, v21\n"
"v_add_u32 v241, v21, v241\n"
"v_add_u32 v241, 0xc000, v241\n"
"v_xor_b32 v242, 32, v241\n"
"v_add_u32 v242, 2048, v242\n"
"v_mov_b32 v243, s16\n"
"v_mul_u32_u24 v243, 0x2000, v243\n"
"v_add_u32 v243, 0x10000, v243\n"
"v_mov_b32 v21, s17\n"
"v_mul_u32_u24 v21, 0x2, v21\n"
"v_lshrrev_b32 v20, 4, v247\n"
"v_lshrrev_b32 v20, 1, v20\n"
"v_add_u32 v21, v20, v21\n"
"v_lshlrev_b32 v21, 11, v21\n"
"v_add_u32 v243, v21, v243\n"
"v_lshrrev_b32 v21, 4, v247\n"
"v_and_b32 v21, 0x1, v21\n"
"v_lshlrev_b32 v21, 2, v21\n"
"v_lshrrev_b32 v20, 2, v247\n"
"v_and_b32 v20, 0x3, v20\n"
"v_add_u32 v21, v20, v21\n"
"v_lshlrev_b32 v21, 6, v21\n"
"v_add_u32 v243, v21, v243\n"
"v_and_b32 v21, 0x3, v247\n"
"v_lshrrev_b32 v21, 1, v21\n"
"v_lshlrev_b32 v21, 1, v21\n"
"v_add_u32 v21, 0x0, v21\n"
"v_lshrrev_b32 v20, 4, v247\n"
"v_xor_b32 v21, v20, v21\n"
"v_lshlrev_b32 v21, 4, v21\n"
"v_add_u32 v243, v21, v243\n"
"v_and_b32 v21, 0x3, v247\n"
"v_and_b32 v21, 0x1, v21\n"
"v_lshlrev_b32 v21, 3, v21\n"
"v_add_u32 v243, v21, v243\n"
"v_add_u32 v243, 0xc000, v243\n"
"v_xor_b32 v244, 16, v243\n"
"v_lshrrev_b32 v245, 2, v247\n"
"v_and_b32 v245, 0x7, v245\n"
"v_mul_u32_u24 v245, 0x100, v245\n"
"v_lshrrev_b32 v21, 5, v247\n"
"v_lshlrev_b32 v21, 6, v21\n"
"v_add_u32 v245, v21, v245\n"
"v_and_b32 v21, 0x3, v247\n"
"v_lshrrev_b32 v20, 2, v247\n"
"v_and_b32 v20, 0x7, v20\n"
"v_lshrrev_b32 v20, 2, v20\n"
"v_xor_b32 v21, v20, v21\n"
"v_lshlrev_b32 v21, 4, v21\n"
"v_add_u32 v245, v21, v245\n"
"v_xor_b32 v246, 32, v245\n"
"s_waitcnt lgkmcnt(0)\n"
"s_add_u32 s92, s92, 1\n"
"s_addc_u32 s93, s93, 0\n"
"s_barrier\n"
"ds_read_b128 a[192:195], v10 offset:0\n"
"ds_read_b64_tr_b16 a[160:161], v6 offset:0\n"
"ds_read_b64_tr_b16 a[162:163], v6 offset:256\n"
"ds_read_b128 a[196:199], v10 offset:2048\n"
"ds_read_b128 a[200:203], v10 offset:4096\n"
"ds_read_b128 a[204:207], v10 offset:6144\n"
"ds_read_b128 a[208:211], v11 offset:0\n"
"ds_read_b64_tr_b16 a[164:165], v7 offset:0\n"
"ds_read_b64_tr_b16 a[166:167], v7 offset:256\n"
"ds_read_b64_tr_b16 a[168:169], v6 offset:512\n"
"ds_read_b64_tr_b16 a[170:171], v6 offset:768\n"
"ds_read_b64_tr_b16 a[172:173], v7 offset:512\n"
"ds_read_b64_tr_b16 a[174:175], v7 offset:768\n"
"ds_read_b64_tr_b16 a[176:177], v8 offset:0\n"
"ds_read_b64_tr_b16 a[178:179], v8 offset:256\n"
"s_waitcnt lgkmcnt(14)\n"
"ds_read_b64_tr_b16 a[180:181], v9 offset:0\n"
"s_waitcnt lgkmcnt(14)\n"
"ds_read_b64_tr_b16 a[182:183], v9 offset:256\n"
"s_mov_b32 s73, 9\n"
"s_lshr_b32 s21, s20, 2\n"
"s_lshl_b32 s21, s21, 5\n"
"s_lshr_b32 s22, s14, 3\n"
"s_add_u32 s21, s21, s22\n"
"s_mul_i32 s21, s21, 40960\n"
"s_add_u32 s0, s56, s21\n"
"s_addc_u32 s1, s57, 0\n"
"s_and_b32 s1, s1, 0xffff\n"
"s_mov_b32 s2, 40960\n"
"s_mov_b32 s3, 0x20000\n"
"s_mul_i32 s21, s22, 43\n"
"s_lshr_b32 s21, s21, 8\n"
"s_mul_i32 s21, s21, 6\n"
"s_sub_u32 s21, s22, s21\n"
"s_sub_u32 s11, 8, s21\n"
"v_lshlrev_b32 v24, 4, v247\n"
"s_waitcnt lgkmcnt(11)\n"
"LQNa_%=:\n"
"v_mfma_f32_16x16x32_bf16 v[96:99], a[160:163], a[192:195], v[96:99]\n"
"ds_read_b64_tr_b16 a[184:185], v8 offset:512\n"
"v_mfma_f32_16x16x32_bf16 v[100:103], a[160:163], a[196:199], v[100:103]\n"
"ds_read_b64_tr_b16 a[186:187], v8 offset:768\n"
"v_mfma_f32_16x16x32_bf16 v[160:163], a[160:163], a[200:203], v[160:163]\n"
"ds_read_b64_tr_b16 a[188:189], v9 offset:512\n"
"v_mfma_f32_16x16x32_bf16 v[164:167], a[160:163], a[204:207], v[164:167]\n"
"ds_read_b64_tr_b16 a[190:191], v9 offset:768\n"
"s_waitcnt lgkmcnt(4)\n"
"v_mfma_f32_16x16x32_bf16 v[80:83], a[160:163], a[208:211], v[80:83]\n"
"v_mfma_f32_16x16x32_bf16 v[104:107], a[164:167], a[192:195], v[104:107]\n"
"v_mfma_f32_16x16x32_bf16 v[108:111], a[164:167], a[196:199], v[108:111]\n"
"v_mfma_f32_16x16x32_bf16 v[168:171], a[164:167], a[200:203], v[168:171]\n"
"v_mfma_f32_16x16x32_bf16 v[172:175], a[164:167], a[204:207], v[172:175]\n"
"v_mfma_f32_16x16x32_bf16 v[84:87], a[164:167], a[208:211], v[84:87]\n"
"v_mfma_f32_16x16x32_bf16 v[112:115], a[168:171], a[192:195], v[112:115]\n"
"v_mfma_f32_16x16x32_bf16 v[116:119], a[168:171], a[196:199], v[116:119]\n"
"v_mfma_f32_16x16x32_bf16 v[176:179], a[168:171], a[200:203], v[176:179]\n"
"v_mfma_f32_16x16x32_bf16 v[180:183], a[168:171], a[204:207], v[180:183]\n"
"v_mfma_f32_16x16x32_bf16 v[88:91], a[168:171], a[208:211], v[88:91]\n"
"v_mfma_f32_16x16x32_bf16 v[120:123], a[172:175], a[192:195], v[120:123]\n"
"v_mfma_f32_16x16x32_bf16 v[124:127], a[172:175], a[196:199], v[124:127]\n"
"v_mfma_f32_16x16x32_bf16 v[184:187], a[172:175], a[200:203], v[184:187]\n"
"v_mfma_f32_16x16x32_bf16 v[188:191], a[172:175], a[204:207], v[188:191]\n"
"v_mfma_f32_16x16x32_bf16 v[92:95], a[172:175], a[208:211], v[92:95]\n"
"s_waitcnt lgkmcnt(0)\n"
"s_barrier\n"
"v_add_u32 v6, s75, v0\n"
"ds_read_b128 a[212:215], v10 offset:512\n"
"v_mfma_f32_16x16x32_bf16 v[128:131], a[176:179], a[192:195], v[128:131]\n"
"ds_read_b64_tr_b16 a[160:161], v6 offset:0\n"
"ds_read_b64_tr_b16 a[162:163], v6 offset:256\n"
"v_mfma_f32_16x16x32_bf16 v[132:135], a[176:179], a[196:199], v[132:135]\n"
"ds_read_b128 a[216:219], v10 offset:2560\n"
"v_mfma_f32_16x16x32_bf16 v[192:195], a[176:179], a[200:203], v[192:195]\n"
"ds_read_b128 a[220:223], v10 offset:4608\n"
"v_mfma_f32_16x16x32_bf16 v[196:199], a[176:179], a[204:207], v[196:199]\n"
"ds_read_b128 a[224:227], v10 offset:6656\n"
"v_add_u32 v7, s75, v1\n"
"v_mfma_f32_16x16x32_bf16 v[136:139], a[180:183], a[192:195], v[136:139]\n"
"ds_read_b128 a[228:231], v11 offset:512\n"
"v_mfma_f32_16x16x32_bf16 v[140:143], a[180:183], a[196:199], v[140:143]\n"
"ds_read_b64_tr_b16 a[164:165], v7 offset:0\n"
"v_mfma_f32_16x16x32_bf16 v[200:203], a[180:183], a[200:203], v[200:203]\n"
"ds_read_b64_tr_b16 a[166:167], v7 offset:256\n"
"v_mfma_f32_16x16x32_bf16 v[204:207], a[180:183], a[204:207], v[204:207]\n"
"ds_read_b64_tr_b16 a[168:169], v6 offset:512\n"
"v_add_u32 v8, s75, v2\n"
"v_mfma_f32_16x16x32_bf16 v[144:147], a[184:187], a[192:195], v[144:147]\n"
"ds_read_b64_tr_b16 a[170:171], v6 offset:768\n"
"v_add_u32 v9, s75, v3\n"
"v_mfma_f32_16x16x32_bf16 v[148:151], a[184:187], a[196:199], v[148:151]\n"
"ds_read_b64_tr_b16 a[172:173], v7 offset:512\n"
"v_mfma_f32_16x16x32_bf16 v[208:211], a[184:187], a[200:203], v[208:211]\n"
"ds_read_b64_tr_b16 a[174:175], v7 offset:768\n"
"v_mfma_f32_16x16x32_bf16 v[212:215], a[184:187], a[204:207], v[212:215]\n"
"ds_read_b64_tr_b16 a[176:177], v8 offset:0\n"
"v_mfma_f32_16x16x32_bf16 v[152:155], a[188:191], a[192:195], v[152:155]\n"
"ds_read_b64_tr_b16 a[178:179], v8 offset:256\n"
"v_mfma_f32_16x16x32_bf16 v[156:159], a[188:191], a[196:199], v[156:159]\n"
"s_waitcnt lgkmcnt(10)\n"
"ds_read_b64_tr_b16 a[180:181], v9 offset:0\n"
"v_mfma_f32_16x16x32_bf16 v[216:219], a[188:191], a[200:203], v[216:219]\n"
"ds_read_b64_tr_b16 a[182:183], v9 offset:256\n"
"v_mfma_f32_16x16x32_bf16 v[220:223], a[188:191], a[204:207], v[220:223]\n"
"v_mfma_f32_16x16x32_bf16 v[96:99], a[160:163], a[212:215], v[96:99]\n"
"ds_read_b64_tr_b16 a[184:185], v8 offset:512\n"
"v_mfma_f32_16x16x32_bf16 v[100:103], a[160:163], a[216:219], v[100:103]\n"
"ds_read_b64_tr_b16 a[186:187], v8 offset:768\n"
"v_mfma_f32_16x16x32_bf16 v[160:163], a[160:163], a[220:223], v[160:163]\n"
"ds_read_b64_tr_b16 a[188:189], v9 offset:512\n"
"s_waitcnt lgkmcnt(10)\n"
"v_mfma_f32_16x16x32_bf16 v[164:167], a[160:163], a[224:227], v[164:167]\n"
"ds_read_b64_tr_b16 a[190:191], v9 offset:768\n"
"v_mfma_f32_16x16x32_bf16 v[80:83], a[160:163], a[228:231], v[80:83]\n"
"v_mfma_f32_16x16x32_bf16 v[104:107], a[164:167], a[212:215], v[104:107]\n"
"v_mfma_f32_16x16x32_bf16 v[108:111], a[164:167], a[216:219], v[108:111]\n"
"v_mfma_f32_16x16x32_bf16 v[168:171], a[164:167], a[220:223], v[168:171]\n"
"v_mfma_f32_16x16x32_bf16 v[172:175], a[164:167], a[224:227], v[172:175]\n"
"v_mfma_f32_16x16x32_bf16 v[84:87], a[164:167], a[228:231], v[84:87]\n"
"s_waitcnt lgkmcnt(5)\n"
"v_mfma_f32_16x16x32_bf16 v[112:115], a[168:171], a[212:215], v[112:115]\n"
"v_mfma_f32_16x16x32_bf16 v[116:119], a[168:171], a[216:219], v[116:119]\n"
"v_mfma_f32_16x16x32_bf16 v[176:179], a[168:171], a[220:223], v[176:179]\n"
"v_mfma_f32_16x16x32_bf16 v[180:183], a[168:171], a[224:227], v[180:183]\n"
"v_mfma_f32_16x16x32_bf16 v[88:91], a[168:171], a[228:231], v[88:91]\n"
"v_mfma_f32_16x16x32_bf16 v[120:123], a[172:175], a[212:215], v[120:123]\n"
"v_mfma_f32_16x16x32_bf16 v[124:127], a[172:175], a[216:219], v[124:127]\n"
"v_mfma_f32_16x16x32_bf16 v[184:187], a[172:175], a[220:223], v[184:187]\n"
"v_mfma_f32_16x16x32_bf16 v[188:191], a[172:175], a[224:227], v[188:191]\n"
"v_mfma_f32_16x16x32_bf16 v[92:95], a[172:175], a[228:231], v[92:95]\n"
"s_waitcnt lgkmcnt(0)\n"
"s_barrier\n"
"v_add_u32 v10, s78, v4\n"
"v_add_u32 v6, s76, v0\n"
"ds_read_b128 a[192:195], v10 offset:0\n"
"v_mfma_f32_16x16x32_bf16 v[128:131], a[176:179], a[212:215], v[128:131]\n"
"ds_read_b64_tr_b16 a[160:161], v6 offset:0\n"
"ds_read_b64_tr_b16 a[162:163], v6 offset:256\n"
"v_mfma_f32_16x16x32_bf16 v[132:135], a[176:179], a[216:219], v[132:135]\n"
"ds_read_b128 a[196:199], v10 offset:2048\n"
"v_mfma_f32_16x16x32_bf16 v[192:195], a[176:179], a[220:223], v[192:195]\n"
"ds_read_b128 a[200:203], v10 offset:4096\n"
"v_add_u32 v11, s78, v5\n"
"v_mfma_f32_16x16x32_bf16 v[196:199], a[176:179], a[224:227], v[196:199]\n"
"ds_read_b128 a[204:207], v10 offset:6144\n"
"v_add_u32 v7, s76, v1\n"
"v_mfma_f32_16x16x32_bf16 v[136:139], a[180:183], a[212:215], v[136:139]\n"
"ds_read_b128 a[208:211], v11 offset:0\n"
"v_mfma_f32_16x16x32_bf16 v[140:143], a[180:183], a[216:219], v[140:143]\n"
"ds_read_b64_tr_b16 a[164:165], v7 offset:0\n"
"v_mfma_f32_16x16x32_bf16 v[200:203], a[180:183], a[220:223], v[200:203]\n"
"ds_read_b64_tr_b16 a[166:167], v7 offset:256\n"
"v_mfma_f32_16x16x32_bf16 v[204:207], a[180:183], a[224:227], v[204:207]\n"
"ds_read_b64_tr_b16 a[168:169], v6 offset:512\n"
"v_add_u32 v8, s76, v2\n"
"v_mfma_f32_16x16x32_bf16 v[144:147], a[184:187], a[212:215], v[144:147]\n"
"ds_read_b64_tr_b16 a[170:171], v6 offset:768\n"
"v_add_u32 v9, s76, v3\n"
"v_mfma_f32_16x16x32_bf16 v[148:151], a[184:187], a[216:219], v[148:151]\n"
"ds_read_b64_tr_b16 a[172:173], v7 offset:512\n"
"s_mov_b32 s72, s74\n"
"s_mov_b32 s74, s76\n"
"v_mfma_f32_16x16x32_bf16 v[208:211], a[184:187], a[220:223], v[208:211]\n"
"ds_read_b64_tr_b16 a[174:175], v7 offset:768\n"
"s_mov_b32 s76, s72\n"
"s_mov_b32 s72, s75\n"
"v_mfma_f32_16x16x32_bf16 v[212:215], a[184:187], a[224:227], v[212:215]\n"
"ds_read_b64_tr_b16 a[176:177], v8 offset:0\n"
"s_mov_b32 s75, s99\n"
"s_mov_b32 s99, s72\n"
"v_mfma_f32_16x16x32_bf16 v[152:155], a[188:191], a[212:215], v[152:155]\n"
"s_mov_b32 s72, s77\n"
"ds_read_b64_tr_b16 a[178:179], v8 offset:256\n"
"s_mov_b32 s77, s78\n"
"s_mov_b32 s78, s96\n"
"v_mfma_f32_16x16x32_bf16 v[156:159], a[188:191], a[216:219], v[156:159]\n"
"s_waitcnt lgkmcnt(9)\n"
"ds_read_b64_tr_b16 a[180:181], v9 offset:0\n"
"s_mov_b32 s96, s72\n"
"v_mfma_f32_16x16x32_bf16 v[216:219], a[188:191], a[220:223], v[216:219]\n"
"ds_read_b64_tr_b16 a[182:183], v9 offset:256\n"
"v_mfma_f32_16x16x32_bf16 v[220:223], a[188:191], a[224:227], v[220:223]\n"
"s_cmp_lg_u32 s73, s11\n"
"s_cbranch_scc1 Lnowpf_%=\n"
"s_mov_b32 m0, 0x26000\n"
"s_mov_b32 s72, 0\n"
"buffer_load_dwordx4 v24, s[0:3], s72 offen lds\n"
"buffer_load_dwordx4 v24, s[0:3], s72 offen offset:1024 lds\n"
"buffer_load_dwordx4 v24, s[0:3], s72 offen offset:2048 lds\n"
"buffer_load_dwordx4 v24, s[0:3], s72 offen offset:3072 lds\n"
"s_mov_b32 s72, 4096\n"
"buffer_load_dwordx4 v24, s[0:3], s72 offen lds\n"
"buffer_load_dwordx4 v24, s[0:3], s72 offen offset:1024 lds\n"
"buffer_load_dwordx4 v24, s[0:3], s72 offen offset:2048 lds\n"
"buffer_load_dwordx4 v24, s[0:3], s72 offen offset:3072 lds\n"
"s_mov_b32 s72, 8192\n"
"buffer_load_dwordx4 v24, s[0:3], s72 offen lds\n"
"buffer_load_dwordx4 v24, s[0:3], s72 offen offset:1024 lds\n"
"buffer_load_dwordx4 v24, s[0:3], s72 offen offset:2048 lds\n"
"buffer_load_dwordx4 v24, s[0:3], s72 offen offset:3072 lds\n"
"s_mov_b32 s72, 12288\n"
"buffer_load_dwordx4 v24, s[0:3], s72 offen lds\n"
"buffer_load_dwordx4 v24, s[0:3], s72 offen offset:1024 lds\n"
"buffer_load_dwordx4 v24, s[0:3], s72 offen offset:2048 lds\n"
"buffer_load_dwordx4 v24, s[0:3], s72 offen offset:3072 lds\n"
"s_mov_b32 s72, 16384\n"
"buffer_load_dwordx4 v24, s[0:3], s72 offen lds\n"
"buffer_load_dwordx4 v24, s[0:3], s72 offen offset:1024 lds\n"
"buffer_load_dwordx4 v24, s[0:3], s72 offen offset:2048 lds\n"
"buffer_load_dwordx4 v24, s[0:3], s72 offen offset:3072 lds\n"
"s_mov_b32 s72, 20480\n"
"buffer_load_dwordx4 v24, s[0:3], s72 offen lds\n"
"buffer_load_dwordx4 v24, s[0:3], s72 offen offset:1024 lds\n"
"buffer_load_dwordx4 v24, s[0:3], s72 offen offset:2048 lds\n"
"buffer_load_dwordx4 v24, s[0:3], s72 offen offset:3072 lds\n"
"s_mov_b32 s72, 24576\n"
"buffer_load_dwordx4 v24, s[0:3], s72 offen lds\n"
"buffer_load_dwordx4 v24, s[0:3], s72 offen offset:1024 lds\n"
"buffer_load_dwordx4 v24, s[0:3], s72 offen offset:2048 lds\n"
"buffer_load_dwordx4 v24, s[0:3], s72 offen offset:3072 lds\n"
"s_mov_b32 s72, 28672\n"
"buffer_load_dwordx4 v24, s[0:3], s72 offen lds\n"
"buffer_load_dwordx4 v24, s[0:3], s72 offen offset:1024 lds\n"
"buffer_load_dwordx4 v24, s[0:3], s72 offen offset:2048 lds\n"
"buffer_load_dwordx4 v24, s[0:3], s72 offen offset:3072 lds\n"
"s_mov_b32 s72, 32768\n"
"buffer_load_dwordx4 v24, s[0:3], s72 offen lds\n"
"buffer_load_dwordx4 v24, s[0:3], s72 offen offset:1024 lds\n"
"buffer_load_dwordx4 v24, s[0:3], s72 offen offset:2048 lds\n"
"buffer_load_dwordx4 v24, s[0:3], s72 offen offset:3072 lds\n"
"s_mov_b32 s72, 36864\n"
"buffer_load_dwordx4 v24, s[0:3], s72 offen lds\n"
"buffer_load_dwordx4 v24, s[0:3], s72 offen offset:1024 lds\n"
"buffer_load_dwordx4 v24, s[0:3], s72 offen offset:2048 lds\n"
"buffer_load_dwordx4 v24, s[0:3], s72 offen offset:3072 lds\n"
"Lnowpf_%=:\n"
"s_sub_u32 s73, s73, 1\n"
"s_cmp_lg_u32 s73, 0\n"
"s_cbranch_scc1 LQNa_%=\n"
"v_mfma_f32_16x16x32_bf16 v[96:99], a[160:163], a[192:195], v[96:99]\n"
"ds_read_b64_tr_b16 a[184:185], v8 offset:512\n"
"v_mfma_f32_16x16x32_bf16 v[100:103], a[160:163], a[196:199], v[100:103]\n"
"ds_read_b64_tr_b16 a[186:187], v8 offset:768\n"
"v_mfma_f32_16x16x32_bf16 v[160:163], a[160:163], a[200:203], v[160:163]\n"
"ds_read_b64_tr_b16 a[188:189], v9 offset:512\n"
"v_mfma_f32_16x16x32_bf16 v[164:167], a[160:163], a[204:207], v[164:167]\n"
"ds_read_b64_tr_b16 a[190:191], v9 offset:768\n"
"s_waitcnt lgkmcnt(4)\n"
"v_mfma_f32_16x16x32_bf16 v[80:83], a[160:163], a[208:211], v[80:83]\n"
"s_add_u32 m0, s97, 0x0\n"
"s_add_u32 s72, s98, 0x0\n"
"v_mfma_f32_16x16x32_bf16 v[104:107], a[164:167], a[192:195], v[104:107]\n"
"buffer_load_dwordx4 v245, s[32:35], s72 offen nt lds\n"
"v_mfma_f32_16x16x32_bf16 v[108:111], a[164:167], a[196:199], v[108:111]\n"
"buffer_load_dwordx4 v246, s[32:35], s72 offen offset:2048 nt lds\n"
"v_mfma_f32_16x16x32_bf16 v[168:171], a[164:167], a[200:203], v[168:171]\n"
"v_mfma_f32_16x16x32_bf16 v[172:175], a[164:167], a[204:207], v[172:175]\n"
"v_mfma_f32_16x16x32_bf16 v[84:87], a[164:167], a[208:211], v[84:87]\n"
"v_mfma_f32_16x16x32_bf16 v[112:115], a[168:171], a[192:195], v[112:115]\n"
"s_add_u32 m0, s97, 0x400\n"
"s_add_u32 s72, s98, 0x80\n"
"v_mfma_f32_16x16x32_bf16 v[116:119], a[168:171], a[196:199], v[116:119]\n"
"buffer_load_dwordx4 v245, s[32:35], s72 offen nt lds\n"
"v_mfma_f32_16x16x32_bf16 v[176:179], a[168:171], a[200:203], v[176:179]\n"
"buffer_load_dwordx4 v246, s[32:35], s72 offen offset:2048 nt lds\n"
"v_mfma_f32_16x16x32_bf16 v[180:183], a[168:171], a[204:207], v[180:183]\n"
"v_mfma_f32_16x16x32_bf16 v[88:91], a[168:171], a[208:211], v[88:91]\n"
"v_mfma_f32_16x16x32_bf16 v[120:123], a[172:175], a[192:195], v[120:123]\n"
"v_mfma_f32_16x16x32_bf16 v[124:127], a[172:175], a[196:199], v[124:127]\n"
"v_mfma_f32_16x16x32_bf16 v[184:187], a[172:175], a[200:203], v[184:187]\n"
"v_mfma_f32_16x16x32_bf16 v[188:191], a[172:175], a[204:207], v[188:191]\n"
"v_mfma_f32_16x16x32_bf16 v[92:95], a[172:175], a[208:211], v[92:95]\n"
"s_waitcnt vmcnt(4) lgkmcnt(0)\n"
"s_barrier\n"
"v_add_u32 v6, s75, v0\n"
"ds_read_b128 a[212:215], v10 offset:512\n"
"v_mfma_f32_16x16x32_bf16 v[128:131], a[176:179], a[192:195], v[128:131]\n"
"ds_read_b64_tr_b16 a[160:161], v6 offset:0\n"
"ds_read_b64_tr_b16 a[162:163], v6 offset:256\n"
"v_mfma_f32_16x16x32_bf16 v[132:135], a[176:179], a[196:199], v[132:135]\n"
"ds_read_b128 a[216:219], v10 offset:2560\n"
"s_add_u32 m0, s97, 0x4000\n"
"s_add_u32 s72, s98, 0x2000\n"
"v_mfma_f32_16x16x32_bf16 v[192:195], a[176:179], a[200:203], v[192:195]\n"
"ds_read_b128 a[220:223], v10 offset:4608\n"
"buffer_load_dwordx4 v245, s[32:35], s72 offen nt lds\n"
"v_mfma_f32_16x16x32_bf16 v[196:199], a[176:179], a[204:207], v[196:199]\n"
"ds_read_b128 a[224:227], v10 offset:6656\n"
"buffer_load_dwordx4 v246, s[32:35], s72 offen offset:2048 nt lds\n"
"v_add_u32 v7, s75, v1\n"
"v_mfma_f32_16x16x32_bf16 v[136:139], a[180:183], a[192:195], v[136:139]\n"
"ds_read_b128 a[228:231], v11 offset:512\n"
"v_mfma_f32_16x16x32_bf16 v[140:143], a[180:183], a[196:199], v[140:143]\n"
"ds_read_b64_tr_b16 a[164:165], v7 offset:0\n"
"v_mfma_f32_16x16x32_bf16 v[200:203], a[180:183], a[200:203], v[200:203]\n"
"ds_read_b64_tr_b16 a[166:167], v7 offset:256\n"
"v_mfma_f32_16x16x32_bf16 v[204:207], a[180:183], a[204:207], v[204:207]\n"
"ds_read_b64_tr_b16 a[168:169], v6 offset:512\n"
"v_add_u32 v8, s75, v2\n"
"v_mfma_f32_16x16x32_bf16 v[144:147], a[184:187], a[192:195], v[144:147]\n"
"ds_read_b64_tr_b16 a[170:171], v6 offset:768\n"
"s_add_u32 m0, s97, 0x4400\n"
"s_add_u32 s72, s98, 0x2080\n"
"v_add_u32 v9, s75, v3\n"
"v_mfma_f32_16x16x32_bf16 v[148:151], a[184:187], a[196:199], v[148:151]\n"
"ds_read_b64_tr_b16 a[172:173], v7 offset:512\n"
"buffer_load_dwordx4 v245, s[32:35], s72 offen nt lds\n"
"v_mfma_f32_16x16x32_bf16 v[208:211], a[184:187], a[200:203], v[208:211]\n"
"ds_read_b64_tr_b16 a[174:175], v7 offset:768\n"
"buffer_load_dwordx4 v246, s[32:35], s72 offen offset:2048 nt lds\n"
"v_mfma_f32_16x16x32_bf16 v[212:215], a[184:187], a[204:207], v[212:215]\n"
"ds_read_b64_tr_b16 a[176:177], v8 offset:0\n"
"v_mfma_f32_16x16x32_bf16 v[152:155], a[188:191], a[192:195], v[152:155]\n"
"ds_read_b64_tr_b16 a[178:179], v8 offset:256\n"
"v_mfma_f32_16x16x32_bf16 v[156:159], a[188:191], a[196:199], v[156:159]\n"
"s_waitcnt lgkmcnt(10)\n"
"ds_read_b64_tr_b16 a[180:181], v9 offset:0\n"
"v_mfma_f32_16x16x32_bf16 v[216:219], a[188:191], a[200:203], v[216:219]\n"
"ds_read_b64_tr_b16 a[182:183], v9 offset:256\n"
"v_mfma_f32_16x16x32_bf16 v[220:223], a[188:191], a[204:207], v[220:223]\n"
"v_mfma_f32_16x16x32_bf16 v[96:99], a[160:163], a[212:215], v[96:99]\n"
"ds_read_b64_tr_b16 a[184:185], v8 offset:512\n"
"v_mfma_f32_16x16x32_bf16 v[100:103], a[160:163], a[216:219], v[100:103]\n"
"ds_read_b64_tr_b16 a[186:187], v8 offset:768\n"
"v_mfma_f32_16x16x32_bf16 v[160:163], a[160:163], a[220:223], v[160:163]\n"
"ds_read_b64_tr_b16 a[188:189], v9 offset:512\n"
"s_waitcnt lgkmcnt(10)\n"
"v_mfma_f32_16x16x32_bf16 v[164:167], a[160:163], a[224:227], v[164:167]\n"
"ds_read_b64_tr_b16 a[190:191], v9 offset:768\n"
"v_mfma_f32_16x16x32_bf16 v[80:83], a[160:163], a[228:231], v[80:83]\n"
"v_mfma_f32_16x16x32_bf16 v[104:107], a[164:167], a[212:215], v[104:107]\n"
"v_mfma_f32_16x16x32_bf16 v[108:111], a[164:167], a[216:219], v[108:111]\n"
"v_mfma_f32_16x16x32_bf16 v[168:171], a[164:167], a[220:223], v[168:171]\n"
"v_mfma_f32_16x16x32_bf16 v[172:175], a[164:167], a[224:227], v[172:175]\n"
"v_mfma_f32_16x16x32_bf16 v[84:87], a[164:167], a[228:231], v[84:87]\n"
"s_waitcnt lgkmcnt(5)\n"
"v_mfma_f32_16x16x32_bf16 v[112:115], a[168:171], a[212:215], v[112:115]\n"
"v_mfma_f32_16x16x32_bf16 v[116:119], a[168:171], a[216:219], v[116:119]\n"
"v_mfma_f32_16x16x32_bf16 v[176:179], a[168:171], a[220:223], v[176:179]\n"
"v_mfma_f32_16x16x32_bf16 v[180:183], a[168:171], a[224:227], v[180:183]\n"
"v_mfma_f32_16x16x32_bf16 v[88:91], a[168:171], a[228:231], v[88:91]\n"
"v_mfma_f32_16x16x32_bf16 v[120:123], a[172:175], a[212:215], v[120:123]\n"
"v_mfma_f32_16x16x32_bf16 v[124:127], a[172:175], a[216:219], v[124:127]\n"
"v_mfma_f32_16x16x32_bf16 v[184:187], a[172:175], a[220:223], v[184:187]\n"
"v_mfma_f32_16x16x32_bf16 v[188:191], a[172:175], a[224:227], v[188:191]\n"
"v_mfma_f32_16x16x32_bf16 v[92:95], a[172:175], a[228:231], v[92:95]\n"
"s_waitcnt lgkmcnt(0)\n"
"s_barrier\n"
"v_mfma_f32_16x16x32_bf16 v[128:131], a[176:179], a[212:215], v[128:131]\n"
"v_mfma_f32_16x16x32_bf16 v[132:135], a[176:179], a[216:219], v[132:135]\n"
"v_mfma_f32_16x16x32_bf16 v[192:195], a[176:179], a[220:223], v[192:195]\n"
"v_mfma_f32_16x16x32_bf16 v[196:199], a[176:179], a[224:227], v[196:199]\n"
"v_mfma_f32_16x16x32_bf16 v[136:139], a[180:183], a[212:215], v[136:139]\n"
"v_mfma_f32_16x16x32_bf16 v[140:143], a[180:183], a[216:219], v[140:143]\n"
"v_mfma_f32_16x16x32_bf16 v[200:203], a[180:183], a[220:223], v[200:203]\n"
"v_mfma_f32_16x16x32_bf16 v[204:207], a[180:183], a[224:227], v[204:207]\n"
"v_mfma_f32_16x16x32_bf16 v[144:147], a[184:187], a[212:215], v[144:147]\n"
"v_mfma_f32_16x16x32_bf16 v[148:151], a[184:187], a[216:219], v[148:151]\n"
"s_mov_b32 s72, s74\n"
"s_mov_b32 s74, s76\n"
"v_mfma_f32_16x16x32_bf16 v[208:211], a[184:187], a[220:223], v[208:211]\n"
"s_mov_b32 s76, s72\n"
"s_mov_b32 s72, s75\n"
"v_mfma_f32_16x16x32_bf16 v[212:215], a[184:187], a[224:227], v[212:215]\n"
"s_mov_b32 s75, s99\n"
"s_mov_b32 s99, s72\n"
"v_mfma_f32_16x16x32_bf16 v[152:155], a[188:191], a[212:215], v[152:155]\n"
"s_mov_b32 s72, s77\n"
"s_mov_b32 s77, s78\n"
"s_mov_b32 s78, s96\n"
"v_mfma_f32_16x16x32_bf16 v[156:159], a[188:191], a[216:219], v[156:159]\n"
"s_mov_b32 s96, s72\n"
"v_mfma_f32_16x16x32_bf16 v[216:219], a[188:191], a[220:223], v[216:219]\n"
"v_mfma_f32_16x16x32_bf16 v[220:223], a[188:191], a[224:227], v[220:223]\n"
"Lqdone_%=:\n"
"s_add_u32 m0, s97, 0x10000\n"
"s_add_u32 s72, s98, 0x0\n"
"buffer_load_dwordx4 v245, s[36:39], s72 offen nt lds\n"
"buffer_load_dwordx4 v246, s[36:39], s72 offen offset:2048 nt lds\n"
"s_add_u32 m0, s97, 0x10400\n"
"s_add_u32 s72, s98, 0x80\n"
"buffer_load_dwordx4 v245, s[36:39], s72 offen nt lds\n"
"buffer_load_dwordx4 v246, s[36:39], s72 offen offset:2048 nt lds\n"
"s_add_u32 m0, s97, 0x8000\n"
"s_add_u32 s72, s98, 0x4000\n"
"buffer_load_dwordx4 v245, s[32:35], s72 offen nt lds\n"
"buffer_load_dwordx4 v246, s[32:35], s72 offen offset:2048 nt lds\n"
"s_add_u32 m0, s97, 0x8400\n"
"s_add_u32 s72, s98, 0x4080\n"
"buffer_load_dwordx4 v245, s[32:35], s72 offen nt lds\n"
"buffer_load_dwordx4 v246, s[32:35], s72 offen offset:2048 nt lds\n"
"s_add_u32 m0, s97, 0xc000\n"
"s_add_u32 s72, s98, 0x6000\n"
"buffer_load_dwordx4 v245, s[32:35], s72 offen nt lds\n"
"buffer_load_dwordx4 v246, s[32:35], s72 offen offset:2048 nt lds\n"
"s_add_u32 m0, s97, 0xc400\n"
"s_add_u32 s72, s98, 0x6080\n"
"buffer_load_dwordx4 v245, s[32:35], s72 offen nt lds\n"
"buffer_load_dwordx4 v246, s[32:35], s72 offen offset:2048 nt lds\n"
"s_add_u32 m0, s97, 0x14000\n"
"s_add_u32 s72, s98, 0x2000\n"
"buffer_load_dwordx4 v245, s[36:39], s72 offen nt lds\n"
"buffer_load_dwordx4 v246, s[36:39], s72 offen offset:2048 nt lds\n"
"s_add_u32 m0, s97, 0x14400\n"
"s_add_u32 s72, s98, 0x2080\n"
"buffer_load_dwordx4 v245, s[36:39], s72 offen nt lds\n"
"buffer_load_dwordx4 v246, s[36:39], s72 offen offset:2048 nt lds\n"
"s_add_u32 s84, s98, 32768\n"
"s_add_u32 s85, s98, 16384\n"
"s_add_u32 s86, s97, 0x0\n"
"s_add_u32 s87, s97, 0x18000\n"
"s_mov_b32 s88, 0x49800000\n"
"s_mov_b32 s82, 0x3e0293ee\n"
"s_mov_b32 s83, 0x3e0293ee\n"
"v_mov_b32 v228, s16\n"
"v_mul_u32_u24 v228, 0x1000, v228\n"
"v_add_u32 v228, 0x26000, v228\n"
"v_and_b32 v237, 0xf, v247\n"
"v_lshlrev_b32 v237, 8, v237\n"
"v_add_u32 v228, v237, v228\n"
"v_mov_b32 v237, s17\n"
"v_mul_u32_u24 v237, 0x8, v237\n"
"v_lshrrev_b32 v236, 4, v247\n"
"v_lshrrev_b32 v236, 1, v236\n"
"v_add_u32 v237, v236, v237\n"
"v_and_b32 v236, 0xf, v247\n"
"v_xor_b32 v237, v236, v237\n"
"v_lshlrev_b32 v237, 4, v237\n"
"v_add_u32 v228, v237, v228\n"
"v_lshrrev_b32 v237, 4, v247\n"
"v_and_b32 v237, 0x1, v237\n"
"v_lshlrev_b32 v237, 3, v237\n"
"v_add_u32 v228, v237, v228\n"
"v_mov_b32 v229, s16\n"
"v_mul_u32_u24 v229, 0x1000, v229\n"
"v_add_u32 v229, 0x26000, v229\n"
"v_and_b32 v237, 0xf, v247\n"
"v_lshlrev_b32 v237, 8, v237\n"
"v_add_u32 v229, v237, v229\n"
"v_lshrrev_b32 v237, 4, v247\n"
"v_and_b32 v236, 0xf, v247\n"
"v_xor_b32 v237, v236, v237\n"
"v_lshlrev_b32 v237, 4, v237\n"
"v_add_u32 v229, v237, v229\n"
"v_pk_mul_f32 v[80:81], v[80:81], s[82:83]\n"
"v_pk_mul_f32 v[82:83], v[82:83], s[82:83]\n"
"v_pk_mul_f32 v[84:85], v[84:85], s[82:83]\n"
"v_pk_mul_f32 v[86:87], v[86:87], s[82:83]\n"
"v_pk_mul_f32 v[88:89], v[88:89], s[82:83]\n"
"v_pk_mul_f32 v[90:91], v[90:91], s[82:83]\n"
"v_pk_mul_f32 v[92:93], v[92:93], s[82:83]\n"
"v_pk_mul_f32 v[94:95], v[94:95], s[82:83]\n"
"v_cvt_pk_bf16_f32 v224, v80, v81\n"
"v_cvt_pk_bf16_f32 v225, v82, v83\n"
"ds_write_b64 v228, v[224:225]\n"
"s_nop 0\n"
"v_cvt_pk_bf16_f32 v224, v84, v85\n"
"v_cvt_pk_bf16_f32 v225, v86, v87\n"
"v_xor_b32 v230, 32, v228\n"
"ds_write_b64 v230, v[224:225]\n"
"s_nop 0\n"
"v_cvt_pk_bf16_f32 v224, v88, v89\n"
"v_cvt_pk_bf16_f32 v225, v90, v91\n"
"v_xor_b32 v230, 64, v228\n"
"ds_write_b64 v230, v[224:225]\n"
"s_nop 0\n"
"v_cvt_pk_bf16_f32 v224, v92, v93\n"
"v_cvt_pk_bf16_f32 v225, v94, v95\n"
"v_xor_b32 v230, 96, v228\n"
"ds_write_b64 v230, v[224:225]\n"
"s_nop 0\n"
"s_cmp_eq_u32 s17, 0\n"
"s_cbranch_scc1 Lqe0_%=\n"
"v_pk_mul_f32 v[96:97], v[96:97], s[82:83]\n"
"v_pk_mul_f32 v[98:99], v[98:99], s[82:83]\n"
"v_pk_mul_f32 v[100:101], v[100:101], s[82:83]\n"
"v_pk_mul_f32 v[102:103], v[102:103], s[82:83]\n"
"v_cvt_pk_bf16_f32 v16, v96, v97\n"
"v_cvt_pk_bf16_f32 v17, v98, v99\n"
"v_cvt_pk_bf16_f32 v18, v100, v101\n"
"v_cvt_pk_bf16_f32 v19, v102, v103\n"
"v_pk_mul_f32 v[104:105], v[104:105], s[82:83]\n"
"v_pk_mul_f32 v[106:107], v[106:107], s[82:83]\n"
"v_pk_mul_f32 v[108:109], v[108:109], s[82:83]\n"
"v_pk_mul_f32 v[110:111], v[110:111], s[82:83]\n"
"v_cvt_pk_bf16_f32 v20, v104, v105\n"
"v_cvt_pk_bf16_f32 v21, v106, v107\n"
"v_cvt_pk_bf16_f32 v22, v108, v109\n"
"v_cvt_pk_bf16_f32 v23, v110, v111\n"
"v_pk_mul_f32 v[112:113], v[112:113], s[82:83]\n"
"v_pk_mul_f32 v[114:115], v[114:115], s[82:83]\n"
"v_pk_mul_f32 v[116:117], v[116:117], s[82:83]\n"
"v_pk_mul_f32 v[118:119], v[118:119], s[82:83]\n"
"v_cvt_pk_bf16_f32 v24, v112, v113\n"
"v_cvt_pk_bf16_f32 v25, v114, v115\n"
"v_cvt_pk_bf16_f32 v26, v116, v117\n"
"v_cvt_pk_bf16_f32 v27, v118, v119\n"
"v_pk_mul_f32 v[120:121], v[120:121], s[82:83]\n"
"v_pk_mul_f32 v[122:123], v[122:123], s[82:83]\n"
"v_pk_mul_f32 v[124:125], v[124:125], s[82:83]\n"
"v_pk_mul_f32 v[126:127], v[126:127], s[82:83]\n"
"v_cvt_pk_bf16_f32 v28, v120, v121\n"
"v_cvt_pk_bf16_f32 v29, v122, v123\n"
"v_cvt_pk_bf16_f32 v30, v124, v125\n"
"v_cvt_pk_bf16_f32 v31, v126, v127\n"
"v_pk_mul_f32 v[128:129], v[128:129], s[82:83]\n"
"v_pk_mul_f32 v[130:131], v[130:131], s[82:83]\n"
"v_pk_mul_f32 v[132:133], v[132:133], s[82:83]\n"
"v_pk_mul_f32 v[134:135], v[134:135], s[82:83]\n"
"v_cvt_pk_bf16_f32 v0, v128, v129\n"
"v_cvt_pk_bf16_f32 v1, v130, v131\n"
"v_cvt_pk_bf16_f32 v2, v132, v133\n"
"v_cvt_pk_bf16_f32 v3, v134, v135\n"
"v_pk_mul_f32 v[136:137], v[136:137], s[82:83]\n"
"v_pk_mul_f32 v[138:139], v[138:139], s[82:83]\n"
"v_pk_mul_f32 v[140:141], v[140:141], s[82:83]\n"
"v_pk_mul_f32 v[142:143], v[142:143], s[82:83]\n"
"v_cvt_pk_bf16_f32 v4, v136, v137\n"
"v_cvt_pk_bf16_f32 v5, v138, v139\n"
"v_cvt_pk_bf16_f32 v6, v140, v141\n"
"v_cvt_pk_bf16_f32 v7, v142, v143\n"
"v_pk_mul_f32 v[144:145], v[144:145], s[82:83]\n"
"v_pk_mul_f32 v[146:147], v[146:147], s[82:83]\n"
"v_pk_mul_f32 v[148:149], v[148:149], s[82:83]\n"
"v_pk_mul_f32 v[150:151], v[150:151], s[82:83]\n"
"v_cvt_pk_bf16_f32 v8, v144, v145\n"
"v_cvt_pk_bf16_f32 v9, v146, v147\n"
"v_cvt_pk_bf16_f32 v10, v148, v149\n"
"v_cvt_pk_bf16_f32 v11, v150, v151\n"
"v_pk_mul_f32 v[152:153], v[152:153], s[82:83]\n"
"v_pk_mul_f32 v[154:155], v[154:155], s[82:83]\n"
"v_pk_mul_f32 v[156:157], v[156:157], s[82:83]\n"
"v_pk_mul_f32 v[158:159], v[158:159], s[82:83]\n"
"v_cvt_pk_bf16_f32 v12, v152, v153\n"
"v_cvt_pk_bf16_f32 v13, v154, v155\n"
"v_cvt_pk_bf16_f32 v14, v156, v157\n"
"v_cvt_pk_bf16_f32 v15, v158, v159\n"
"v_pk_mul_f32 v[160:161], v[160:161], s[82:83]\n"
"v_pk_mul_f32 v[162:163], v[162:163], s[82:83]\n"
"v_pk_mul_f32 v[164:165], v[164:165], s[82:83]\n"
"v_pk_mul_f32 v[166:167], v[166:167], s[82:83]\n"
"v_cvt_pk_bf16_f32 v48, v160, v161\n"
"v_cvt_pk_bf16_f32 v49, v162, v163\n"
"v_cvt_pk_bf16_f32 v50, v164, v165\n"
"v_cvt_pk_bf16_f32 v51, v166, v167\n"
"v_pk_mul_f32 v[168:169], v[168:169], s[82:83]\n"
"v_pk_mul_f32 v[170:171], v[170:171], s[82:83]\n"
"v_pk_mul_f32 v[172:173], v[172:173], s[82:83]\n"
"v_pk_mul_f32 v[174:175], v[174:175], s[82:83]\n"
"v_cvt_pk_bf16_f32 v52, v168, v169\n"
"v_cvt_pk_bf16_f32 v53, v170, v171\n"
"v_cvt_pk_bf16_f32 v54, v172, v173\n"
"v_cvt_pk_bf16_f32 v55, v174, v175\n"
"v_pk_mul_f32 v[176:177], v[176:177], s[82:83]\n"
"v_pk_mul_f32 v[178:179], v[178:179], s[82:83]\n"
"v_pk_mul_f32 v[180:181], v[180:181], s[82:83]\n"
"v_pk_mul_f32 v[182:183], v[182:183], s[82:83]\n"
"v_cvt_pk_bf16_f32 v56, v176, v177\n"
"v_cvt_pk_bf16_f32 v57, v178, v179\n"
"v_cvt_pk_bf16_f32 v58, v180, v181\n"
"v_cvt_pk_bf16_f32 v59, v182, v183\n"
"v_pk_mul_f32 v[184:185], v[184:185], s[82:83]\n"
"v_pk_mul_f32 v[186:187], v[186:187], s[82:83]\n"
"v_pk_mul_f32 v[188:189], v[188:189], s[82:83]\n"
"v_pk_mul_f32 v[190:191], v[190:191], s[82:83]\n"
"v_cvt_pk_bf16_f32 v60, v184, v185\n"
"v_cvt_pk_bf16_f32 v61, v186, v187\n"
"v_cvt_pk_bf16_f32 v62, v188, v189\n"
"v_cvt_pk_bf16_f32 v63, v190, v191\n"
"v_pk_mul_f32 v[192:193], v[192:193], s[82:83]\n"
"v_pk_mul_f32 v[194:195], v[194:195], s[82:83]\n"
"v_pk_mul_f32 v[196:197], v[196:197], s[82:83]\n"
"v_pk_mul_f32 v[198:199], v[198:199], s[82:83]\n"
"v_cvt_pk_bf16_f32 v32, v192, v193\n"
"v_cvt_pk_bf16_f32 v33, v194, v195\n"
"v_cvt_pk_bf16_f32 v34, v196, v197\n"
"v_cvt_pk_bf16_f32 v35, v198, v199\n"
"v_pk_mul_f32 v[200:201], v[200:201], s[82:83]\n"
"v_pk_mul_f32 v[202:203], v[202:203], s[82:83]\n"
"v_pk_mul_f32 v[204:205], v[204:205], s[82:83]\n"
"v_pk_mul_f32 v[206:207], v[206:207], s[82:83]\n"
"v_cvt_pk_bf16_f32 v36, v200, v201\n"
"v_cvt_pk_bf16_f32 v37, v202, v203\n"
"v_cvt_pk_bf16_f32 v38, v204, v205\n"
"v_cvt_pk_bf16_f32 v39, v206, v207\n"
"v_pk_mul_f32 v[208:209], v[208:209], s[82:83]\n"
"v_pk_mul_f32 v[210:211], v[210:211], s[82:83]\n"
"v_pk_mul_f32 v[212:213], v[212:213], s[82:83]\n"
"v_pk_mul_f32 v[214:215], v[214:215], s[82:83]\n"
"v_cvt_pk_bf16_f32 v40, v208, v209\n"
"v_cvt_pk_bf16_f32 v41, v210, v211\n"
"v_cvt_pk_bf16_f32 v42, v212, v213\n"
"v_cvt_pk_bf16_f32 v43, v214, v215\n"
"v_pk_mul_f32 v[216:217], v[216:217], s[82:83]\n"
"v_pk_mul_f32 v[218:219], v[218:219], s[82:83]\n"
"v_pk_mul_f32 v[220:221], v[220:221], s[82:83]\n"
"v_pk_mul_f32 v[222:223], v[222:223], s[82:83]\n"
"v_cvt_pk_bf16_f32 v44, v216, v217\n"
"v_cvt_pk_bf16_f32 v45, v218, v219\n"
"v_cvt_pk_bf16_f32 v46, v220, v221\n"
"v_cvt_pk_bf16_f32 v47, v222, v223\n"
"s_branch Lqe1_%=\n"
"Lqe0_%=:\n"
"v_pk_mul_f32 v[96:97], v[96:97], s[82:83]\n"
"v_pk_mul_f32 v[98:99], v[98:99], s[82:83]\n"
"v_pk_mul_f32 v[100:101], v[100:101], s[82:83]\n"
"v_pk_mul_f32 v[102:103], v[102:103], s[82:83]\n"
"v_cvt_pk_bf16_f32 v0, v96, v97\n"
"v_cvt_pk_bf16_f32 v1, v98, v99\n"
"v_cvt_pk_bf16_f32 v2, v100, v101\n"
"v_cvt_pk_bf16_f32 v3, v102, v103\n"
"v_pk_mul_f32 v[104:105], v[104:105], s[82:83]\n"
"v_pk_mul_f32 v[106:107], v[106:107], s[82:83]\n"
"v_pk_mul_f32 v[108:109], v[108:109], s[82:83]\n"
"v_pk_mul_f32 v[110:111], v[110:111], s[82:83]\n"
"v_cvt_pk_bf16_f32 v4, v104, v105\n"
"v_cvt_pk_bf16_f32 v5, v106, v107\n"
"v_cvt_pk_bf16_f32 v6, v108, v109\n"
"v_cvt_pk_bf16_f32 v7, v110, v111\n"
"v_pk_mul_f32 v[112:113], v[112:113], s[82:83]\n"
"v_pk_mul_f32 v[114:115], v[114:115], s[82:83]\n"
"v_pk_mul_f32 v[116:117], v[116:117], s[82:83]\n"
"v_pk_mul_f32 v[118:119], v[118:119], s[82:83]\n"
"v_cvt_pk_bf16_f32 v8, v112, v113\n"
"v_cvt_pk_bf16_f32 v9, v114, v115\n"
"v_cvt_pk_bf16_f32 v10, v116, v117\n"
"v_cvt_pk_bf16_f32 v11, v118, v119\n"
"v_pk_mul_f32 v[120:121], v[120:121], s[82:83]\n"
"v_pk_mul_f32 v[122:123], v[122:123], s[82:83]\n"
"v_pk_mul_f32 v[124:125], v[124:125], s[82:83]\n"
"v_pk_mul_f32 v[126:127], v[126:127], s[82:83]\n"
"v_cvt_pk_bf16_f32 v12, v120, v121\n"
"v_cvt_pk_bf16_f32 v13, v122, v123\n"
"v_cvt_pk_bf16_f32 v14, v124, v125\n"
"v_cvt_pk_bf16_f32 v15, v126, v127\n"
"v_pk_mul_f32 v[128:129], v[128:129], s[82:83]\n"
"v_pk_mul_f32 v[130:131], v[130:131], s[82:83]\n"
"v_pk_mul_f32 v[132:133], v[132:133], s[82:83]\n"
"v_pk_mul_f32 v[134:135], v[134:135], s[82:83]\n"
"v_cvt_pk_bf16_f32 v16, v128, v129\n"
"v_cvt_pk_bf16_f32 v17, v130, v131\n"
"v_cvt_pk_bf16_f32 v18, v132, v133\n"
"v_cvt_pk_bf16_f32 v19, v134, v135\n"
"v_pk_mul_f32 v[136:137], v[136:137], s[82:83]\n"
"v_pk_mul_f32 v[138:139], v[138:139], s[82:83]\n"
"v_pk_mul_f32 v[140:141], v[140:141], s[82:83]\n"
"v_pk_mul_f32 v[142:143], v[142:143], s[82:83]\n"
"v_cvt_pk_bf16_f32 v20, v136, v137\n"
"v_cvt_pk_bf16_f32 v21, v138, v139\n"
"v_cvt_pk_bf16_f32 v22, v140, v141\n"
"v_cvt_pk_bf16_f32 v23, v142, v143\n"
"v_pk_mul_f32 v[144:145], v[144:145], s[82:83]\n"
"v_pk_mul_f32 v[146:147], v[146:147], s[82:83]\n"
"v_pk_mul_f32 v[148:149], v[148:149], s[82:83]\n"
"v_pk_mul_f32 v[150:151], v[150:151], s[82:83]\n"
"v_cvt_pk_bf16_f32 v24, v144, v145\n"
"v_cvt_pk_bf16_f32 v25, v146, v147\n"
"v_cvt_pk_bf16_f32 v26, v148, v149\n"
"v_cvt_pk_bf16_f32 v27, v150, v151\n"
"v_pk_mul_f32 v[152:153], v[152:153], s[82:83]\n"
"v_pk_mul_f32 v[154:155], v[154:155], s[82:83]\n"
"v_pk_mul_f32 v[156:157], v[156:157], s[82:83]\n"
"v_pk_mul_f32 v[158:159], v[158:159], s[82:83]\n"
"v_cvt_pk_bf16_f32 v28, v152, v153\n"
"v_cvt_pk_bf16_f32 v29, v154, v155\n"
"v_cvt_pk_bf16_f32 v30, v156, v157\n"
"v_cvt_pk_bf16_f32 v31, v158, v159\n"
"v_pk_mul_f32 v[160:161], v[160:161], s[82:83]\n"
"v_pk_mul_f32 v[162:163], v[162:163], s[82:83]\n"
"v_pk_mul_f32 v[164:165], v[164:165], s[82:83]\n"
"v_pk_mul_f32 v[166:167], v[166:167], s[82:83]\n"
"v_cvt_pk_bf16_f32 v32, v160, v161\n"
"v_cvt_pk_bf16_f32 v33, v162, v163\n"
"v_cvt_pk_bf16_f32 v34, v164, v165\n"
"v_cvt_pk_bf16_f32 v35, v166, v167\n"
"v_pk_mul_f32 v[168:169], v[168:169], s[82:83]\n"
"v_pk_mul_f32 v[170:171], v[170:171], s[82:83]\n"
"v_pk_mul_f32 v[172:173], v[172:173], s[82:83]\n"
"v_pk_mul_f32 v[174:175], v[174:175], s[82:83]\n"
"v_cvt_pk_bf16_f32 v36, v168, v169\n"
"v_cvt_pk_bf16_f32 v37, v170, v171\n"
"v_cvt_pk_bf16_f32 v38, v172, v173\n"
"v_cvt_pk_bf16_f32 v39, v174, v175\n"
"v_pk_mul_f32 v[176:177], v[176:177], s[82:83]\n"
"v_pk_mul_f32 v[178:179], v[178:179], s[82:83]\n"
"v_pk_mul_f32 v[180:181], v[180:181], s[82:83]\n"
"v_pk_mul_f32 v[182:183], v[182:183], s[82:83]\n"
"v_cvt_pk_bf16_f32 v40, v176, v177\n"
"v_cvt_pk_bf16_f32 v41, v178, v179\n"
"v_cvt_pk_bf16_f32 v42, v180, v181\n"
"v_cvt_pk_bf16_f32 v43, v182, v183\n"
"v_pk_mul_f32 v[184:185], v[184:185], s[82:83]\n"
"v_pk_mul_f32 v[186:187], v[186:187], s[82:83]\n"
"v_pk_mul_f32 v[188:189], v[188:189], s[82:83]\n"
"v_pk_mul_f32 v[190:191], v[190:191], s[82:83]\n"
"v_cvt_pk_bf16_f32 v44, v184, v185\n"
"v_cvt_pk_bf16_f32 v45, v186, v187\n"
"v_cvt_pk_bf16_f32 v46, v188, v189\n"
"v_cvt_pk_bf16_f32 v47, v190, v191\n"
"v_pk_mul_f32 v[192:193], v[192:193], s[82:83]\n"
"v_pk_mul_f32 v[194:195], v[194:195], s[82:83]\n"
"v_pk_mul_f32 v[196:197], v[196:197], s[82:83]\n"
"v_pk_mul_f32 v[198:199], v[198:199], s[82:83]\n"
"v_cvt_pk_bf16_f32 v48, v192, v193\n"
"v_cvt_pk_bf16_f32 v49, v194, v195\n"
"v_cvt_pk_bf16_f32 v50, v196, v197\n"
"v_cvt_pk_bf16_f32 v51, v198, v199\n"
"v_pk_mul_f32 v[200:201], v[200:201], s[82:83]\n"
"v_pk_mul_f32 v[202:203], v[202:203], s[82:83]\n"
"v_pk_mul_f32 v[204:205], v[204:205], s[82:83]\n"
"v_pk_mul_f32 v[206:207], v[206:207], s[82:83]\n"
"v_cvt_pk_bf16_f32 v52, v200, v201\n"
"v_cvt_pk_bf16_f32 v53, v202, v203\n"
"v_cvt_pk_bf16_f32 v54, v204, v205\n"
"v_cvt_pk_bf16_f32 v55, v206, v207\n"
"v_pk_mul_f32 v[208:209], v[208:209], s[82:83]\n"
"v_pk_mul_f32 v[210:211], v[210:211], s[82:83]\n"
"v_pk_mul_f32 v[212:213], v[212:213], s[82:83]\n"
"v_pk_mul_f32 v[214:215], v[214:215], s[82:83]\n"
"v_cvt_pk_bf16_f32 v56, v208, v209\n"
"v_cvt_pk_bf16_f32 v57, v210, v211\n"
"v_cvt_pk_bf16_f32 v58, v212, v213\n"
"v_cvt_pk_bf16_f32 v59, v214, v215\n"
"v_pk_mul_f32 v[216:217], v[216:217], s[82:83]\n"
"v_pk_mul_f32 v[218:219], v[218:219], s[82:83]\n"
"v_pk_mul_f32 v[220:221], v[220:221], s[82:83]\n"
"v_pk_mul_f32 v[222:223], v[222:223], s[82:83]\n"
"v_cvt_pk_bf16_f32 v60, v216, v217\n"
"v_cvt_pk_bf16_f32 v61, v218, v219\n"
"v_cvt_pk_bf16_f32 v62, v220, v221\n"
"v_cvt_pk_bf16_f32 v63, v222, v223\n"
"Lqe1_%=:\n"
"s_nop 1\n"
"v_permlane16_swap_b32 v0, v2\n"
"v_permlane16_swap_b32 v1, v3\n"
"v_permlane16_swap_b32 v4, v6\n"
"v_permlane16_swap_b32 v5, v7\n"
"v_permlane16_swap_b32 v8, v10\n"
"v_permlane16_swap_b32 v9, v11\n"
"v_permlane16_swap_b32 v12, v14\n"
"v_permlane16_swap_b32 v13, v15\n"
"v_permlane16_swap_b32 v16, v18\n"
"v_permlane16_swap_b32 v17, v19\n"
"v_permlane16_swap_b32 v20, v22\n"
"v_permlane16_swap_b32 v21, v23\n"
"v_permlane16_swap_b32 v24, v26\n"
"v_permlane16_swap_b32 v25, v27\n"
"v_permlane16_swap_b32 v28, v30\n"
"v_permlane16_swap_b32 v29, v31\n"
"v_permlane16_swap_b32 v32, v34\n"
"v_permlane16_swap_b32 v33, v35\n"
"v_permlane16_swap_b32 v36, v38\n"
"v_permlane16_swap_b32 v37, v39\n"
"v_permlane16_swap_b32 v40, v42\n"
"v_permlane16_swap_b32 v41, v43\n"
"v_permlane16_swap_b32 v44, v46\n"
"v_permlane16_swap_b32 v45, v47\n"
"v_permlane16_swap_b32 v48, v50\n"
"v_permlane16_swap_b32 v49, v51\n"
"v_permlane16_swap_b32 v52, v54\n"
"v_permlane16_swap_b32 v53, v55\n"
"v_permlane16_swap_b32 v56, v58\n"
"v_permlane16_swap_b32 v57, v59\n"
"v_permlane16_swap_b32 v60, v62\n"
"v_permlane16_swap_b32 v61, v63\n"
"s_waitcnt vmcnt(16) lgkmcnt(0)\n"
"s_barrier\n"
"ds_read_b128 v[64:67], v229\n"
"v_xor_b32 v230, 64, v229\n"
"ds_read_b128 v[68:71], v230\n"
"v_xor_b32 v230, 128, v229\n"
"ds_read_b128 v[72:75], v230\n"
"v_xor_b32 v230, 192, v229\n"
"ds_read_b128 v[76:79], v230\n"
"s_waitcnt lgkmcnt(0)\n"
"ds_read_b128 a[160:163], v238 offset:0\n"
"ds_read_b128 a[164:167], v239 offset:0\n"
"ds_read_b128 a[168:171], v238 offset:512\n"
"ds_read_b128 a[172:175], v239 offset:512\n"
"ds_read_b128 a[176:179], v238 offset:1024\n"
"ds_read_b128 a[180:183], v239 offset:1024\n"
"ds_read_b128 a[184:187], v238 offset:1536\n"
"ds_read_b128 a[188:191], v239 offset:1536\n"
"ds_read_b128 a[224:227], v240 offset:0\n"
"ds_read_b128 a[228:231], v240 offset:512\n"
"ds_read_b128 a[232:235], v240 offset:1024\n"
"ds_read_b128 a[236:239], v240 offset:1536\n"
"s_waitcnt lgkmcnt(11)\n"
"v_mfma_f32_32x32x16_bf16 v[80:95], a[160:163], v[0:3], 0\n"
"v_xor_b32 v238, 0x4000, v238\n"
"v_xor_b32 v239, 0x4000, v239\n"
"v_xor_b32 v240, 0x4000, v240\n"
"v_mfma_f32_32x32x16_bf16 v[96:111], a[160:163], v[32:35], 0\n"
"ds_read_b128 a[160:163], v238 offset:0\n"
"s_waitcnt lgkmcnt(11)\n"
"v_mfma_f32_32x32x16_bf16 v[80:95], a[164:167], v[4:7], v[80:95]\n"
"v_mfma_f32_32x32x16_bf16 v[96:111], a[164:167], v[36:39], v[96:111]\n"
"ds_read_b128 a[164:167], v239 offset:0\n"
"s_waitcnt lgkmcnt(11)\n"
"v_mfma_f32_32x32x16_bf16 v[80:95], a[168:171], v[8:11], v[80:95]\n"
"v_mfma_f32_32x32x16_bf16 v[96:111], a[168:171], v[40:43], v[96:111]\n"
"ds_read_b128 a[168:171], v238 offset:512\n"
"s_waitcnt lgkmcnt(3)\n"
"v_mfma_f32_32x32x16_bf16 v[80:95], a[172:175], v[12:15], v[80:95]\n"
"v_mfma_f32_32x32x16_bf16 v[96:111], a[172:175], v[44:47], v[96:111]\n"
"ds_read_b128 a[172:175], v239 offset:512\n"
"v_mfma_f32_32x32x16_bf16 v[80:95], a[176:179], v[16:19], v[80:95]\n"
"v_mfma_f32_32x32x16_bf16 v[96:111], a[176:179], v[48:51], v[96:111]\n"
"ds_read_b128 a[176:179], v238 offset:1024\n"
"v_mfma_f32_32x32x16_bf16 v[80:95], a[180:183], v[20:23], v[80:95]\n"
"v_mfma_f32_32x32x16_bf16 v[96:111], a[180:183], v[52:55], v[96:111]\n"
"ds_read_b128 a[180:183], v239 offset:1024\n"
"v_mfma_f32_32x32x16_bf16 v[80:95], a[184:187], v[24:27], v[80:95]\n"
"v_mfma_f32_32x32x16_bf16 v[96:111], a[184:187], v[56:59], v[96:111]\n"
"ds_read_b128 a[184:187], v238 offset:1536\n"
"v_mfma_f32_32x32x16_bf16 v[80:95], a[188:191], v[28:31], v[80:95]\n"
"v_mfma_f32_32x32x16_bf16 v[96:111], a[188:191], v[60:63], v[96:111]\n"
"ds_read_b128 a[188:191], v239 offset:1536\n"
"v_mfma_f32_16x16x32_bf16 v[112:115], a[224:227], v[64:67], 0\n"
"v_mfma_f32_16x16x32_bf16 v[112:115], a[228:231], v[68:71], v[112:115]\n"
"s_waitcnt lgkmcnt(8)\n"
"s_barrier\n"
"v_mfma_f32_16x16x32_bf16 v[112:115], a[232:235], v[72:75], v[112:115]\n"
"v_mfma_f32_16x16x32_bf16 v[112:115], a[236:239], v[76:79], v[112:115]\n"
"s_nop 3\n"
"v_max3_f32 v252, v80, v81, v82\n"
"v_max3_f32 v252, v252, v83, v84\n"
"v_max3_f32 v252, v252, v85, v86\n"
"v_max3_f32 v252, v252, v87, v88\n"
"v_max3_f32 v252, v252, v89, v90\n"
"v_max3_f32 v252, v252, v91, v92\n"
"v_max3_f32 v252, v252, v93, v94\n"
"v_max_f32 v252, v252, v95\n"
"v_max3_f32 v253, v96, v97, v98\n"
"v_max3_f32 v253, v253, v99, v100\n"
"v_max3_f32 v253, v253, v101, v102\n"
"v_max3_f32 v253, v253, v103, v104\n"
"v_max3_f32 v253, v253, v105, v106\n"
"v_max3_f32 v253, v253, v107, v108\n"
"v_max3_f32 v253, v253, v109, v110\n"
"v_max_f32 v253, v253, v111\n"
"v_max3_f32 v254, v112, v113, v114\n"
"v_max_f32 v254, v254, v115\n"
"v_mov_b32 v224, v252\n"
"s_nop 1\n"
"v_permlane32_swap_b32 v252, v224\n"
"v_max_f32 v252, v252, v224\n"
"v_mov_b32 v224, v253\n"
"s_nop 1\n"
"v_permlane32_swap_b32 v253, v224\n"
"v_max_f32 v253, v253, v224\n"
"v_mov_b32 v224, v254\n"
"s_nop 1\n"
"v_permlane32_swap_b32 v254, v224\n"
"v_max_f32 v254, v254, v224\n"
"v_mov_b32 v224, v254\n"
"s_nop 1\n"
"v_permlane16_swap_b32 v254, v224\n"
"v_max_f32 v254, v254, v224\n"
"v_sub_f32 v252, 0, v252\n"
"v_sub_f32 v253, 0, v253\n"
"v_sub_f32 v254, 0, v254\n"
"v_mov_b32 v152, v252\n"
"v_mov_b32 v153, v252\n"
"v_mov_b32 v154, v252\n"
"v_mov_b32 v155, v252\n"
"v_mov_b32 v156, v252\n"
"v_mov_b32 v157, v252\n"
"v_mov_b32 v158, v252\n"
"v_mov_b32 v159, v252\n"
"v_mov_b32 v160, v252\n"
"v_mov_b32 v161, v252\n"
"v_mov_b32 v162, v252\n"
"v_mov_b32 v163, v252\n"
"v_mov_b32 v164, v252\n"
"v_mov_b32 v165, v252\n"
"v_mov_b32 v166, v252\n"
"v_mov_b32 v167, v252\n"
"v_mov_b32 v168, v253\n"
"v_mov_b32 v169, v253\n"
"v_mov_b32 v170, v253\n"
"v_mov_b32 v171, v253\n"
"v_mov_b32 v172, v253\n"
"v_mov_b32 v173, v253\n"
"v_mov_b32 v174, v253\n"
"v_mov_b32 v175, v253\n"
"v_mov_b32 v176, v253\n"
"v_mov_b32 v177, v253\n"
"v_mov_b32 v178, v253\n"
"v_mov_b32 v179, v253\n"
"v_mov_b32 v180, v253\n"
"v_mov_b32 v181, v253\n"
"v_mov_b32 v182, v253\n"
"v_mov_b32 v183, v253\n"
"v_mov_b32 v184, v254\n"
"v_mov_b32 v185, v254\n"
"v_mov_b32 v186, v254\n"
"v_mov_b32 v187, v254\n"
"v_add_f32 v80, v80, v252\n"
"v_add_f32 v81, v81, v252\n"
"v_add_f32 v82, v82, v252\n"
"v_add_f32 v83, v83, v252\n"
"v_add_f32 v84, v84, v252\n"
"v_add_f32 v85, v85, v252\n"
"v_add_f32 v86, v86, v252\n"
"v_add_f32 v87, v87, v252\n"
"v_add_f32 v88, v88, v252\n"
"v_add_f32 v89, v89, v252\n"
"v_add_f32 v90, v90, v252\n"
"v_add_f32 v91, v91, v252\n"
"v_add_f32 v92, v92, v252\n"
"v_add_f32 v93, v93, v252\n"
"v_add_f32 v94, v94, v252\n"
"v_add_f32 v95, v95, v252\n"
"v_add_f32 v96, v96, v253\n"
"v_add_f32 v97, v97, v253\n"
"v_add_f32 v98, v98, v253\n"
"v_add_f32 v99, v99, v253\n"
"v_add_f32 v100, v100, v253\n"
"v_add_f32 v101, v101, v253\n"
"v_add_f32 v102, v102, v253\n"
"v_add_f32 v103, v103, v253\n"
"v_add_f32 v104, v104, v253\n"
"v_add_f32 v105, v105, v253\n"
"v_add_f32 v106, v106, v253\n"
"v_add_f32 v107, v107, v253\n"
"v_add_f32 v108, v108, v253\n"
"v_add_f32 v109, v109, v253\n"
"v_add_f32 v110, v110, v253\n"
"v_add_f32 v111, v111, v253\n"
"v_add_f32 v112, v112, v254\n"
"v_add_f32 v113, v113, v254\n"
"v_add_f32 v114, v114, v254\n"
"v_add_f32 v115, v115, v254\n"
"v_mov_b32 v235, 0\n"
"v_mov_b32 v236, 0\n"
"v_mov_b32 v237, 0\n"
"v_mov_b32 v248, 0\n"
"v_mov_b32 v249, 0\n"
"v_mov_b32 v250, 0\n"
"v_mov_b32 v251, 0\n"
"s_waitcnt lgkmcnt(0)\n"
"v_mfma_f32_32x32x16_bf16 v[116:131], a[160:163], v[0:3], v[152:167]\n"
"ds_read_b128 a[224:227], v240 offset:0\n"
"s_waitcnt vmcnt(5)\n"
"s_add_u32 m0, s86, 0x0\n"
"s_add_u32 s72, s84, 0x0\n"
"buffer_load_dwordx4 v245, s[32:35], s72 offen nt lds\n"
"buffer_load_dwordx4 v246, s[32:35], s72 offen offset:2048 nt lds\n"
"v_exp_f32 v224, v80\n"
"v_exp_f32 v225, v81\n"
"v_exp_f32 v226, v82\n"
"v_mfma_f32_32x32x16_bf16 v[132:147], a[160:163], v[32:35], v[168:183]\n"
"ds_read_b128 a[228:231], v240 offset:512\n"
"v_exp_f32 v227, v83\n"
"v_add_f32 v232, v224, v225\n"
"v_cvt_pk_bf16_f32 v188, v224, v225\n"
"v_exp_f32 v228, v84\n"
"v_exp_f32 v229, v85\n"
"v_mfma_f32_32x32x16_bf16 v[116:131], a[164:167], v[4:7], v[116:131]\n"
"ds_read_b128 a[232:235], v240 offset:1024\n"
"v_add_f32 v232, v232, v226\n"
"v_add_f32 v232, v232, v227\n"
"v_cvt_pk_bf16_f32 v189, v226, v227\n"
"v_exp_f32 v230, v86\n"
"v_exp_f32 v231, v87\n"
"v_mfma_f32_32x32x16_bf16 v[132:147], a[164:167], v[36:39], v[132:147]\n"
"ds_read_b128 a[236:239], v240 offset:1536\n"
"v_add_f32 v232, v232, v228\n"
"v_add_f32 v232, v232, v229\n"
"v_cvt_pk_bf16_f32 v190, v228, v229\n"
"v_exp_f32 v224, v88\n"
"v_exp_f32 v225, v89\n"
"v_mfma_f32_32x32x16_bf16 v[116:131], a[168:171], v[8:11], v[116:131]\n"
"s_waitcnt vmcnt(5)\n"
"s_add_u32 m0, s86, 0x400\n"
"s_add_u32 s72, s84, 0x80\n"
"buffer_load_dwordx4 v245, s[32:35], s72 offen nt lds\n"
"buffer_load_dwordx4 v246, s[32:35], s72 offen offset:2048 nt lds\n"
"v_add_f32 v232, v232, v230\n"
"v_add_f32 v232, v232, v231\n"
"v_cvt_pk_bf16_f32 v191, v230, v231\n"
"v_exp_f32 v226, v90\n"
"v_exp_f32 v227, v91\n"
"v_mfma_f32_32x32x16_bf16 v[132:147], a[168:171], v[40:43], v[132:147]\n"
"v_add_f32 v232, v232, v224\n"
"v_add_f32 v232, v232, v225\n"
"v_cvt_pk_bf16_f32 v192, v224, v225\n"
"v_exp_f32 v228, v92\n"
"v_exp_f32 v229, v93\n"
"v_mfma_f32_32x32x16_bf16 v[116:131], a[172:175], v[12:15], v[116:131]\n"
"v_add_f32 v232, v232, v226\n"
"v_add_f32 v232, v232, v227\n"
"v_cvt_pk_bf16_f32 v193, v226, v227\n"
"v_exp_f32 v230, v94\n"
"v_mfma_f32_32x32x16_bf16 v[132:147], a[172:175], v[44:47], v[132:147]\n"
"v_exp_f32 v231, v95\n"
"v_add_f32 v232, v232, v228\n"
"v_add_f32 v232, v232, v229\n"
"v_cvt_pk_bf16_f32 v194, v228, v229\n"
"v_exp_f32 v224, v96\n"
"v_mfma_f32_32x32x16_bf16 v[116:131], a[176:179], v[16:19], v[116:131]\n"
"s_waitcnt vmcnt(5)\n"
"s_add_u32 m0, s87, 0x0\n"
"s_add_u32 s72, s85, 0x0\n"
"buffer_load_dwordx4 v245, s[36:39], s72 offen nt lds\n"
"buffer_load_dwordx4 v246, s[36:39], s72 offen offset:2048 nt lds\n"
"v_exp_f32 v225, v97\n"
"v_add_f32 v232, v232, v230\n"
"v_add_f32 v232, v232, v231\n"
"v_cvt_pk_bf16_f32 v195, v230, v231\n"
"v_exp_f32 v226, v98\n"
"v_mfma_f32_32x32x16_bf16 v[132:147], a[176:179], v[48:51], v[132:147]\n"
"v_exp_f32 v227, v99\n"
"v_add_f32 v233, v224, v225\n"
"v_cvt_pk_bf16_f32 v196, v224, v225\n"
"v_exp_f32 v228, v100\n"
"v_exp_f32 v229, v101\n"
"v_mfma_f32_32x32x16_bf16 v[116:131], a[180:183], v[20:23], v[116:131]\n"
"v_add_f32 v233, v233, v226\n"
"v_add_f32 v233, v233, v227\n"
"v_cvt_pk_bf16_f32 v197, v226, v227\n"
"v_exp_f32 v230, v102\n"
"v_exp_f32 v231, v103\n"
"v_mfma_f32_32x32x16_bf16 v[132:147], a[180:183], v[52:55], v[132:147]\n"
"v_add_f32 v233, v233, v228\n"
"v_add_f32 v233, v233, v229\n"
"v_cvt_pk_bf16_f32 v198, v228, v229\n"
"v_exp_f32 v224, v104\n"
"v_exp_f32 v225, v105\n"
"v_mfma_f32_32x32x16_bf16 v[116:131], a[184:187], v[24:27], v[116:131]\n"
"s_waitcnt vmcnt(5)\n"
"s_add_u32 m0, s87, 0x400\n"
"s_add_u32 s72, s85, 0x80\n"
"buffer_load_dwordx4 v245, s[36:39], s72 offen nt lds\n"
"buffer_load_dwordx4 v246, s[36:39], s72 offen offset:2048 nt lds\n"
"v_add_f32 v233, v233, v230\n"
"v_add_f32 v233, v233, v231\n"
"v_cvt_pk_bf16_f32 v199, v230, v231\n"
"v_exp_f32 v226, v106\n"
"v_mfma_f32_32x32x16_bf16 v[132:147], a[184:187], v[56:59], v[132:147]\n"
"v_exp_f32 v227, v107\n"
"v_add_f32 v233, v233, v224\n"
"v_add_f32 v233, v233, v225\n"
"v_cvt_pk_bf16_f32 v200, v224, v225\n"
"v_exp_f32 v228, v108\n"
"v_mfma_f32_32x32x16_bf16 v[116:131], a[188:191], v[28:31], v[116:131]\n"
"v_exp_f32 v229, v109\n"
"v_add_f32 v233, v233, v226\n"
"v_add_f32 v233, v233, v227\n"
"v_cvt_pk_bf16_f32 v201, v226, v227\n"
"v_exp_f32 v230, v110\n"
"v_mfma_f32_32x32x16_bf16 v[132:147], a[188:191], v[60:63], v[132:147]\n"
"v_exp_f32 v231, v111\n"
"v_add_f32 v233, v233, v228\n"
"v_add_f32 v233, v233, v229\n"
"v_cvt_pk_bf16_f32 v202, v228, v229\n"
"v_exp_f32 v224, v112\n"
"s_waitcnt lgkmcnt(0)\n"
"v_mfma_f32_16x16x32_bf16 v[148:151], a[224:227], v[64:67], v[184:187]\n"
"s_add_u32 s84, s84, 8192\n"
"s_add_u32 s85, s85, 8192\n"
"s_xor_b32 s86, s86, 0x4000\n"
"s_xor_b32 s87, s87, 0x4000\n"
"s_cmp_ge_u32 s84, 0x20000\n"
"s_cselect_b32 s86, 0x20000, s86\n"
"s_cmp_ge_u32 s85, 0x20000\n"
"s_cselect_b32 s87, 0x20000, s87\n"
"v_exp_f32 v225, v113\n"
"v_mfma_f32_16x16x32_bf16 v[148:151], a[228:231], v[68:71], v[148:151]\n"
"v_add_f32 v233, v233, v230\n"
"v_add_f32 v233, v233, v231\n"
"v_mfma_f32_16x16x32_bf16 v[148:151], a[232:235], v[72:75], v[148:151]\n"
"v_cvt_pk_bf16_f32 v203, v230, v231\n"
"v_mfma_f32_16x16x32_bf16 v[148:151], a[236:239], v[76:79], v[148:151]\n"
"v_exp_f32 v226, v114\n"
"v_mfma_f32_32x32x16_bf16 a[0:15], v[248:251], v[248:251], 0\n"
"v_exp_f32 v227, v115\n"
"v_add_f32 v234, v224, v225\n"
"v_cvt_pk_bf16_f32 v204, v224, v225\n"
"v_add_f32 v234, v234, v226\n"
"v_add_f32 v234, v234, v227\n"
"v_cvt_pk_bf16_f32 v205, v226, v227\n"
"v_mfma_f32_32x32x16_bf16 a[16:31], v[248:251], v[248:251], 0\n"
"s_waitcnt vmcnt(16) lgkmcnt(0)\n"
"s_barrier\n"
"v_xor_b32 v238, 0xc000, v238\n"
"v_xor_b32 v239, 0xc000, v239\n"
"v_mfma_f32_32x32x16_bf16 a[32:47], v[248:251], v[248:251], 0\n"
"ds_read_b128 a[160:163], v238 offset:0\n"
"v_mfma_f32_32x32x16_bf16 a[48:63], v[248:251], v[248:251], 0\n"
"ds_read_b128 a[164:167], v239 offset:0\n"
"v_xor_b32 v240, 0xc000, v240\n"
"v_mfma_f32_32x32x16_bf16 a[64:79], v[248:251], v[248:251], 0\n"
"ds_read_b128 a[168:171], v238 offset:512\n"
"v_xor_b32 v241, 0xc000, v241\n"
"v_mfma_f32_32x32x16_bf16 a[80:95], v[248:251], v[248:251], 0\n"
"ds_read_b128 a[172:175], v239 offset:512\n"
"v_xor_b32 v242, 0xc000, v242\n"
"v_mfma_f32_32x32x16_bf16 a[96:111], v[248:251], v[248:251], 0\n"
"ds_read_b128 a[176:179], v238 offset:1024\n"
"v_xor_b32 v243, 0xc000, v243\n"
"v_mfma_f32_32x32x16_bf16 a[112:127], v[248:251], v[248:251], 0\n"
"ds_read_b128 a[180:183], v239 offset:1024\n"
"v_xor_b32 v244, 0xc000, v244\n"
"v_mfma_f32_32x32x16_bf16 a[128:143], v[248:251], v[248:251], 0\n"
"ds_read_b128 a[184:187], v238 offset:1536\n"
"v_mfma_f32_32x32x16_bf16 a[144:159], v[248:251], v[248:251], 0\n"
"ds_read_b128 a[188:191], v239 offset:1536\n"
"v_max3_f32 v248, v232, v233, v234\n"
"v_cmp_lt_f32 vcc, s88, v248\n"
"s_nop 0\n"
"s_cbranch_vccnz Lstubi0_%=\n"
"Lbacki0_%=:\n"
"v_add_f32 v235, v235, v232\n"
"v_add_f32 v236, v236, v233\n"
"v_add_f32 v237, v237, v234\n"
"s_mov_b32 s73, 7\n"
"Latt_%=:\n"
"s_waitcnt lgkmcnt(7)\n"
"v_mfma_f32_32x32x16_bf16 v[80:95], a[160:163], v[0:3], v[152:167]\n"
"ds_read_b128 a[224:227], v240 offset:0\n"
"s_waitcnt vmcnt(5)\n"
"s_add_u32 m0, s86, 0x0\n"
"s_add_u32 s72, s84, 0x0\n"
"buffer_load_dwordx4 v245, s[32:35], s72 offen nt lds\n"
"buffer_load_dwordx4 v246, s[32:35], s72 offen offset:2048 nt lds\n"
"v_exp_f32 v224, v116\n"
"v_exp_f32 v225, v117\n"
"v_mfma_f32_32x32x16_bf16 v[96:111], a[160:163], v[32:35], v[168:183]\n"
"ds_read_b128 a[228:231], v240 offset:512\n"
"v_exp_f32 v226, v118\n"
"v_exp_f32 v227, v119\n"
"s_waitcnt lgkmcnt(7)\n"
"v_mfma_f32_32x32x16_bf16 v[80:95], a[164:167], v[4:7], v[80:95]\n"
"ds_read_b128 a[232:235], v240 offset:1024\n"
"v_add_f32 v232, v224, v225\n"
"v_cvt_pk_bf16_f32 v206, v224, v225\n"
"v_exp_f32 v228, v120\n"
"v_mfma_f32_32x32x16_bf16 v[96:111], a[164:167], v[36:39], v[96:111]\n"
"ds_read_b128 a[236:239], v240 offset:1536\n"
"v_exp_f32 v229, v121\n"
"v_add_f32 v232, v232, v226\n"
"v_mfma_f32_32x32x16_bf16 v[80:95], a[168:171], v[8:11], v[80:95]\n"
"ds_read_b64_tr_b16 a[192:193], v241 offset:0\n"
"s_waitcnt vmcnt(5)\n"
"s_add_u32 m0, s86, 0x400\n"
"s_add_u32 s72, s84, 0x80\n"
"buffer_load_dwordx4 v245, s[32:35], s72 offen nt lds\n"
"buffer_load_dwordx4 v246, s[32:35], s72 offen offset:2048 nt lds\n"
"v_add_f32 v232, v232, v227\n"
"v_cvt_pk_bf16_f32 v207, v226, v227\n"
"v_exp_f32 v230, v122\n"
"v_mfma_f32_32x32x16_bf16 v[96:111], a[168:171], v[40:43], v[96:111]\n"
"ds_read_b64_tr_b16 a[194:195], v242 offset:0\n"
"v_exp_f32 v231, v123\n"
"v_add_f32 v232, v232, v228\n"
"v_add_f32 v232, v232, v229\n"
"s_waitcnt lgkmcnt(5)\n"
"v_mfma_f32_32x32x16_bf16 v[80:95], a[172:175], v[12:15], v[80:95]\n"
"ds_read_b64_tr_b16 a[196:197], v241 offset:512\n"
"v_cvt_pk_bf16_f32 v208, v228, v229\n"
"v_exp_f32 v224, v124\n"
"v_mfma_f32_32x32x16_bf16 v[96:111], a[172:175], v[44:47], v[96:111]\n"
"ds_read_b64_tr_b16 a[198:199], v242 offset:512\n"
"v_exp_f32 v225, v125\n"
"v_add_f32 v232, v232, v230\n"
"v_add_f32 v232, v232, v231\n"
"v_cvt_pk_bf16_f32 v209, v230, v231\n"
"v_mfma_f32_32x32x16_bf16 v[80:95], a[176:179], v[16:19], v[80:95]\n"
"ds_read_b64_tr_b16 a[200:201], v241 offset:1024\n"
"s_waitcnt vmcnt(5)\n"
"s_add_u32 m0, s87, 0x0\n"
"s_add_u32 s72, s85, 0x0\n"
"buffer_load_dwordx4 v245, s[36:39], s72 offen nt lds\n"
"buffer_load_dwordx4 v246, s[36:39], s72 offen offset:2048 nt lds\n"
"v_exp_f32 v226, v126\n"
"v_exp_f32 v227, v127\n"
"v_mfma_f32_32x32x16_bf16 v[96:111], a[176:179], v[48:51], v[96:111]\n"
"ds_read_b64_tr_b16 a[202:203], v242 offset:1024\n"
"v_add_f32 v232, v232, v224\n"
"v_add_f32 v232, v232, v225\n"
"v_cvt_pk_bf16_f32 v210, v224, v225\n"
"v_mfma_f32_32x32x16_bf16 v[80:95], a[180:183], v[20:23], v[80:95]\n"
"ds_read_b64_tr_b16 a[204:205], v241 offset:1536\n"
"v_exp_f32 v228, v128\n"
"v_exp_f32 v229, v129\n"
"v_mfma_f32_32x32x16_bf16 v[96:111], a[180:183], v[52:55], v[96:111]\n"
"ds_read_b64_tr_b16 a[206:207], v242 offset:1536\n"
"v_add_f32 v232, v232, v226\n"
"v_add_f32 v232, v232, v227\n"
"v_cvt_pk_bf16_f32 v211, v226, v227\n"
"v_exp_f32 v230, v130\n"
"v_mfma_f32_32x32x16_bf16 v[80:95], a[184:187], v[24:27], v[80:95]\n"
"ds_read_b64_tr_b16 a[208:209], v241 offset:4096\n"
"s_waitcnt vmcnt(5)\n"
"s_add_u32 m0, s87, 0x400\n"
"s_add_u32 s72, s85, 0x80\n"
"buffer_load_dwordx4 v245, s[36:39], s72 offen nt lds\n"
"buffer_load_dwordx4 v246, s[36:39], s72 offen offset:2048 nt lds\n"
"v_exp_f32 v231, v131\n"
"v_add_f32 v232, v232, v228\n"
"v_mfma_f32_32x32x16_bf16 v[96:111], a[184:187], v[56:59], v[96:111]\n"
"ds_read_b64_tr_b16 a[210:211], v242 offset:4096\n"
"v_add_f32 v232, v232, v229\n"
"v_cvt_pk_bf16_f32 v212, v228, v229\n"
"v_exp_f32 v224, v132\n"
"v_mfma_f32_32x32x16_bf16 v[80:95], a[188:191], v[28:31], v[80:95]\n"
"ds_read_b64_tr_b16 a[212:213], v241 offset:4608\n"
"v_exp_f32 v225, v133\n"
"v_add_f32 v232, v232, v230\n"
"v_add_f32 v232, v232, v231\n"
"v_mfma_f32_32x32x16_bf16 v[96:111], a[188:191], v[60:63], v[96:111]\n"
"ds_read_b64_tr_b16 a[214:215], v242 offset:4608\n"
"v_cvt_pk_bf16_f32 v213, v230, v231\n"
"v_exp_f32 v226, v134\n"
"v_mfma_f32_16x16x32_bf16 v[112:115], a[224:227], v[64:67], v[184:187]\n"
"s_waitcnt lgkmcnt(5)\n"
"ds_read_b64_tr_b16 a[216:217], v241 offset:5120\n"
"s_add_u32 s84, s84, 8192\n"
"s_add_u32 s85, s85, 8192\n"
"s_xor_b32 s86, s86, 0xc000\n"
"s_xor_b32 s87, s87, 0xc000\n"
"s_cmp_ge_u32 s84, 0x20000\n"
"s_cselect_b32 s86, 0x20000, s86\n"
"s_cmp_ge_u32 s85, 0x20000\n"
"s_cselect_b32 s87, 0x20000, s87\n"
"v_exp_f32 v227, v135\n"
"v_mfma_f32_16x16x32_bf16 v[112:115], a[228:231], v[68:71], v[112:115]\n"
"ds_read_b64_tr_b16 a[218:219], v242 offset:5120\n"
"v_mfma_f32_16x16x32_bf16 v[112:115], a[232:235], v[72:75], v[112:115]\n"
"ds_read_b64_tr_b16 a[220:221], v241 offset:5632\n"
"v_add_f32 v233, v224, v225\n"
"v_mfma_f32_16x16x32_bf16 v[112:115], a[236:239], v[76:79], v[112:115]\n"
"ds_read_b64_tr_b16 a[222:223], v242 offset:5632\n"
"v_cvt_pk_bf16_f32 v214, v224, v225\n"
"v_mfma_f32_32x32x16_bf16 a[0:15], a[192:195], v[188:191], a[0:15]\n"
"ds_read_b64_tr_b16 a[240:241], v243 offset:0\n"
"v_exp_f32 v228, v136\n"
"v_exp_f32 v229, v137\n"
"v_mfma_f32_32x32x16_bf16 a[16:31], a[192:195], v[196:199], a[16:31]\n"
"ds_read_b64_tr_b16 a[242:243], v244 offset:0\n"
"v_add_f32 v233, v233, v226\n"
"v_add_f32 v233, v233, v227\n"
"v_cvt_pk_bf16_f32 v215, v226, v227\n"
"v_exp_f32 v230, v138\n"
"v_mfma_f32_32x32x16_bf16 a[32:47], a[196:199], v[188:191], a[32:47]\n"
"ds_read_b64_tr_b16 a[244:245], v243 offset:512\n"
"v_exp_f32 v231, v139\n"
"v_add_f32 v233, v233, v228\n"
"v_mfma_f32_32x32x16_bf16 a[48:63], a[196:199], v[196:199], a[48:63]\n"
"v_add_f32 v233, v233, v229\n"
"v_cvt_pk_bf16_f32 v216, v228, v229\n"
"v_exp_f32 v224, v140\n"
"ds_read_b64_tr_b16 a[246:247], v244 offset:512\n"
"v_mfma_f32_32x32x16_bf16 a[64:79], a[200:203], v[188:191], a[64:79]\n"
"v_exp_f32 v225, v141\n"
"v_add_f32 v233, v233, v230\n"
"v_add_f32 v233, v233, v231\n"
"v_mfma_f32_32x32x16_bf16 a[80:95], a[200:203], v[196:199], a[80:95]\n"
"ds_read_b64_tr_b16 a[248:249], v243 offset:1024\n"
"v_cvt_pk_bf16_f32 v217, v230, v231\n"
"v_exp_f32 v226, v142\n"
"s_waitcnt lgkmcnt(4)\n"
"v_mfma_f32_32x32x16_bf16 a[96:111], a[204:207], v[188:191], a[96:111]\n"
"ds_read_b64_tr_b16 a[250:251], v244 offset:1024\n"
"v_exp_f32 v227, v143\n"
"v_add_f32 v233, v233, v224\n"
"v_add_f32 v233, v233, v225\n"
"v_mfma_f32_32x32x16_bf16 a[112:127], a[204:207], v[196:199], a[112:127]\n"
"v_cvt_pk_bf16_f32 v218, v224, v225\n"
"ds_read_b64_tr_b16 a[252:253], v243 offset:1536\n"
"v_exp_f32 v228, v144\n"
"v_exp_f32 v229, v145\n"
"v_mfma_f32_32x32x16_bf16 a[0:15], a[208:211], v[192:195], a[0:15]\n"
"v_add_f32 v233, v233, v226\n"
"v_add_f32 v233, v233, v227\n"
"ds_read_b64_tr_b16 a[254:255], v244 offset:1536\n"
"v_cvt_pk_bf16_f32 v219, v226, v227\n"
"v_mfma_f32_32x32x16_bf16 a[16:31], a[208:211], v[200:203], a[16:31]\n"
"v_exp_f32 v230, v146\n"
"v_exp_f32 v231, v147\n"
"v_mfma_f32_32x32x16_bf16 a[32:47], a[212:215], v[192:195], a[32:47]\n"
"v_add_f32 v233, v233, v228\n"
"v_add_f32 v233, v233, v229\n"
"v_cvt_pk_bf16_f32 v220, v228, v229\n"
"v_mfma_f32_32x32x16_bf16 a[48:63], a[212:215], v[200:203], a[48:63]\n"
"v_exp_f32 v224, v148\n"
"v_exp_f32 v225, v149\n"
"v_mfma_f32_32x32x16_bf16 a[64:79], a[216:219], v[192:195], a[64:79]\n"
"v_add_f32 v233, v233, v230\n"
"v_add_f32 v233, v233, v231\n"
"v_cvt_pk_bf16_f32 v221, v230, v231\n"
"v_exp_f32 v226, v150\n"
"v_mfma_f32_32x32x16_bf16 a[80:95], a[216:219], v[200:203], a[80:95]\n"
"v_exp_f32 v227, v151\n"
"v_add_f32 v234, v224, v225\n"
"v_mfma_f32_32x32x16_bf16 a[96:111], a[220:223], v[192:195], a[96:111]\n"
"v_cvt_pk_bf16_f32 v222, v224, v225\n"
"v_add_f32 v234, v234, v226\n"
"v_add_f32 v234, v234, v227\n"
"v_cvt_pk_bf16_f32 v223, v226, v227\n"
"v_mfma_f32_32x32x16_bf16 a[112:127], a[220:223], v[200:203], a[112:127]\n"
"s_waitcnt vmcnt(16) lgkmcnt(0)\n"
"s_barrier\n"
"v_xor_b32 v238, 0x4000, v238\n"
"v_xor_b32 v239, 0x4000, v239\n"
"v_mfma_f32_16x16x16_bf16 a[128:131], a[240:241], v[204:205], a[128:131]\n"
"ds_read_b128 a[160:163], v238 offset:0\n"
"v_mfma_f32_16x16x16_bf16 a[132:135], a[242:243], v[204:205], a[132:135]\n"
"ds_read_b128 a[164:167], v239 offset:0\n"
"v_xor_b32 v240, 0x4000, v240\n"
"v_mfma_f32_16x16x16_bf16 a[136:139], a[244:245], v[204:205], a[136:139]\n"
"ds_read_b128 a[168:171], v238 offset:512\n"
"v_xor_b32 v241, 0x4000, v241\n"
"v_mfma_f32_16x16x16_bf16 a[140:143], a[246:247], v[204:205], a[140:143]\n"
"ds_read_b128 a[172:175], v239 offset:512\n"
"v_xor_b32 v242, 0x4000, v242\n"
"v_mfma_f32_16x16x16_bf16 a[144:147], a[248:249], v[204:205], a[144:147]\n"
"ds_read_b128 a[176:179], v238 offset:1024\n"
"v_xor_b32 v243, 0x4000, v243\n"
"v_mfma_f32_16x16x16_bf16 a[148:151], a[250:251], v[204:205], a[148:151]\n"
"ds_read_b128 a[180:183], v239 offset:1024\n"
"v_xor_b32 v244, 0x4000, v244\n"
"v_mfma_f32_16x16x16_bf16 a[152:155], a[252:253], v[204:205], a[152:155]\n"
"ds_read_b128 a[184:187], v238 offset:1536\n"
"v_mfma_f32_16x16x16_bf16 a[156:159], a[254:255], v[204:205], a[156:159]\n"
"ds_read_b128 a[188:191], v239 offset:1536\n"
"v_max3_f32 v248, v232, v233, v234\n"
"v_cmp_lt_f32 vcc, s88, v248\n"
"s_nop 0\n"
"s_cbranch_vccnz Lstubod_%=\n"
"Lbackod_%=:\n"
"v_add_f32 v235, v235, v232\n"
"v_add_f32 v236, v236, v233\n"
"v_add_f32 v237, v237, v234\n"
"s_waitcnt lgkmcnt(7)\n"
"v_mfma_f32_32x32x16_bf16 v[116:131], a[160:163], v[0:3], v[152:167]\n"
"ds_read_b128 a[224:227], v240 offset:0\n"
"s_waitcnt vmcnt(5)\n"
"s_add_u32 m0, s86, 0x0\n"
"s_add_u32 s72, s84, 0x0\n"
"buffer_load_dwordx4 v245, s[32:35], s72 offen nt lds\n"
"buffer_load_dwordx4 v246, s[32:35], s72 offen offset:2048 nt lds\n"
"v_exp_f32 v224, v80\n"
"v_exp_f32 v225, v81\n"
"v_mfma_f32_32x32x16_bf16 v[132:147], a[160:163], v[32:35], v[168:183]\n"
"ds_read_b128 a[228:231], v240 offset:512\n"
"v_exp_f32 v226, v82\n"
"v_exp_f32 v227, v83\n"
"s_waitcnt lgkmcnt(8)\n"
"v_mfma_f32_32x32x16_bf16 v[116:131], a[164:167], v[4:7], v[116:131]\n"
"ds_read_b128 a[232:235], v240 offset:1024\n"
"v_add_f32 v232, v224, v225\n"
"v_cvt_pk_bf16_f32 v188, v224, v225\n"
"v_exp_f32 v228, v84\n"
"v_mfma_f32_32x32x16_bf16 v[132:147], a[164:167], v[36:39], v[132:147]\n"
"ds_read_b128 a[236:239], v240 offset:1536\n"
"v_exp_f32 v229, v85\n"
"v_add_f32 v232, v232, v226\n"
"s_waitcnt lgkmcnt(6)\n"
"v_mfma_f32_32x32x16_bf16 v[116:131], a[168:171], v[8:11], v[116:131]\n"
"ds_read_b64_tr_b16 a[192:193], v241 offset:0\n"
"s_waitcnt vmcnt(5)\n"
"s_add_u32 m0, s86, 0x400\n"
"s_add_u32 s72, s84, 0x80\n"
"buffer_load_dwordx4 v245, s[32:35], s72 offen nt lds\n"
"buffer_load_dwordx4 v246, s[32:35], s72 offen offset:2048 nt lds\n"
"v_add_f32 v232, v232, v227\n"
"v_cvt_pk_bf16_f32 v189, v226, v227\n"
"v_exp_f32 v230, v86\n"
"v_mfma_f32_32x32x16_bf16 v[132:147], a[168:171], v[40:43], v[132:147]\n"
"ds_read_b64_tr_b16 a[194:195], v242 offset:0\n"
"v_exp_f32 v231, v87\n"
"v_add_f32 v232, v232, v228\n"
"v_add_f32 v232, v232, v229\n"
"v_mfma_f32_32x32x16_bf16 v[116:131], a[172:175], v[12:15], v[116:131]\n"
"ds_read_b64_tr_b16 a[196:197], v241 offset:512\n"
"v_cvt_pk_bf16_f32 v190, v228, v229\n"
"v_exp_f32 v224, v88\n"
"v_mfma_f32_32x32x16_bf16 v[132:147], a[172:175], v[44:47], v[132:147]\n"
"ds_read_b64_tr_b16 a[198:199], v242 offset:512\n"
"v_exp_f32 v225, v89\n"
"v_add_f32 v232, v232, v230\n"
"v_add_f32 v232, v232, v231\n"
"v_cvt_pk_bf16_f32 v191, v230, v231\n"
"v_mfma_f32_32x32x16_bf16 v[116:131], a[176:179], v[16:19], v[116:131]\n"
"ds_read_b64_tr_b16 a[200:201], v241 offset:1024\n"
"s_waitcnt vmcnt(5)\n"
"s_add_u32 m0, s87, 0x0\n"
"s_add_u32 s72, s85, 0x0\n"
"buffer_load_dwordx4 v245, s[36:39], s72 offen nt lds\n"
"buffer_load_dwordx4 v246, s[36:39], s72 offen offset:2048 nt lds\n"
"v_exp_f32 v226, v90\n"
"v_exp_f32 v227, v91\n"
"v_mfma_f32_32x32x16_bf16 v[132:147], a[176:179], v[48:51], v[132:147]\n"
"ds_read_b64_tr_b16 a[202:203], v242 offset:1024\n"
"v_add_f32 v232, v232, v224\n"
"v_add_f32 v232, v232, v225\n"
"v_cvt_pk_bf16_f32 v192, v224, v225\n"
"v_mfma_f32_32x32x16_bf16 v[116:131], a[180:183], v[20:23], v[116:131]\n"
"ds_read_b64_tr_b16 a[204:205], v241 offset:1536\n"
"v_exp_f32 v228, v92\n"
"v_exp_f32 v229, v93\n"
"v_mfma_f32_32x32x16_bf16 v[132:147], a[180:183], v[52:55], v[132:147]\n"
"ds_read_b64_tr_b16 a[206:207], v242 offset:1536\n"
"v_add_f32 v232, v232, v226\n"
"v_add_f32 v232, v232, v227\n"
"v_cvt_pk_bf16_f32 v193, v226, v227\n"
"v_exp_f32 v230, v94\n"
"s_waitcnt lgkmcnt(5)\n"
"v_mfma_f32_32x32x16_bf16 v[116:131], a[184:187], v[24:27], v[116:131]\n"
"ds_read_b64_tr_b16 a[208:209], v241 offset:4096\n"
"s_waitcnt vmcnt(5)\n"
"s_add_u32 m0, s87, 0x400\n"
"s_add_u32 s72, s85, 0x80\n"
"buffer_load_dwordx4 v245, s[36:39], s72 offen nt lds\n"
"buffer_load_dwordx4 v246, s[36:39], s72 offen offset:2048 nt lds\n"
"v_exp_f32 v231, v95\n"
"v_add_f32 v232, v232, v228\n"
"v_mfma_f32_32x32x16_bf16 v[132:147], a[184:187], v[56:59], v[132:147]\n"
"ds_read_b64_tr_b16 a[210:211], v242 offset:4096\n"
"v_add_f32 v232, v232, v229\n"
"v_cvt_pk_bf16_f32 v194, v228, v229\n"
"v_exp_f32 v224, v96\n"
"v_mfma_f32_32x32x16_bf16 v[116:131], a[188:191], v[28:31], v[116:131]\n"
"ds_read_b64_tr_b16 a[212:213], v241 offset:4608\n"
"v_exp_f32 v225, v97\n"
"v_add_f32 v232, v232, v230\n"
"v_add_f32 v232, v232, v231\n"
"v_mfma_f32_32x32x16_bf16 v[132:147], a[188:191], v[60:63], v[132:147]\n"
"ds_read_b64_tr_b16 a[214:215], v242 offset:4608\n"
"v_cvt_pk_bf16_f32 v195, v230, v231\n"
"v_exp_f32 v226, v98\n"
"v_mfma_f32_16x16x32_bf16 v[148:151], a[224:227], v[64:67], v[184:187]\n"
"ds_read_b64_tr_b16 a[216:217], v241 offset:5120\n"
"s_add_u32 s84, s84, 8192\n"
"s_add_u32 s85, s85, 8192\n"
"s_xor_b32 s86, s86, 0x4000\n"
"s_xor_b32 s87, s87, 0x4000\n"
"s_cmp_ge_u32 s84, 0x20000\n"
"s_cselect_b32 s86, 0x20000, s86\n"
"s_cmp_ge_u32 s85, 0x20000\n"
"s_cselect_b32 s87, 0x20000, s87\n"
"v_exp_f32 v227, v99\n"
"v_mfma_f32_16x16x32_bf16 v[148:151], a[228:231], v[68:71], v[148:151]\n"
"ds_read_b64_tr_b16 a[218:219], v242 offset:5120\n"
"v_mfma_f32_16x16x32_bf16 v[148:151], a[232:235], v[72:75], v[148:151]\n"
"ds_read_b64_tr_b16 a[220:221], v241 offset:5632\n"
"v_add_f32 v233, v224, v225\n"
"v_mfma_f32_16x16x32_bf16 v[148:151], a[236:239], v[76:79], v[148:151]\n"
"ds_read_b64_tr_b16 a[222:223], v242 offset:5632\n"
"v_cvt_pk_bf16_f32 v196, v224, v225\n"
"v_mfma_f32_32x32x16_bf16 a[0:15], a[192:195], v[206:209], a[0:15]\n"
"ds_read_b64_tr_b16 a[240:241], v243 offset:0\n"
"v_exp_f32 v228, v100\n"
"v_exp_f32 v229, v101\n"
"v_mfma_f32_32x32x16_bf16 a[16:31], a[192:195], v[214:217], a[16:31]\n"
"ds_read_b64_tr_b16 a[242:243], v244 offset:0\n"
"v_add_f32 v233, v233, v226\n"
"v_add_f32 v233, v233, v227\n"
"v_cvt_pk_bf16_f32 v197, v226, v227\n"
"v_exp_f32 v230, v102\n"
"s_waitcnt lgkmcnt(7)\n"
"v_mfma_f32_32x32x16_bf16 a[32:47], a[196:199], v[206:209], a[32:47]\n"
"ds_read_b64_tr_b16 a[244:245], v243 offset:512\n"
"v_exp_f32 v231, v103\n"
"v_add_f32 v233, v233, v228\n"
"v_mfma_f32_32x32x16_bf16 a[48:63], a[196:199], v[214:217], a[48:63]\n"
"v_add_f32 v233, v233, v229\n"
"v_cvt_pk_bf16_f32 v198, v228, v229\n"
"v_exp_f32 v224, v104\n"
"ds_read_b64_tr_b16 a[246:247], v244 offset:512\n"
"v_mfma_f32_32x32x16_bf16 a[64:79], a[200:203], v[206:209], a[64:79]\n"
"v_exp_f32 v225, v105\n"
"v_add_f32 v233, v233, v230\n"
"v_add_f32 v233, v233, v231\n"
"v_mfma_f32_32x32x16_bf16 a[80:95], a[200:203], v[214:217], a[80:95]\n"
"ds_read_b64_tr_b16 a[248:249], v243 offset:1024\n"
"v_cvt_pk_bf16_f32 v199, v230, v231\n"
"v_exp_f32 v226, v106\n"
"v_mfma_f32_32x32x16_bf16 a[96:111], a[204:207], v[206:209], a[96:111]\n"
"ds_read_b64_tr_b16 a[250:251], v244 offset:1024\n"
"v_exp_f32 v227, v107\n"
"v_add_f32 v233, v233, v224\n"
"v_add_f32 v233, v233, v225\n"
"v_mfma_f32_32x32x16_bf16 a[112:127], a[204:207], v[214:217], a[112:127]\n"
"v_cvt_pk_bf16_f32 v200, v224, v225\n"
"ds_read_b64_tr_b16 a[252:253], v243 offset:1536\n"
"v_exp_f32 v228, v108\n"
"v_exp_f32 v229, v109\n"
"v_mfma_f32_32x32x16_bf16 a[0:15], a[208:211], v[210:213], a[0:15]\n"
"v_add_f32 v233, v233, v226\n"
"v_add_f32 v233, v233, v227\n"
"ds_read_b64_tr_b16 a[254:255], v244 offset:1536\n"
"v_cvt_pk_bf16_f32 v201, v226, v227\n"
"v_mfma_f32_32x32x16_bf16 a[16:31], a[208:211], v[218:221], a[16:31]\n"
"v_exp_f32 v230, v110\n"
"v_exp_f32 v231, v111\n"
"s_waitcnt lgkmcnt(4)\n"
"v_mfma_f32_32x32x16_bf16 a[32:47], a[212:215], v[210:213], a[32:47]\n"
"v_add_f32 v233, v233, v228\n"
"v_add_f32 v233, v233, v229\n"
"v_cvt_pk_bf16_f32 v202, v228, v229\n"
"v_mfma_f32_32x32x16_bf16 a[48:63], a[212:215], v[218:221], a[48:63]\n"
"v_exp_f32 v224, v112\n"
"v_exp_f32 v225, v113\n"
"v_mfma_f32_32x32x16_bf16 a[64:79], a[216:219], v[210:213], a[64:79]\n"
"v_add_f32 v233, v233, v230\n"
"v_add_f32 v233, v233, v231\n"
"v_cvt_pk_bf16_f32 v203, v230, v231\n"
"v_exp_f32 v226, v114\n"
"v_mfma_f32_32x32x16_bf16 a[80:95], a[216:219], v[218:221], a[80:95]\n"
"v_exp_f32 v227, v115\n"
"v_add_f32 v234, v224, v225\n"
"v_mfma_f32_32x32x16_bf16 a[96:111], a[220:223], v[210:213], a[96:111]\n"
"v_cvt_pk_bf16_f32 v204, v224, v225\n"
"v_add_f32 v234, v234, v226\n"
"v_add_f32 v234, v234, v227\n"
"v_cvt_pk_bf16_f32 v205, v226, v227\n"
"v_mfma_f32_32x32x16_bf16 a[112:127], a[220:223], v[218:221], a[112:127]\n"
"s_waitcnt vmcnt(16) lgkmcnt(0)\n"
"s_barrier\n"
"v_xor_b32 v238, 0xc000, v238\n"
"v_xor_b32 v239, 0xc000, v239\n"
"v_mfma_f32_16x16x16_bf16 a[128:131], a[240:241], v[222:223], a[128:131]\n"
"ds_read_b128 a[160:163], v238 offset:0\n"
"v_mfma_f32_16x16x16_bf16 a[132:135], a[242:243], v[222:223], a[132:135]\n"
"ds_read_b128 a[164:167], v239 offset:0\n"
"v_xor_b32 v240, 0xc000, v240\n"
"v_mfma_f32_16x16x16_bf16 a[136:139], a[244:245], v[222:223], a[136:139]\n"
"ds_read_b128 a[168:171], v238 offset:512\n"
"v_xor_b32 v241, 0xc000, v241\n"
"v_mfma_f32_16x16x16_bf16 a[140:143], a[246:247], v[222:223], a[140:143]\n"
"ds_read_b128 a[172:175], v239 offset:512\n"
"v_xor_b32 v242, 0xc000, v242\n"
"v_mfma_f32_16x16x16_bf16 a[144:147], a[248:249], v[222:223], a[144:147]\n"
"ds_read_b128 a[176:179], v238 offset:1024\n"
"v_xor_b32 v243, 0xc000, v243\n"
"v_mfma_f32_16x16x16_bf16 a[148:151], a[250:251], v[222:223], a[148:151]\n"
"ds_read_b128 a[180:183], v239 offset:1024\n"
"v_xor_b32 v244, 0xc000, v244\n"
"v_mfma_f32_16x16x16_bf16 a[152:155], a[252:253], v[222:223], a[152:155]\n"
"ds_read_b128 a[184:187], v238 offset:1536\n"
"v_mfma_f32_16x16x16_bf16 a[156:159], a[254:255], v[222:223], a[156:159]\n"
"ds_read_b128 a[188:191], v239 offset:1536\n"
"v_max3_f32 v248, v232, v233, v234\n"
"v_cmp_lt_f32 vcc, s88, v248\n"
"s_nop 0\n"
"s_cbranch_vccnz Lstubev_%=\n"
"Lbackev_%=:\n"
"v_add_f32 v235, v235, v232\n"
"v_add_f32 v236, v236, v233\n"
"v_add_f32 v237, v237, v234\n"
"s_sub_u32 s73, s73, 1\n"
"s_cmp_lg_u32 s73, 0\n"
"s_cbranch_scc1 Latt_%=\n"
"ds_read_b64_tr_b16 a[192:193], v241 offset:0\n"
"ds_read_b64_tr_b16 a[194:195], v242 offset:0\n"
"ds_read_b64_tr_b16 a[196:197], v241 offset:512\n"
"ds_read_b64_tr_b16 a[198:199], v242 offset:512\n"
"ds_read_b64_tr_b16 a[200:201], v241 offset:1024\n"
"ds_read_b64_tr_b16 a[202:203], v242 offset:1024\n"
"s_waitcnt lgkmcnt(4)\n"
"v_mfma_f32_32x32x16_bf16 a[0:15], a[192:195], v[188:191], a[0:15]\n"
"ds_read_b64_tr_b16 a[204:205], v241 offset:1536\n"
"v_lshlrev_b32 v90, 4, v247\n"
"s_mul_i32 s21, s17, 0x87000\n"
"v_add_u32 v86, s21, v90\n"
"v_add_u32 v87, 0x21c00, v86\n"
"v_add_u32 v88, 0x21c00, v87\n"
"v_add_u32 v89, 0x21c00, v88\n"
"v_add_u32 v90, 0x10e000, v90\n"
"s_lshr_b32 s81, s20, 2\n"
"v_exp_f32 v224, v116\n"
"s_and_b32 s80, s20, 3\n"
"s_mul_i32 s21, s18, 72\n"
"v_exp_f32 v225, v117\n"
"s_mul_i32 s22, s19, 9\n"
"s_add_u32 s21, s21, s22\n"
"v_exp_f32 v226, v118\n"
"s_mul_i32 s74, s21, 0x21c00\n"
"s_lshl_b32 s21, s81, 15\n"
"ds_read_b64_tr_b16 a[206:207], v242 offset:1536\n"
"s_add_u32 s74, s74, s21\n"
"v_exp_f32 v227, v119\n"
"s_lshl_b32 s21, s16, 12\n"
"s_add_u32 s74, s74, s21\n"
"v_mfma_f32_32x32x16_bf16 a[16:31], a[192:195], v[196:199], a[16:31]\n"
"s_lshl_b32 s21, s80, 13\n"
"s_add_u32 s82, s74, s21\n"
"s_mul_i32 s21, s80, 320\n"
"s_add_u32 s23, s56, s21\n"
"s_addc_u32 s72, s57, 0\n"
"s_sub_u32 s21, 0x500000, s21\n"
"s_mov_b32 s24, s23\n"
"s_and_b32 s25, s72, 0xffff\n"
"s_mov_b32 s26, s21\n"
"s_mov_b32 s27, 0x20000\n"
"s_mov_b32 s28, s68\n"
"s_and_b32 s29, s69, 0xffff\n"
"v_add_f32 v232, v224, v225\n"
"ds_read_b64_tr_b16 a[208:209], v241 offset:4096\n"
"s_mov_b32 s30, 0x2d0000\n"
"v_cvt_pk_bf16_f32 v206, v224, v225\n"
"s_mov_b32 s31, 0x20000\n"
"s_add_u32 s23, s70, 0x2800000\n"
"v_exp_f32 v228, v120\n"
"s_addc_u32 s72, s71, 0\n"
"s_mov_b32 s32, s23\n"
"v_exp_f32 v229, v121\n"
"s_and_b32 s33, s72, 0xffff\n"
"s_mov_b32 s34, 0xc00000\n"
"s_mov_b32 s35, 0x20000\n"
"s_waitcnt lgkmcnt(5)\n"
"v_mfma_f32_32x32x16_bf16 a[32:47], a[196:199], v[188:191], a[32:47]\n"
"s_lshl_b32 s21, s81, 3\n"
"s_add_u32 s21, s21, s16\n"
"s_lshl_b32 s21, s21, 3\n"
"s_add_u32 s21, s21, s17\n"
"s_mul_i32 s75, s21, 20480\n"
"s_lshl_b32 s21, s16, 1\n"
"s_add_u32 s21, s21, s17\n"
"ds_read_b64_tr_b16 a[210:211], v242 offset:4096\n"
"s_mul_i32 s76, s21, 5120\n"
"v_lshrrev_b32 v84, 2, v247\n"
"v_and_b32 v84, 0x7, v84\n"
"v_mul_u32_u24 v84, 0x500, v84\n"
"v_add_f32 v232, v232, v226\n"
"v_lshrrev_b32 v253, 5, v247\n"
"v_add_f32 v232, v232, v227\n"
"v_lshlrev_b32 v253, 6, v253\n"
"v_cvt_pk_bf16_f32 v207, v226, v227\n"
"v_add_u32 v84, v253, v84\n"
"v_and_b32 v253, 0x3, v247\n"
"v_exp_f32 v230, v122\n"
"v_lshrrev_b32 v252, 2, v247\n"
"v_and_b32 v252, 0x7, v252\n"
"v_exp_f32 v231, v123\n"
"v_lshrrev_b32 v252, 2, v252\n"
"v_lshlrev_b32 v252, 1, v252\n"
"v_xor_b32 v253, v252, v253\n"
"v_lshlrev_b32 v253, 4, v253\n"
"v_mfma_f32_32x32x16_bf16 a[48:63], a[196:199], v[196:199], a[48:63]\n"
"v_add_u32 v84, v253, v84\n"
"v_lshrrev_b32 v85, 2, v247\n"
"ds_read_b64_tr_b16 a[212:213], v241 offset:4608\n"
"v_and_b32 v85, 0x7, v85\n"
"v_mul_u32_u24 v85, 0x500, v85\n"
"v_lshrrev_b32 v253, 5, v247\n"
"v_mul_u32_u24 v253, 0x2700, v253\n"
"v_add_u32 v85, v253, v85\n"
"v_and_b32 v253, 0x3, v247\n"
"v_lshrrev_b32 v252, 2, v247\n"
"v_and_b32 v252, 0x7, v252\n"
"v_add_f32 v232, v232, v228\n"
"v_lshrrev_b32 v252, 2, v252\n"
"v_add_f32 v232, v232, v229\n"
"v_lshlrev_b32 v252, 1, v252\n"
"v_cvt_pk_bf16_f32 v208, v228, v229\n"
"v_xor_b32 v253, v252, v253\n"
"v_lshlrev_b32 v253, 4, v253\n"
"v_exp_f32 v224, v124\n"
"v_add_u32 v85, v253, v85\n"
"v_lshrrev_b32 v80, 5, v247\n"
"v_mul_u32_u24 v80, 0x1400, v80\n"
"v_exp_f32 v225, v125\n"
"ds_read_b64_tr_b16 a[214:215], v242 offset:4608\n"
"v_lshrrev_b32 v253, 4, v247\n"
"v_and_b32 v253, 0x1, v253\n"
"v_lshlrev_b32 v253, 2, v253\n"
"s_waitcnt lgkmcnt(4)\n"
"v_mfma_f32_32x32x16_bf16 a[64:79], a[200:203], v[188:191], a[64:79]\n"
"v_lshrrev_b32 v252, 2, v247\n"
"v_and_b32 v252, 0x3, v252\n"
"v_add_u32 v253, v252, v253\n"
"v_lshlrev_b32 v253, 6, v253\n"
"v_add_u32 v80, v253, v80\n"
"v_and_b32 v253, 0x3, v247\n"
"v_lshrrev_b32 v253, 1, v253\n"
"v_add_u32 v253, 0x0, v253\n"
"v_lshrrev_b32 v252, 4, v247\n"
"v_and_b32 v252, 0x1, v252\n"
"v_lshlrev_b32 v252, 1, v252\n"
"v_add_f32 v232, v232, v230\n"
"v_xor_b32 v253, v252, v253\n"
"v_add_f32 v232, v232, v231\n"
"ds_read_b64_tr_b16 a[216:217], v241 offset:5120\n"
"v_lshlrev_b32 v253, 4, v253\n"
"v_cvt_pk_bf16_f32 v209, v230, v231\n"
"v_add_u32 v80, v253, v80\n"
"v_and_b32 v253, 0x3, v247\n"
"v_exp_f32 v226, v126\n"
"v_and_b32 v253, 0x1, v253\n"
"v_lshlrev_b32 v253, 3, v253\n"
"v_exp_f32 v227, v127\n"
"v_add_u32 v80, v253, v80\n"
"s_lshl_b32 s21, s17, 5\n"
"v_xor_b32 v80, s21, v80\n"
"v_mfma_f32_32x32x16_bf16 a[80:95], a[200:203], v[196:199], a[80:95]\n"
"s_mul_i32 s21, s16, 10240\n"
"s_add_u32 s21, s21, 4096\n"
"v_add_u32 v80, s21, v80\n"
"v_xor_b32 v81, 32, v80\n"
"v_mov_b32 v82, v81\n"
"v_mov_b32 v83, v80\n"
"s_mul_i32 s21, s17, 1024\n"
"ds_read_b64_tr_b16 a[218:219], v242 offset:5120\n"
"v_add_u32 v80, s21, v80\n"
"s_mul_i32 s21, s17, 1536\n"
"v_add_u32 v81, s21, v81\n"
"s_mul_i32 s21, s17, -1024\n"
"v_add_f32 v232, v232, v224\n"
"v_add_u32 v82, s21, v82\n"
"v_add_f32 v232, v232, v225\n"
"s_mul_i32 s21, s17, -1536\n"
"v_cvt_pk_bf16_f32 v210, v224, v225\n"
"v_add_u32 v83, s21, v83\n"
"v_exp_f32 v228, v128\n"
"v_exp_f32 v229, v129\n"
"v_mfma_f32_32x32x16_bf16 a[96:111], a[204:207], v[188:191], a[96:111]\n"
"s_add_u32 s78, s80, 0\n"
"s_and_b32 s78, s78, 3\n"
"ds_read_b64_tr_b16 a[220:221], v241 offset:5632\n"
"s_mul_i32 s78, s78, 327680\n"
"s_add_u32 s78, s78, s75\n"
"s_add_u32 m0, s76, 0x1000\n"
"s_add_u32 s79, s78, 0x0\n"
"v_add_f32 v232, v232, v226\n"
"v_add_f32 v232, v232, v227\n"
"v_cvt_pk_bf16_f32 v211, v226, v227\n"
"buffer_load_dwordx4 v84, s[24:27], s79 offen lds\n"
"v_exp_f32 v230, v130\n"
"v_exp_f32 v231, v131\n"
"ds_read_b64_tr_b16 a[222:223], v242 offset:5632\n"
"v_mfma_f32_32x32x16_bf16 a[112:127], a[204:207], v[196:199], a[112:127]\n"
"s_add_u32 m0, s76, 0x1400\n"
"s_add_u32 s79, s78, 0x80\n"
"buffer_load_dwordx4 v84, s[24:27], s79 offen lds\n"
"v_add_f32 v232, v232, v228\n"
"v_add_f32 v232, v232, v229\n"
"s_add_u32 m0, s76, 0x1800\n"
"s_add_u32 s79, s78, 0x100\n"
"v_cvt_pk_bf16_f32 v212, v228, v229\n"
"v_exp_f32 v224, v132\n"
"v_exp_f32 v225, v133\n"
"buffer_load_dwordx4 v85, s[24:27], s79 offen lds\n"
"s_waitcnt lgkmcnt(3)\n"
"v_mfma_f32_32x32x16_bf16 a[0:15], a[208:211], v[192:195], a[0:15]\n"
"ds_read_b64_tr_b16 a[240:241], v243 offset:0\n"
"s_add_u32 m0, s76, 0x1c00\n"
"s_add_u32 s79, s78, 0x2840\n"
"buffer_load_dwordx4 v84, s[24:27], s79 offen lds\n"
"v_add_f32 v232, v232, v230\n"
"v_add_f32 v232, v232, v231\n"
"v_cvt_pk_bf16_f32 v213, v230, v231\n"
"v_exp_f32 v226, v134\n"
"s_add_u32 m0, s76, 0x2000\n"
"s_add_u32 s79, s78, 0x28c0\n"
"v_exp_f32 v227, v135\n"
"v_mfma_f32_32x32x16_bf16 a[16:31], a[208:211], v[200:203], a[16:31]\n"
"ds_read_b64_tr_b16 a[242:243], v244 offset:0\n"
"buffer_load_dwordx4 v84, s[24:27], s79 offen lds\n"
"s_add_u32 s78, s80, 0\n"
"s_and_b32 s78, s78, 3\n"
"s_mul_i32 s78, s78, 327680\n"
"v_add_f32 v233, v224, v225\n"
"s_add_u32 s78, s78, s75\n"
"v_cvt_pk_bf16_f32 v214, v224, v225\n"
"v_exp_f32 v228, v136\n"
"v_exp_f32 v229, v137\n"
"s_add_u32 m0, s76, 0x6000\n"
"s_add_u32 s79, s78, 0xa000\n"
"v_add_f32 v233, v233, v226\n"
"v_mfma_f32_32x32x16_bf16 a[32:47], a[212:215], v[192:195], a[32:47]\n"
"ds_read_b64_tr_b16 a[244:245], v243 offset:512\n"
"buffer_load_dwordx4 v84, s[24:27], s79 offen lds\n"
"s_add_u32 m0, s76, 0x6400\n"
"s_add_u32 s79, s78, 0xa080\n"
"v_add_f32 v233, v233, v227\n"
"v_cvt_pk_bf16_f32 v215, v226, v227\n"
"v_exp_f32 v230, v138\n"
"buffer_load_dwordx4 v84, s[24:27], s79 offen lds\n"
"v_exp_f32 v231, v139\n"
"v_add_f32 v233, v233, v228\n"
"v_mfma_f32_32x32x16_bf16 a[48:63], a[212:215], v[200:203], a[48:63]\n"
"s_add_u32 m0, s76, 0x6800\n"
"s_add_u32 s79, s78, 0xa100\n"
"ds_read_b64_tr_b16 a[246:247], v244 offset:512\n"
"buffer_load_dwordx4 v85, s[24:27], s79 offen lds\n"
"v_add_f32 v233, v233, v229\n"
"v_cvt_pk_bf16_f32 v216, v228, v229\n"
"s_add_u32 m0, s76, 0x6c00\n"
"s_add_u32 s79, s78, 0xc840\n"
"v_exp_f32 v224, v140\n"
"v_exp_f32 v225, v141\n"
"v_add_f32 v233, v233, v230\n"
"buffer_load_dwordx4 v84, s[24:27], s79 offen lds\n"
"s_waitcnt lgkmcnt(4)\n"
"v_mfma_f32_32x32x16_bf16 a[64:79], a[216:219], v[192:195], a[64:79]\n"
"ds_read_b64_tr_b16 a[248:249], v243 offset:1024\n"
"s_add_u32 m0, s76, 0x7000\n"
"s_add_u32 s79, s78, 0xc8c0\n"
"buffer_load_dwordx4 v84, s[24:27], s79 offen lds\n"
"v_add_f32 v233, v233, v231\n"
"v_cvt_pk_bf16_f32 v217, v230, v231\n"
"s_add_u32 s78, s80, 0\n"
"s_and_b32 s78, s78, 3\n"
"v_exp_f32 v226, v142\n"
"s_mul_i32 s78, s78, 327680\n"
"s_add_u32 s78, s78, s75\n"
"v_exp_f32 v227, v143\n"
"v_add_f32 v233, v233, v224\n"
"v_mfma_f32_32x32x16_bf16 a[80:95], a[216:219], v[200:203], a[80:95]\n"
"s_add_u32 m0, s76, 0xb000\n"
"s_add_u32 s79, s78, 0x14000\n"
"ds_read_b64_tr_b16 a[250:251], v244 offset:1024\n"
"buffer_load_dwordx4 v84, s[24:27], s79 offen lds\n"
"v_add_f32 v233, v233, v225\n"
"s_add_u32 m0, s76, 0xb400\n"
"s_add_u32 s79, s78, 0x14080\n"
"v_cvt_pk_bf16_f32 v218, v224, v225\n"
"v_exp_f32 v228, v144\n"
"v_exp_f32 v229, v145\n"
"buffer_load_dwordx4 v84, s[24:27], s79 offen lds\n"
"v_add_f32 v233, v233, v226\n"
"v_mfma_f32_32x32x16_bf16 a[96:111], a[220:223], v[192:195], a[96:111]\n"
"ds_read_b64_tr_b16 a[252:253], v243 offset:1536\n"
"s_add_u32 m0, s76, 0xb800\n"
"s_add_u32 s79, s78, 0x14100\n"
"buffer_load_dwordx4 v85, s[24:27], s79 offen lds\n"
"v_add_f32 v233, v233, v227\n"
"v_cvt_pk_bf16_f32 v219, v226, v227\n"
"v_exp_f32 v230, v146\n"
"s_add_u32 m0, s76, 0xbc00\n"
"s_add_u32 s79, s78, 0x16840\n"
"v_exp_f32 v231, v147\n"
"v_add_f32 v233, v233, v228\n"
"buffer_load_dwordx4 v84, s[24:27], s79 offen lds\n"
"v_mfma_f32_32x32x16_bf16 a[112:127], a[220:223], v[200:203], a[112:127]\n"
"ds_read_b64_tr_b16 a[254:255], v244 offset:1536\n"
"s_add_u32 m0, s76, 0xc000\n"
"s_add_u32 s79, s78, 0x168c0\n"
"buffer_load_dwordx4 v84, s[24:27], s79 offen lds\n"
"v_add_f32 v233, v233, v229\n"
"v_cvt_pk_bf16_f32 v220, v228, v229\n"
"v_exp_f32 v224, v148\n"
"v_exp_f32 v225, v149\n"
"v_add_f32 v233, v233, v230\n"
"s_waitcnt vmcnt(15) lgkmcnt(0)\n"
"s_barrier\n"
"v_xor_b32 v238, 0x4000, v238\n"
"v_xor_b32 v239, 0x4000, v239\n"
"v_mfma_f32_16x16x16_bf16 a[128:131], a[240:241], v[204:205], a[128:131]\n"
"v_add_f32 v233, v233, v231\n"
"v_cvt_pk_bf16_f32 v221, v230, v231\n"
"v_mfma_f32_16x16x16_bf16 a[132:135], a[242:243], v[204:205], a[132:135]\n"
"v_xor_b32 v240, 0x4000, v240\n"
"v_exp_f32 v226, v150\n"
"v_mfma_f32_16x16x16_bf16 a[136:139], a[244:245], v[204:205], a[136:139]\n"
"v_xor_b32 v241, 0x4000, v241\n"
"v_exp_f32 v227, v151\n"
"v_mfma_f32_16x16x16_bf16 a[140:143], a[246:247], v[204:205], a[140:143]\n"
"v_xor_b32 v242, 0x4000, v242\n"
"v_add_f32 v234, v224, v225\n"
"v_mfma_f32_16x16x16_bf16 a[144:147], a[248:249], v[204:205], a[144:147]\n"
"v_xor_b32 v243, 0x4000, v243\n"
"v_cvt_pk_bf16_f32 v222, v224, v225\n"
"v_add_f32 v234, v234, v226\n"
"v_mfma_f32_16x16x16_bf16 a[148:151], a[250:251], v[204:205], a[148:151]\n"
"v_xor_b32 v244, 0x4000, v244\n"
"v_add_f32 v234, v234, v227\n"
"v_cvt_pk_bf16_f32 v223, v226, v227\n"
"v_mfma_f32_16x16x16_bf16 a[152:155], a[252:253], v[204:205], a[152:155]\n"
"v_mfma_f32_16x16x16_bf16 a[156:159], a[254:255], v[204:205], a[156:159]\n"
"v_max3_f32 v248, v232, v233, v234\n"
"v_cmp_lt_f32 vcc, s88, v248\n"
"s_nop 0\n"
"s_cbranch_vccnz Lstubil_%=\n"
"Lbackil_%=:\n"
"v_add_f32 v235, v235, v232\n"
"v_add_f32 v236, v236, v233\n"
"v_add_f32 v237, v237, v234\n"
"ds_read_b64_tr_b16 a[192:193], v241 offset:0\n"
"ds_read_b64_tr_b16 a[194:195], v242 offset:0\n"
"ds_read_b64_tr_b16 a[196:197], v241 offset:512\n"
"ds_read_b64_tr_b16 a[198:199], v242 offset:512\n"
"ds_read_b64_tr_b16 a[200:201], v241 offset:1024\n"
"ds_read_b64_tr_b16 a[202:203], v242 offset:1024\n"
"ds_read_b64_tr_b16 a[204:205], v241 offset:1536\n"
"ds_read_b64_tr_b16 a[206:207], v242 offset:1536\n"
"s_waitcnt lgkmcnt(6)\n"
"v_mfma_f32_32x32x16_bf16 a[0:15], a[192:195], v[206:209], a[0:15]\n"
"ds_read_b64_tr_b16 a[208:209], v241 offset:4096\n"
"ds_read_b64_tr_b16 a[210:211], v242 offset:4096\n"
"v_mfma_f32_32x32x16_bf16 a[16:31], a[192:195], v[214:217], a[16:31]\n"
"ds_read_b64_tr_b16 a[212:213], v241 offset:4608\n"
"s_waitcnt lgkmcnt(7)\n"
"v_mfma_f32_32x32x16_bf16 a[32:47], a[196:199], v[206:209], a[32:47]\n"
"ds_read_b64_tr_b16 a[214:215], v242 offset:4608\n"
"s_add_u32 s78, s80, 0\n"
"s_and_b32 s78, s78, 3\n"
"ds_read_b64_tr_b16 a[216:217], v241 offset:5120\n"
"v_mfma_f32_32x32x16_bf16 a[48:63], a[196:199], v[214:217], a[48:63]\n"
"s_mul_i32 s78, s78, 327680\n"
"s_add_u32 s78, s78, s75\n"
"ds_read_b64_tr_b16 a[218:219], v242 offset:5120\n"
"s_waitcnt lgkmcnt(8)\n"
"v_mfma_f32_32x32x16_bf16 a[64:79], a[200:203], v[206:209], a[64:79]\n"
"s_add_u32 m0, s76, 0x10000\n"
"s_add_u32 s79, s78, 0x1e000\n"
"ds_read_b64_tr_b16 a[220:221], v241 offset:5632\n"
"buffer_load_dwordx4 v84, s[24:27], s79 offen lds\n"
"ds_read_b64_tr_b16 a[222:223], v242 offset:5632\n"
"v_mfma_f32_32x32x16_bf16 a[80:95], a[200:203], v[214:217], a[80:95]\n"
"s_add_u32 m0, s76, 0x10400\n"
"s_add_u32 s79, s78, 0x1e080\n"
"s_waitcnt lgkmcnt(8)\n"
"v_mfma_f32_32x32x16_bf16 a[96:111], a[204:207], v[206:209], a[96:111]\n"
"ds_read_b64_tr_b16 a[240:241], v243 offset:0\n"
"buffer_load_dwordx4 v84, s[24:27], s79 offen lds\n"
"v_mfma_f32_32x32x16_bf16 a[112:127], a[204:207], v[214:217], a[112:127]\n"
"s_add_u32 m0, s76, 0x10800\n"
"s_add_u32 s79, s78, 0x1e100\n"
"ds_read_b64_tr_b16 a[242:243], v244 offset:0\n"
"buffer_load_dwordx4 v85, s[24:27], s79 offen lds\n"
"s_waitcnt lgkmcnt(6)\n"
"v_mfma_f32_32x32x16_bf16 a[0:15], a[208:211], v[210:213], a[0:15]\n"
"ds_read_b64_tr_b16 a[244:245], v243 offset:512\n"
"s_add_u32 m0, s76, 0x10c00\n"
"s_add_u32 s79, s78, 0x20840\n"
"v_mfma_f32_32x32x16_bf16 a[16:31], a[208:211], v[218:221], a[16:31]\n"
"ds_read_b64_tr_b16 a[246:247], v244 offset:512\n"
"buffer_load_dwordx4 v84, s[24:27], s79 offen lds\n"
"v_mfma_f32_32x32x16_bf16 a[32:47], a[212:215], v[210:213], a[32:47]\n"
"s_add_u32 m0, s76, 0x11000\n"
"s_add_u32 s79, s78, 0x208c0\n"
"ds_read_b64_tr_b16 a[248:249], v243 offset:1024\n"
"buffer_load_dwordx4 v84, s[24:27], s79 offen lds\n"
"v_mfma_f32_32x32x16_bf16 a[48:63], a[212:215], v[218:221], a[48:63]\n"
"ds_read_b64_tr_b16 a[250:251], v244 offset:1024\n"
"s_waitcnt lgkmcnt(6)\n"
"v_mfma_f32_32x32x16_bf16 a[64:79], a[216:219], v[210:213], a[64:79]\n"
"ds_read_b64_tr_b16 a[252:253], v243 offset:1536\n"
"v_mfma_f32_32x32x16_bf16 a[80:95], a[216:219], v[218:221], a[80:95]\n"
"ds_read_b64_tr_b16 a[254:255], v244 offset:1536\n"
"v_mfma_f32_32x32x16_bf16 a[96:111], a[220:223], v[210:213], a[96:111]\n"
"v_mfma_f32_32x32x16_bf16 a[112:127], a[220:223], v[218:221], a[112:127]\n"
"s_waitcnt lgkmcnt(4)\n"
"v_mfma_f32_16x16x16_bf16 a[128:131], a[240:241], v[222:223], a[128:131]\n"
"v_mfma_f32_16x16x16_bf16 a[132:135], a[242:243], v[222:223], a[132:135]\n"
"v_mfma_f32_16x16x16_bf16 a[136:139], a[244:245], v[222:223], a[136:139]\n"
"v_mfma_f32_16x16x16_bf16 a[140:143], a[246:247], v[222:223], a[140:143]\n"
"s_waitcnt lgkmcnt(3)\n"
"v_mfma_f32_16x16x16_bf16 a[144:147], a[248:249], v[222:223], a[144:147]\n"
"s_waitcnt lgkmcnt(2)\n"
"v_mfma_f32_16x16x16_bf16 a[148:151], a[250:251], v[222:223], a[148:151]\n"
"s_waitcnt lgkmcnt(1)\n"
"v_mfma_f32_16x16x16_bf16 a[152:155], a[252:253], v[222:223], a[152:155]\n"
"s_waitcnt lgkmcnt(0)\n"
"v_mfma_f32_16x16x16_bf16 a[156:159], a[254:255], v[222:223], a[156:159]\n"
"v_mov_b32 v132, v184\n"
"v_mov_b32 v254, v235\n"
"s_nop 1\n"
"v_permlane32_swap_b32 v235, v254\n"
"v_add_f32 v235, v235, v254\n"
"v_mov_b32 v254, v236\n"
"s_nop 1\n"
"v_permlane32_swap_b32 v236, v254\n"
"v_add_f32 v236, v236, v254\n"
"v_mov_b32 v254, v237\n"
"s_nop 1\n"
"v_permlane32_swap_b32 v237, v254\n"
"v_add_f32 v237, v237, v254\n"
"v_mov_b32 v254, v237\n"
"s_nop 1\n"
"v_permlane16_swap_b32 v237, v254\n"
"v_add_f32 v133, v237, v254\n"
"v_mov_b32 v128, s16\n"
"v_mul_u32_u24 v128, 0x2800, v128\n"
"v_add_u32 v128, 0x20000, v128\n"
"v_mov_b32 v253, s17\n"
"v_mul_u32_u24 v253, 0x1400, v253\n"
"v_add_u32 v128, v253, v128\n"
"v_lshlrev_b32 v253, 4, v247\n"
"v_add_u32 v128, v253, v128\n"
"v_mov_b32 v129, s16\n"
"v_mul_u32_u24 v129, 0x2800, v129\n"
"v_add_u32 v129, 0x20000, v129\n"
"v_mov_b32 v253, s17\n"
"v_xor_b32 v253, 0x1, v253\n"
"v_mul_u32_u24 v253, 0x1400, v253\n"
"v_add_u32 v129, v253, v129\n"
"v_lshlrev_b32 v253, 4, v247\n"
"v_add_u32 v129, v253, v129\n"
"v_mov_b32 v130, s16\n"
"v_mul_u32_u24 v130, 0x2800, v130\n"
"v_add_u32 v130, 0x21000, v130\n"
"v_mov_b32 v253, s17\n"
"v_mul_u32_u24 v253, 0x1400, v253\n"
"v_add_u32 v130, v253, v130\n"
"v_lshlrev_b32 v253, 3, v247\n"
"v_add_u32 v130, v253, v130\n"
"v_mov_b32 v131, s16\n"
"v_mul_u32_u24 v131, 0x2800, v131\n"
"v_add_u32 v131, 0x21000, v131\n"
"v_mov_b32 v253, s17\n"
"v_xor_b32 v253, 0x1, v253\n"
"v_mul_u32_u24 v253, 0x1400, v253\n"
"v_add_u32 v131, v253, v131\n"
"v_lshlrev_b32 v253, 3, v247\n"
"v_add_u32 v131, v253, v131\n"
"ds_write_b64 v130, v[132:133]\n"
"s_nop 7\n"
"s_nop 7\n"
"s_cmp_eq_u32 s17, 0\n"
"s_cbranch_scc1 Lexw0_%=\n"
"ds_write_b128 v128, a[128:131] offset:0\n"
"ds_write_b128 v128, a[132:135] offset:1024\n"
"ds_write_b128 v128, a[136:139] offset:2048\n"
"ds_write_b128 v128, a[140:143] offset:3072\n"
"v_accvgpr_read_b32 v136, a144\n"
"v_accvgpr_read_b32 v137, a145\n"
"v_accvgpr_read_b32 v138, a146\n"
"v_accvgpr_read_b32 v139, a147\n"
"v_accvgpr_read_b32 v140, a148\n"
"v_accvgpr_read_b32 v141, a149\n"
"v_accvgpr_read_b32 v142, a150\n"
"v_accvgpr_read_b32 v143, a151\n"
"v_accvgpr_read_b32 v144, a152\n"
"v_accvgpr_read_b32 v145, a153\n"
"v_accvgpr_read_b32 v146, a154\n"
"v_accvgpr_read_b32 v147, a155\n"
"v_accvgpr_read_b32 v148, a156\n"
"v_accvgpr_read_b32 v149, a157\n"
"v_accvgpr_read_b32 v150, a158\n"
"v_accvgpr_read_b32 v151, a159\n"
"s_branch Lexw1_%=\n"
"Lexw0_%=:\n"
"ds_write_b128 v128, a[144:147] offset:0\n"
"ds_write_b128 v128, a[148:151] offset:1024\n"
"ds_write_b128 v128, a[152:155] offset:2048\n"
"ds_write_b128 v128, a[156:159] offset:3072\n"
"v_accvgpr_read_b32 v136, a128\n"
"v_accvgpr_read_b32 v137, a129\n"
"v_accvgpr_read_b32 v138, a130\n"
"v_accvgpr_read_b32 v139, a131\n"
"v_accvgpr_read_b32 v140, a132\n"
"v_accvgpr_read_b32 v141, a133\n"
"v_accvgpr_read_b32 v142, a134\n"
"v_accvgpr_read_b32 v143, a135\n"
"v_accvgpr_read_b32 v144, a136\n"
"v_accvgpr_read_b32 v145, a137\n"
"v_accvgpr_read_b32 v146, a138\n"
"v_accvgpr_read_b32 v147, a139\n"
"v_accvgpr_read_b32 v148, a140\n"
"v_accvgpr_read_b32 v149, a141\n"
"v_accvgpr_read_b32 v150, a142\n"
"v_accvgpr_read_b32 v151, a143\n"
"Lexw1_%=:\n"
"v_rcp_f32 v134, v235\n"
"v_rcp_f32 v135, v236\n"
"s_waitcnt lgkmcnt(0)\n"
"s_barrier\n"
"ds_read_b64 v[152:153], v131\n"
"ds_read_b128 v[154:157], v129 offset:0\n"
"ds_read_b128 v[158:161], v129 offset:1024\n"
"ds_read_b128 v[162:165], v129 offset:2048\n"
"ds_read_b128 v[166:169], v129 offset:3072\n"
"s_add_u32 s78, s80, 1\n"
"s_and_b32 s78, s78, 3\n"
"s_mul_i32 s78, s78, 327680\n"
"s_add_u32 s78, s78, s75\n"
"s_add_u32 m0, s76, 0x15000\n"
"s_add_u32 s79, s78, 0x0\n"
"buffer_load_dwordx4 v84, s[24:27], s79 offen lds\n"
"s_add_u32 m0, s76, 0x15400\n"
"s_add_u32 s79, s78, 0x80\n"
"buffer_load_dwordx4 v84, s[24:27], s79 offen lds\n"
"s_add_u32 m0, s76, 0x15800\n"
"s_add_u32 s79, s78, 0x100\n"
"buffer_load_dwordx4 v85, s[24:27], s79 offen lds\n"
"s_add_u32 m0, s76, 0x15c00\n"
"s_add_u32 s79, s78, 0x2840\n"
"buffer_load_dwordx4 v84, s[24:27], s79 offen lds\n"
"s_add_u32 m0, s76, 0x16000\n"
"s_add_u32 s79, s78, 0x28c0\n"
"buffer_load_dwordx4 v84, s[24:27], s79 offen lds\n"
"v_accvgpr_read_b32 v176, a0\n"
"v_accvgpr_read_b32 v177, a1\n"
"v_accvgpr_read_b32 v178, a2\n"
"v_accvgpr_read_b32 v179, a3\n"
"v_accvgpr_read_b32 v180, a4\n"
"v_accvgpr_read_b32 v181, a5\n"
"v_accvgpr_read_b32 v182, a6\n"
"v_accvgpr_read_b32 v183, a7\n"
"v_accvgpr_read_b32 v184, a8\n"
"v_accvgpr_read_b32 v185, a9\n"
"v_accvgpr_read_b32 v186, a10\n"
"v_accvgpr_read_b32 v187, a11\n"
"v_accvgpr_read_b32 v188, a12\n"
"v_accvgpr_read_b32 v189, a13\n"
"v_accvgpr_read_b32 v190, a14\n"
"v_accvgpr_read_b32 v191, a15\n"
"v_pk_mul_f32 v[176:177], v[176:177], v[134:135] op_sel:[0,0] op_sel_hi:[1,0]\n"
"v_pk_mul_f32 v[178:179], v[178:179], v[134:135] op_sel:[0,0] op_sel_hi:[1,0]\n"
"v_pk_mul_f32 v[180:181], v[180:181], v[134:135] op_sel:[0,0] op_sel_hi:[1,0]\n"
"v_pk_mul_f32 v[182:183], v[182:183], v[134:135] op_sel:[0,0] op_sel_hi:[1,0]\n"
"v_pk_mul_f32 v[184:185], v[184:185], v[134:135] op_sel:[0,0] op_sel_hi:[1,0]\n"
"v_pk_mul_f32 v[186:187], v[186:187], v[134:135] op_sel:[0,0] op_sel_hi:[1,0]\n"
"v_pk_mul_f32 v[188:189], v[188:189], v[134:135] op_sel:[0,0] op_sel_hi:[1,0]\n"
"v_pk_mul_f32 v[190:191], v[190:191], v[134:135] op_sel:[0,0] op_sel_hi:[1,0]\n"
"v_cvt_pk_bf16_f32 v0, v176, v177\n"
"v_cvt_pk_bf16_f32 v4, v184, v185\n"
"v_cvt_pk_bf16_f32 v1, v178, v179\n"
"v_cvt_pk_bf16_f32 v5, v186, v187\n"
"v_cvt_pk_bf16_f32 v2, v180, v181\n"
"v_cvt_pk_bf16_f32 v6, v188, v189\n"
"v_cvt_pk_bf16_f32 v3, v182, v183\n"
"v_cvt_pk_bf16_f32 v7, v190, v191\n"
"v_permlane32_swap_b32 v0, v4\n"
"v_permlane32_swap_b32 v1, v5\n"
"v_permlane32_swap_b32 v2, v6\n"
"v_permlane32_swap_b32 v3, v7\n"
"v_permlane16_swap_b32 v0, v4\n"
"v_permlane16_swap_b32 v1, v5\n"
"v_permlane16_swap_b32 v2, v6\n"
"v_permlane16_swap_b32 v3, v7\n"
"buffer_store_dwordx4 v[0:3], v86, s[40:43], s82 offen offset:0 sc1\n"
"buffer_store_dwordx4 v[4:7], v87, s[40:43], s82 offen offset:0 sc1\n"
"v_accvgpr_read_b32 v192, a16\n"
"v_accvgpr_read_b32 v193, a17\n"
"v_accvgpr_read_b32 v194, a18\n"
"v_accvgpr_read_b32 v195, a19\n"
"v_accvgpr_read_b32 v196, a20\n"
"v_accvgpr_read_b32 v197, a21\n"
"v_accvgpr_read_b32 v198, a22\n"
"v_accvgpr_read_b32 v199, a23\n"
"v_accvgpr_read_b32 v200, a24\n"
"v_accvgpr_read_b32 v201, a25\n"
"v_accvgpr_read_b32 v202, a26\n"
"v_accvgpr_read_b32 v203, a27\n"
"v_accvgpr_read_b32 v204, a28\n"
"v_accvgpr_read_b32 v205, a29\n"
"v_accvgpr_read_b32 v206, a30\n"
"v_accvgpr_read_b32 v207, a31\n"
"v_pk_mul_f32 v[192:193], v[192:193], v[134:135] op_sel:[0,1] op_sel_hi:[1,1]\n"
"v_pk_mul_f32 v[194:195], v[194:195], v[134:135] op_sel:[0,1] op_sel_hi:[1,1]\n"
"v_pk_mul_f32 v[196:197], v[196:197], v[134:135] op_sel:[0,1] op_sel_hi:[1,1]\n"
"v_pk_mul_f32 v[198:199], v[198:199], v[134:135] op_sel:[0,1] op_sel_hi:[1,1]\n"
"v_pk_mul_f32 v[200:201], v[200:201], v[134:135] op_sel:[0,1] op_sel_hi:[1,1]\n"
"v_pk_mul_f32 v[202:203], v[202:203], v[134:135] op_sel:[0,1] op_sel_hi:[1,1]\n"
"v_pk_mul_f32 v[204:205], v[204:205], v[134:135] op_sel:[0,1] op_sel_hi:[1,1]\n"
"v_pk_mul_f32 v[206:207], v[206:207], v[134:135] op_sel:[0,1] op_sel_hi:[1,1]\n"
"v_cvt_pk_bf16_f32 v8, v192, v193\n"
"v_cvt_pk_bf16_f32 v12, v200, v201\n"
"v_cvt_pk_bf16_f32 v9, v194, v195\n"
"v_cvt_pk_bf16_f32 v13, v202, v203\n"
"v_cvt_pk_bf16_f32 v10, v196, v197\n"
"v_cvt_pk_bf16_f32 v14, v204, v205\n"
"v_cvt_pk_bf16_f32 v11, v198, v199\n"
"v_cvt_pk_bf16_f32 v15, v206, v207\n"
"v_permlane32_swap_b32 v8, v12\n"
"v_permlane32_swap_b32 v9, v13\n"
"v_permlane32_swap_b32 v10, v14\n"
"v_permlane32_swap_b32 v11, v15\n"
"v_permlane16_swap_b32 v8, v12\n"
"v_permlane16_swap_b32 v9, v13\n"
"v_permlane16_swap_b32 v10, v14\n"
"v_permlane16_swap_b32 v11, v15\n"
"buffer_store_dwordx4 v[8:11], v88, s[40:43], s82 offen offset:0 sc1\n"
"buffer_store_dwordx4 v[12:15], v89, s[40:43], s82 offen offset:0 sc1\n"
"v_accvgpr_read_b32 v176, a32\n"
"v_accvgpr_read_b32 v177, a33\n"
"v_accvgpr_read_b32 v178, a34\n"
"v_accvgpr_read_b32 v179, a35\n"
"v_accvgpr_read_b32 v180, a36\n"
"v_accvgpr_read_b32 v181, a37\n"
"v_accvgpr_read_b32 v182, a38\n"
"v_accvgpr_read_b32 v183, a39\n"
"v_accvgpr_read_b32 v184, a40\n"
"v_accvgpr_read_b32 v185, a41\n"
"v_accvgpr_read_b32 v186, a42\n"
"v_accvgpr_read_b32 v187, a43\n"
"v_accvgpr_read_b32 v188, a44\n"
"v_accvgpr_read_b32 v189, a45\n"
"v_accvgpr_read_b32 v190, a46\n"
"v_accvgpr_read_b32 v191, a47\n"
"v_pk_mul_f32 v[176:177], v[176:177], v[134:135] op_sel:[0,0] op_sel_hi:[1,0]\n"
"v_pk_mul_f32 v[178:179], v[178:179], v[134:135] op_sel:[0,0] op_sel_hi:[1,0]\n"
"v_pk_mul_f32 v[180:181], v[180:181], v[134:135] op_sel:[0,0] op_sel_hi:[1,0]\n"
"v_pk_mul_f32 v[182:183], v[182:183], v[134:135] op_sel:[0,0] op_sel_hi:[1,0]\n"
"v_pk_mul_f32 v[184:185], v[184:185], v[134:135] op_sel:[0,0] op_sel_hi:[1,0]\n"
"v_pk_mul_f32 v[186:187], v[186:187], v[134:135] op_sel:[0,0] op_sel_hi:[1,0]\n"
"v_pk_mul_f32 v[188:189], v[188:189], v[134:135] op_sel:[0,0] op_sel_hi:[1,0]\n"
"v_pk_mul_f32 v[190:191], v[190:191], v[134:135] op_sel:[0,0] op_sel_hi:[1,0]\n"
"v_cvt_pk_bf16_f32 v20, v176, v177\n"
"v_cvt_pk_bf16_f32 v24, v184, v185\n"
"v_cvt_pk_bf16_f32 v21, v178, v179\n"
"v_cvt_pk_bf16_f32 v25, v186, v187\n"
"v_cvt_pk_bf16_f32 v22, v180, v181\n"
"v_cvt_pk_bf16_f32 v26, v188, v189\n"
"v_cvt_pk_bf16_f32 v23, v182, v183\n"
"v_cvt_pk_bf16_f32 v27, v190, v191\n"
"v_permlane32_swap_b32 v20, v24\n"
"v_permlane32_swap_b32 v21, v25\n"
"v_permlane32_swap_b32 v22, v26\n"
"v_permlane32_swap_b32 v23, v27\n"
"v_permlane16_swap_b32 v20, v24\n"
"v_permlane16_swap_b32 v21, v25\n"
"v_permlane16_swap_b32 v22, v26\n"
"v_permlane16_swap_b32 v23, v27\n"
"buffer_store_dwordx4 v[20:23], v86, s[40:43], s82 offen offset:1024 sc1\n"
"buffer_store_dwordx4 v[24:27], v87, s[40:43], s82 offen offset:1024 sc1\n"
"v_accvgpr_read_b32 v192, a48\n"
"v_accvgpr_read_b32 v193, a49\n"
"v_accvgpr_read_b32 v194, a50\n"
"v_accvgpr_read_b32 v195, a51\n"
"v_accvgpr_read_b32 v196, a52\n"
"v_accvgpr_read_b32 v197, a53\n"
"v_accvgpr_read_b32 v198, a54\n"
"v_accvgpr_read_b32 v199, a55\n"
"v_accvgpr_read_b32 v200, a56\n"
"v_accvgpr_read_b32 v201, a57\n"
"v_accvgpr_read_b32 v202, a58\n"
"v_accvgpr_read_b32 v203, a59\n"
"v_accvgpr_read_b32 v204, a60\n"
"v_accvgpr_read_b32 v205, a61\n"
"v_accvgpr_read_b32 v206, a62\n"
"v_accvgpr_read_b32 v207, a63\n"
"v_pk_mul_f32 v[192:193], v[192:193], v[134:135] op_sel:[0,1] op_sel_hi:[1,1]\n"
"v_pk_mul_f32 v[194:195], v[194:195], v[134:135] op_sel:[0,1] op_sel_hi:[1,1]\n"
"v_pk_mul_f32 v[196:197], v[196:197], v[134:135] op_sel:[0,1] op_sel_hi:[1,1]\n"
"v_pk_mul_f32 v[198:199], v[198:199], v[134:135] op_sel:[0,1] op_sel_hi:[1,1]\n"
"v_pk_mul_f32 v[200:201], v[200:201], v[134:135] op_sel:[0,1] op_sel_hi:[1,1]\n"
"v_pk_mul_f32 v[202:203], v[202:203], v[134:135] op_sel:[0,1] op_sel_hi:[1,1]\n"
"v_pk_mul_f32 v[204:205], v[204:205], v[134:135] op_sel:[0,1] op_sel_hi:[1,1]\n"
"v_pk_mul_f32 v[206:207], v[206:207], v[134:135] op_sel:[0,1] op_sel_hi:[1,1]\n"
"v_cvt_pk_bf16_f32 v28, v192, v193\n"
"v_cvt_pk_bf16_f32 v32, v200, v201\n"
"v_cvt_pk_bf16_f32 v29, v194, v195\n"
"v_cvt_pk_bf16_f32 v33, v202, v203\n"
"v_cvt_pk_bf16_f32 v30, v196, v197\n"
"v_cvt_pk_bf16_f32 v34, v204, v205\n"
"v_cvt_pk_bf16_f32 v31, v198, v199\n"
"v_cvt_pk_bf16_f32 v35, v206, v207\n"
"v_permlane32_swap_b32 v28, v32\n"
"v_permlane32_swap_b32 v29, v33\n"
"v_permlane32_swap_b32 v30, v34\n"
"v_permlane32_swap_b32 v31, v35\n"
"v_permlane16_swap_b32 v28, v32\n"
"v_permlane16_swap_b32 v29, v33\n"
"v_permlane16_swap_b32 v30, v34\n"
"v_permlane16_swap_b32 v31, v35\n"
"buffer_store_dwordx4 v[28:31], v88, s[40:43], s82 offen offset:1024 sc1\n"
"buffer_store_dwordx4 v[32:35], v89, s[40:43], s82 offen offset:1024 sc1\n"
"s_add_u32 s78, s80, 1\n"
"s_and_b32 s78, s78, 3\n"
"s_mul_i32 s78, s78, 327680\n"
"s_add_u32 s78, s78, s75\n"
"s_add_u32 m0, s76, 0x1a000\n"
"s_add_u32 s79, s78, 0xa000\n"
"buffer_load_dwordx4 v84, s[24:27], s79 offen lds\n"
"s_add_u32 m0, s76, 0x1a400\n"
"s_add_u32 s79, s78, 0xa080\n"
"buffer_load_dwordx4 v84, s[24:27], s79 offen lds\n"
"s_add_u32 m0, s76, 0x1a800\n"
"s_add_u32 s79, s78, 0xa100\n"
"buffer_load_dwordx4 v85, s[24:27], s79 offen lds\n"
"s_add_u32 m0, s76, 0x1ac00\n"
"s_add_u32 s79, s78, 0xc840\n"
"buffer_load_dwordx4 v84, s[24:27], s79 offen lds\n"
"s_add_u32 m0, s76, 0x1b000\n"
"s_add_u32 s79, s78, 0xc8c0\n"
"buffer_load_dwordx4 v84, s[24:27], s79 offen lds\n"
"v_accvgpr_read_b32 v176, a64\n"
"v_accvgpr_read_b32 v177, a65\n"
"v_accvgpr_read_b32 v178, a66\n"
"v_accvgpr_read_b32 v179, a67\n"
"v_accvgpr_read_b32 v180, a68\n"
"v_accvgpr_read_b32 v181, a69\n"
"v_accvgpr_read_b32 v182, a70\n"
"v_accvgpr_read_b32 v183, a71\n"
"v_accvgpr_read_b32 v184, a72\n"
"v_accvgpr_read_b32 v185, a73\n"
"v_accvgpr_read_b32 v186, a74\n"
"v_accvgpr_read_b32 v187, a75\n"
"v_accvgpr_read_b32 v188, a76\n"
"v_accvgpr_read_b32 v189, a77\n"
"v_accvgpr_read_b32 v190, a78\n"
"v_accvgpr_read_b32 v191, a79\n"
"v_pk_mul_f32 v[176:177], v[176:177], v[134:135] op_sel:[0,0] op_sel_hi:[1,0]\n"
"v_pk_mul_f32 v[178:179], v[178:179], v[134:135] op_sel:[0,0] op_sel_hi:[1,0]\n"
"v_pk_mul_f32 v[180:181], v[180:181], v[134:135] op_sel:[0,0] op_sel_hi:[1,0]\n"
"v_pk_mul_f32 v[182:183], v[182:183], v[134:135] op_sel:[0,0] op_sel_hi:[1,0]\n"
"v_pk_mul_f32 v[184:185], v[184:185], v[134:135] op_sel:[0,0] op_sel_hi:[1,0]\n"
"v_pk_mul_f32 v[186:187], v[186:187], v[134:135] op_sel:[0,0] op_sel_hi:[1,0]\n"
"v_pk_mul_f32 v[188:189], v[188:189], v[134:135] op_sel:[0,0] op_sel_hi:[1,0]\n"
"v_pk_mul_f32 v[190:191], v[190:191], v[134:135] op_sel:[0,0] op_sel_hi:[1,0]\n"
"v_cvt_pk_bf16_f32 v40, v176, v177\n"
"v_cvt_pk_bf16_f32 v44, v184, v185\n"
"v_cvt_pk_bf16_f32 v41, v178, v179\n"
"v_cvt_pk_bf16_f32 v45, v186, v187\n"
"v_cvt_pk_bf16_f32 v42, v180, v181\n"
"v_cvt_pk_bf16_f32 v46, v188, v189\n"
"v_cvt_pk_bf16_f32 v43, v182, v183\n"
"v_cvt_pk_bf16_f32 v47, v190, v191\n"
"v_permlane32_swap_b32 v40, v44\n"
"v_permlane32_swap_b32 v41, v45\n"
"v_permlane32_swap_b32 v42, v46\n"
"v_permlane32_swap_b32 v43, v47\n"
"v_permlane16_swap_b32 v40, v44\n"
"v_permlane16_swap_b32 v41, v45\n"
"v_permlane16_swap_b32 v42, v46\n"
"v_permlane16_swap_b32 v43, v47\n"
"buffer_store_dwordx4 v[40:43], v86, s[40:43], s82 offen offset:2048 sc1\n"
"buffer_store_dwordx4 v[44:47], v87, s[40:43], s82 offen offset:2048 sc1\n"
"v_accvgpr_read_b32 v192, a80\n"
"v_accvgpr_read_b32 v193, a81\n"
"v_accvgpr_read_b32 v194, a82\n"
"v_accvgpr_read_b32 v195, a83\n"
"v_accvgpr_read_b32 v196, a84\n"
"v_accvgpr_read_b32 v197, a85\n"
"v_accvgpr_read_b32 v198, a86\n"
"v_accvgpr_read_b32 v199, a87\n"
"v_accvgpr_read_b32 v200, a88\n"
"v_accvgpr_read_b32 v201, a89\n"
"v_accvgpr_read_b32 v202, a90\n"
"v_accvgpr_read_b32 v203, a91\n"
"v_accvgpr_read_b32 v204, a92\n"
"v_accvgpr_read_b32 v205, a93\n"
"v_accvgpr_read_b32 v206, a94\n"
"v_accvgpr_read_b32 v207, a95\n"
"v_pk_mul_f32 v[192:193], v[192:193], v[134:135] op_sel:[0,1] op_sel_hi:[1,1]\n"
"v_pk_mul_f32 v[194:195], v[194:195], v[134:135] op_sel:[0,1] op_sel_hi:[1,1]\n"
"v_pk_mul_f32 v[196:197], v[196:197], v[134:135] op_sel:[0,1] op_sel_hi:[1,1]\n"
"v_pk_mul_f32 v[198:199], v[198:199], v[134:135] op_sel:[0,1] op_sel_hi:[1,1]\n"
"v_pk_mul_f32 v[200:201], v[200:201], v[134:135] op_sel:[0,1] op_sel_hi:[1,1]\n"
"v_pk_mul_f32 v[202:203], v[202:203], v[134:135] op_sel:[0,1] op_sel_hi:[1,1]\n"
"v_pk_mul_f32 v[204:205], v[204:205], v[134:135] op_sel:[0,1] op_sel_hi:[1,1]\n"
"v_pk_mul_f32 v[206:207], v[206:207], v[134:135] op_sel:[0,1] op_sel_hi:[1,1]\n"
"v_cvt_pk_bf16_f32 v48, v192, v193\n"
"v_cvt_pk_bf16_f32 v52, v200, v201\n"
"v_cvt_pk_bf16_f32 v49, v194, v195\n"
"v_cvt_pk_bf16_f32 v53, v202, v203\n"
"v_cvt_pk_bf16_f32 v50, v196, v197\n"
"v_cvt_pk_bf16_f32 v54, v204, v205\n"
"v_cvt_pk_bf16_f32 v51, v198, v199\n"
"v_cvt_pk_bf16_f32 v55, v206, v207\n"
"v_permlane32_swap_b32 v48, v52\n"
"v_permlane32_swap_b32 v49, v53\n"
"v_permlane32_swap_b32 v50, v54\n"
"v_permlane32_swap_b32 v51, v55\n"
"v_permlane16_swap_b32 v48, v52\n"
"v_permlane16_swap_b32 v49, v53\n"
"v_permlane16_swap_b32 v50, v54\n"
"v_permlane16_swap_b32 v51, v55\n"
"buffer_store_dwordx4 v[48:51], v88, s[40:43], s82 offen offset:2048 sc1\n"
"buffer_store_dwordx4 v[52:55], v89, s[40:43], s82 offen offset:2048 sc1\n"
"v_accvgpr_read_b32 v176, a96\n"
"v_accvgpr_read_b32 v177, a97\n"
"v_accvgpr_read_b32 v178, a98\n"
"v_accvgpr_read_b32 v179, a99\n"
"v_accvgpr_read_b32 v180, a100\n"
"v_accvgpr_read_b32 v181, a101\n"
"v_accvgpr_read_b32 v182, a102\n"
"v_accvgpr_read_b32 v183, a103\n"
"v_accvgpr_read_b32 v184, a104\n"
"v_accvgpr_read_b32 v185, a105\n"
"v_accvgpr_read_b32 v186, a106\n"
"v_accvgpr_read_b32 v187, a107\n"
"v_accvgpr_read_b32 v188, a108\n"
"v_accvgpr_read_b32 v189, a109\n"
"v_accvgpr_read_b32 v190, a110\n"
"v_accvgpr_read_b32 v191, a111\n"
"v_pk_mul_f32 v[176:177], v[176:177], v[134:135] op_sel:[0,0] op_sel_hi:[1,0]\n"
"v_pk_mul_f32 v[178:179], v[178:179], v[134:135] op_sel:[0,0] op_sel_hi:[1,0]\n"
"v_pk_mul_f32 v[180:181], v[180:181], v[134:135] op_sel:[0,0] op_sel_hi:[1,0]\n"
"v_pk_mul_f32 v[182:183], v[182:183], v[134:135] op_sel:[0,0] op_sel_hi:[1,0]\n"
"v_pk_mul_f32 v[184:185], v[184:185], v[134:135] op_sel:[0,0] op_sel_hi:[1,0]\n"
"v_pk_mul_f32 v[186:187], v[186:187], v[134:135] op_sel:[0,0] op_sel_hi:[1,0]\n"
"v_pk_mul_f32 v[188:189], v[188:189], v[134:135] op_sel:[0,0] op_sel_hi:[1,0]\n"
"v_pk_mul_f32 v[190:191], v[190:191], v[134:135] op_sel:[0,0] op_sel_hi:[1,0]\n"
"v_cvt_pk_bf16_f32 v60, v176, v177\n"
"v_cvt_pk_bf16_f32 v64, v184, v185\n"
"v_cvt_pk_bf16_f32 v61, v178, v179\n"
"v_cvt_pk_bf16_f32 v65, v186, v187\n"
"v_cvt_pk_bf16_f32 v62, v180, v181\n"
"v_cvt_pk_bf16_f32 v66, v188, v189\n"
"v_cvt_pk_bf16_f32 v63, v182, v183\n"
"v_cvt_pk_bf16_f32 v67, v190, v191\n"
"v_permlane32_swap_b32 v60, v64\n"
"v_permlane32_swap_b32 v61, v65\n"
"v_permlane32_swap_b32 v62, v66\n"
"v_permlane32_swap_b32 v63, v67\n"
"v_permlane16_swap_b32 v60, v64\n"
"v_permlane16_swap_b32 v61, v65\n"
"v_permlane16_swap_b32 v62, v66\n"
"v_permlane16_swap_b32 v63, v67\n"
"buffer_store_dwordx4 v[60:63], v86, s[40:43], s82 offen offset:3072 sc1\n"
"buffer_store_dwordx4 v[64:67], v87, s[40:43], s82 offen offset:3072 sc1\n"
"v_accvgpr_read_b32 v192, a112\n"
"v_accvgpr_read_b32 v193, a113\n"
"v_accvgpr_read_b32 v194, a114\n"
"v_accvgpr_read_b32 v195, a115\n"
"v_accvgpr_read_b32 v196, a116\n"
"v_accvgpr_read_b32 v197, a117\n"
"v_accvgpr_read_b32 v198, a118\n"
"v_accvgpr_read_b32 v199, a119\n"
"v_accvgpr_read_b32 v200, a120\n"
"v_accvgpr_read_b32 v201, a121\n"
"v_accvgpr_read_b32 v202, a122\n"
"v_accvgpr_read_b32 v203, a123\n"
"v_accvgpr_read_b32 v204, a124\n"
"v_accvgpr_read_b32 v205, a125\n"
"v_accvgpr_read_b32 v206, a126\n"
"v_accvgpr_read_b32 v207, a127\n"
"v_pk_mul_f32 v[192:193], v[192:193], v[134:135] op_sel:[0,1] op_sel_hi:[1,1]\n"
"v_pk_mul_f32 v[194:195], v[194:195], v[134:135] op_sel:[0,1] op_sel_hi:[1,1]\n"
"v_pk_mul_f32 v[196:197], v[196:197], v[134:135] op_sel:[0,1] op_sel_hi:[1,1]\n"
"v_pk_mul_f32 v[198:199], v[198:199], v[134:135] op_sel:[0,1] op_sel_hi:[1,1]\n"
"v_pk_mul_f32 v[200:201], v[200:201], v[134:135] op_sel:[0,1] op_sel_hi:[1,1]\n"
"v_pk_mul_f32 v[202:203], v[202:203], v[134:135] op_sel:[0,1] op_sel_hi:[1,1]\n"
"v_pk_mul_f32 v[204:205], v[204:205], v[134:135] op_sel:[0,1] op_sel_hi:[1,1]\n"
"v_pk_mul_f32 v[206:207], v[206:207], v[134:135] op_sel:[0,1] op_sel_hi:[1,1]\n"
"v_cvt_pk_bf16_f32 v68, v192, v193\n"
"v_cvt_pk_bf16_f32 v72, v200, v201\n"
"v_cvt_pk_bf16_f32 v69, v194, v195\n"
"v_cvt_pk_bf16_f32 v73, v202, v203\n"
"v_cvt_pk_bf16_f32 v70, v196, v197\n"
"v_cvt_pk_bf16_f32 v74, v204, v205\n"
"v_cvt_pk_bf16_f32 v71, v198, v199\n"
"v_cvt_pk_bf16_f32 v75, v206, v207\n"
"v_permlane32_swap_b32 v68, v72\n"
"v_permlane32_swap_b32 v69, v73\n"
"v_permlane32_swap_b32 v70, v74\n"
"v_permlane32_swap_b32 v71, v75\n"
"v_permlane16_swap_b32 v68, v72\n"
"v_permlane16_swap_b32 v69, v73\n"
"v_permlane16_swap_b32 v70, v74\n"
"v_permlane16_swap_b32 v71, v75\n"
"buffer_store_dwordx4 v[68:71], v88, s[40:43], s82 offen offset:3072 sc1\n"
"buffer_store_dwordx4 v[72:75], v89, s[40:43], s82 offen offset:3072 sc1\n"
"s_waitcnt lgkmcnt(0)\n"
"v_min_f32 v170, v132, v152\n"
"v_sub_f32 v171, v170, v132\n"
"v_sub_f32 v172, v170, v152\n"
"v_exp_f32 v171, v171\n"
"v_exp_f32 v172, v172\n"
"s_nop 0\n"
"v_mul_f32 v173, v133, v171\n"
"v_fma_f32 v173, v153, v172, v173\n"
"v_rcp_f32 v173, v173\n"
"s_nop 0\n"
"v_mul_f32 v171, v171, v173\n"
"v_mul_f32 v172, v172, v173\n"
"v_mul_f32 v136, v136, v171\n"
"v_fma_f32 v136, v154, v172, v136\n"
"v_mul_f32 v137, v137, v171\n"
"v_fma_f32 v137, v155, v172, v137\n"
"v_mul_f32 v138, v138, v171\n"
"v_fma_f32 v138, v156, v172, v138\n"
"v_mul_f32 v139, v139, v171\n"
"v_fma_f32 v139, v157, v172, v139\n"
"v_mul_f32 v140, v140, v171\n"
"v_fma_f32 v140, v158, v172, v140\n"
"v_mul_f32 v141, v141, v171\n"
"v_fma_f32 v141, v159, v172, v141\n"
"v_mul_f32 v142, v142, v171\n"
"v_fma_f32 v142, v160, v172, v142\n"
"v_mul_f32 v143, v143, v171\n"
"v_fma_f32 v143, v161, v172, v143\n"
"v_mul_f32 v144, v144, v171\n"
"v_fma_f32 v144, v162, v172, v144\n"
"v_mul_f32 v145, v145, v171\n"
"v_fma_f32 v145, v163, v172, v145\n"
"v_mul_f32 v146, v146, v171\n"
"v_fma_f32 v146, v164, v172, v146\n"
"v_mul_f32 v147, v147, v171\n"
"v_fma_f32 v147, v165, v172, v147\n"
"v_mul_f32 v148, v148, v171\n"
"v_fma_f32 v148, v166, v172, v148\n"
"v_mul_f32 v149, v149, v171\n"
"v_fma_f32 v149, v167, v172, v149\n"
"v_mul_f32 v150, v150, v171\n"
"v_fma_f32 v150, v168, v172, v150\n"
"v_mul_f32 v151, v151, v171\n"
"v_fma_f32 v151, v169, v172, v151\n"
"s_lshl_b32 s22, s17, 11\n"
"s_add_u32 s23, s82, s22\n"
"v_mov_b32 v128, s16\n"
"v_mul_u32_u24 v128, 0x1000, v128\n"
"v_add_u32 v128, 0x26000, v128\n"
"v_mov_b32 v253, s17\n"
"v_mul_u32_u24 v253, 0x800, v253\n"
"v_add_u32 v128, v253, v128\n"
"v_lshlrev_b32 v253, 4, v247\n"
"v_add_u32 v128, v253, v128\n"
"v_mov_b32 v129, s16\n"
"v_mul_u32_u24 v129, 0x1000, v129\n"
"v_add_u32 v129, 0x26000, v129\n"
"v_lshlrev_b32 v253, 4, v247\n"
"v_add_u32 v129, v253, v129\n"
"v_cvt_pk_bf16_f32 v176, v136, v137\n"
"v_cvt_pk_bf16_f32 v177, v138, v139\n"
"v_cvt_pk_bf16_f32 v178, v140, v141\n"
"v_cvt_pk_bf16_f32 v179, v142, v143\n"
"buffer_store_dwordx4 v[176:179], v90, s[40:43], s23 offen offset:0 sc1\n"
"ds_write_b128 v128, v[176:179] offset:0\n"
"v_cvt_pk_bf16_f32 v180, v144, v145\n"
"v_cvt_pk_bf16_f32 v181, v146, v147\n"
"v_cvt_pk_bf16_f32 v182, v148, v149\n"
"v_cvt_pk_bf16_f32 v183, v150, v151\n"
"buffer_store_dwordx4 v[180:183], v90, s[40:43], s23 offen offset:1024 sc1\n"
"ds_write_b128 v128, v[180:183] offset:1024\n"
"s_waitcnt vmcnt(43) lgkmcnt(0)\n"
"s_barrier\n"
"ds_read_b128 v[16:19], v129 offset:0\n"
"ds_read_b128 v[36:39], v129 offset:1024\n"
"ds_read_b128 v[56:59], v129 offset:2048\n"
"ds_read_b128 v[76:79], v129 offset:3072\n"
"ds_read_b64_tr_b16 a[180:181], v80 offset:0\n"
"ds_read_b64_tr_b16 a[182:183], v80 offset:2560\n"
"ds_read_b64_tr_b16 a[184:185], v81 offset:0\n"
"ds_read_b64_tr_b16 a[186:187], v81 offset:2560\n"
"ds_read_b64_tr_b16 a[188:189], v80 offset:512\n"
"ds_read_b64_tr_b16 a[190:191], v80 offset:3072\n"
"ds_read_b64_tr_b16 a[192:193], v81 offset:512\n"
"ds_read_b64_tr_b16 a[194:195], v81 offset:3072\n"
"ds_read_b64_tr_b16 a[196:197], v80 offset:1024\n"
"ds_read_b64_tr_b16 a[198:199], v80 offset:3584\n"
"ds_read_b64_tr_b16 a[200:201], v82 offset:1024\n"
"s_waitcnt lgkmcnt(14)\n"
"ds_read_b64_tr_b16 a[202:203], v82 offset:3584\n"
"s_waitcnt lgkmcnt(14)\n"
"ds_read_b64_tr_b16 a[204:205], v83 offset:1536\n"
"s_waitcnt lgkmcnt(14)\n"
"ds_read_b64_tr_b16 a[206:207], v83 offset:4096\n"
"s_waitcnt lgkmcnt(14)\n"
"ds_read_b64_tr_b16 a[208:209], v82 offset:1536\n"
"s_waitcnt lgkmcnt(14)\n"
"ds_read_b64_tr_b16 a[210:211], v82 offset:4096\n"
"s_waitcnt lgkmcnt(14)\n"
"ds_read_b64_tr_b16 a[212:213], v83 offset:2048\n"
"s_waitcnt lgkmcnt(14)\n"
"ds_read_b64_tr_b16 a[214:215], v83 offset:4608\n"
"v_mfma_f32_16x16x32_bf16 a[0:3], a[180:183], v[0:3], 0\n"
"s_waitcnt lgkmcnt(14)\n"
"ds_read_b64_tr_b16 a[216:217], v82 offset:2048\n"
"v_mfma_f32_16x16x32_bf16 a[4:7], a[180:183], v[4:7], 0\n"
"s_waitcnt lgkmcnt(14)\n"
"ds_read_b64_tr_b16 a[218:219], v82 offset:4608\n"
"v_mfma_f32_16x16x32_bf16 a[8:11], a[180:183], v[8:11], 0\n"
"v_mfma_f32_16x16x32_bf16 a[12:15], a[180:183], v[12:15], 0\n"
"v_mfma_f32_16x16x32_bf16 a[160:163], a[180:183], v[16:19], 0\n"
"v_mfma_f32_16x16x32_bf16 a[16:19], a[184:187], v[0:3], 0\n"
"v_mfma_f32_16x16x32_bf16 a[20:23], a[184:187], v[4:7], 0\n"
"v_mfma_f32_16x16x32_bf16 a[24:27], a[184:187], v[8:11], 0\n"
"v_mfma_f32_16x16x32_bf16 a[28:31], a[184:187], v[12:15], 0\n"
"v_mfma_f32_16x16x32_bf16 a[164:167], a[184:187], v[16:19], 0\n"
"s_waitcnt lgkmcnt(14)\n"
"v_mfma_f32_16x16x32_bf16 a[32:35], a[188:191], v[0:3], 0\n"
"v_mfma_f32_16x16x32_bf16 a[36:39], a[188:191], v[4:7], 0\n"
"v_mfma_f32_16x16x32_bf16 a[40:43], a[188:191], v[8:11], 0\n"
"v_mfma_f32_16x16x32_bf16 a[44:47], a[188:191], v[12:15], 0\n"
"v_mfma_f32_16x16x32_bf16 a[168:171], a[188:191], v[16:19], 0\n"
"s_waitcnt lgkmcnt(0)\n"
"v_mfma_f32_16x16x32_bf16 a[48:51], a[192:195], v[0:3], 0\n"
"v_mfma_f32_16x16x32_bf16 a[52:55], a[192:195], v[4:7], 0\n"
"v_mfma_f32_16x16x32_bf16 a[56:59], a[192:195], v[8:11], 0\n"
"v_mfma_f32_16x16x32_bf16 a[60:63], a[192:195], v[12:15], 0\n"
"v_mfma_f32_16x16x32_bf16 a[172:175], a[192:195], v[16:19], 0\n"
"v_mfma_f32_16x16x32_bf16 a[64:67], a[196:199], v[0:3], 0\n"
"v_mfma_f32_16x16x32_bf16 a[68:71], a[196:199], v[4:7], 0\n"
"v_mfma_f32_16x16x32_bf16 a[72:75], a[196:199], v[8:11], 0\n"
"v_mfma_f32_16x16x32_bf16 a[76:79], a[196:199], v[12:15], 0\n"
"v_mfma_f32_16x16x32_bf16 a[176:179], a[196:199], v[16:19], 0\n"
"s_waitcnt vmcnt(0) lgkmcnt(0)\n"
"s_barrier\n"
"s_cmp_lg_u32 s15, 0\n"
"s_cbranch_scc1 Lpub1_%=\n"
"s_lshl_b32 s21, s18, 3\n"
"s_add_u32 s21, s21, s19\n"
"s_lshl_b32 s21, s21, 4\n"
"s_add_u32 s21, s21, s20\n"
"s_lshl_b32 s21, s21, 3\n"
"v_mov_b32 v218, s21\n"
"v_mov_b32 v220, s92\n"
"v_mov_b32 v221, s93\n"
"s_mov_b64 exec, 1\n"
"global_store_dwordx2 v218, v[220:221], s[94:95] sc1\n"
"s_mov_b64 exec, -1\n"
"Lpub1_%=:\n"
"v_add_u32 v80, 0x5000, v80\n"
"v_add_u32 v81, 0x5000, v81\n"
"ds_read_b64_tr_b16 a[180:181], v80 offset:0\n"
"s_add_u32 s78, s80, 1\n"
"s_and_b32 s78, s78, 3\n"
"v_mfma_f32_16x16x32_bf16 a[80:83], a[200:203], v[0:3], 0\n"
"s_mul_i32 s78, s78, 327680\n"
"s_add_u32 s78, s78, s75\n"
"ds_read_b64_tr_b16 a[182:183], v80 offset:2560\n"
"s_add_u32 m0, s76, 0x1f000\n"
"s_add_u32 s79, s78, 0x14000\n"
"v_mfma_f32_16x16x32_bf16 a[84:87], a[200:203], v[4:7], 0\n"
"ds_read_b64_tr_b16 a[184:185], v81 offset:0\n"
"buffer_load_dwordx4 v84, s[24:27], s79 offen lds\n"
"v_add_u32 v82, 0x5000, v82\n"
"v_add_u32 v83, 0x5000, v83\n"
"v_mfma_f32_16x16x32_bf16 a[88:91], a[200:203], v[8:11], 0\n"
"ds_read_b64_tr_b16 a[186:187], v81 offset:2560\n"
"v_mfma_f32_16x16x32_bf16 a[92:95], a[200:203], v[12:15], 0\n"
"ds_read_b64_tr_b16 a[188:189], v80 offset:512\n"
"v_mfma_f32_16x16x32_bf16 a[96:99], a[204:207], v[0:3], 0\n"
"s_add_u32 m0, s76, 0x1f400\n"
"s_add_u32 s79, s78, 0x14080\n"
"ds_read_b64_tr_b16 a[190:191], v80 offset:3072\n"
"v_mfma_f32_16x16x32_bf16 a[100:103], a[204:207], v[4:7], 0\n"
"buffer_load_dwordx4 v84, s[24:27], s79 offen lds\n"
"ds_read_b64_tr_b16 a[192:193], v81 offset:512\n"
"v_mfma_f32_16x16x32_bf16 a[104:107], a[204:207], v[8:11], 0\n"
"ds_read_b64_tr_b16 a[194:195], v81 offset:3072\n"
"v_mfma_f32_16x16x32_bf16 a[108:111], a[204:207], v[12:15], 0\n"
"ds_read_b64_tr_b16 a[196:197], v80 offset:1024\n"
"s_add_u32 m0, s76, 0x1f800\n"
"s_add_u32 s79, s78, 0x14100\n"
"v_mfma_f32_16x16x32_bf16 a[112:115], a[208:211], v[0:3], 0\n"
"ds_read_b64_tr_b16 a[198:199], v80 offset:3584\n"
"buffer_load_dwordx4 v85, s[24:27], s79 offen lds\n"
"v_mfma_f32_16x16x32_bf16 a[116:119], a[208:211], v[4:7], 0\n"
"ds_read_b64_tr_b16 a[200:201], v82 offset:1024\n"
"v_mfma_f32_16x16x32_bf16 a[120:123], a[208:211], v[8:11], 0\n"
"ds_read_b64_tr_b16 a[202:203], v82 offset:3584\n"
"v_mfma_f32_16x16x32_bf16 a[124:127], a[208:211], v[12:15], 0\n"
"ds_read_b64_tr_b16 a[204:205], v83 offset:1536\n"
"s_add_u32 m0, s76, 0x1fc00\n"
"s_add_u32 s79, s78, 0x16840\n"
"v_mfma_f32_16x16x32_bf16 a[128:131], a[212:215], v[0:3], 0\n"
"buffer_load_dwordx4 v84, s[24:27], s79 offen lds\n"
"ds_read_b64_tr_b16 a[206:207], v83 offset:4096\n"
"v_mfma_f32_16x16x32_bf16 a[132:135], a[212:215], v[4:7], 0\n"
"ds_read_b64_tr_b16 a[208:209], v82 offset:1536\n"
"v_mfma_f32_16x16x32_bf16 a[136:139], a[212:215], v[8:11], 0\n"
"s_waitcnt lgkmcnt(10)\n"
"ds_read_b64_tr_b16 a[210:211], v82 offset:4096\n"
"v_mfma_f32_16x16x32_bf16 a[140:143], a[212:215], v[12:15], 0\n"
"s_add_u32 m0, s76, 0x20000\n"
"s_add_u32 s79, s78, 0x168c0\n"
"buffer_load_dwordx4 v84, s[24:27], s79 offen lds\n"
"v_mfma_f32_16x16x32_bf16 a[144:147], a[216:219], v[0:3], 0\n"
"v_mfma_f32_16x16x32_bf16 a[148:151], a[216:219], v[4:7], 0\n"
"ds_read_b64_tr_b16 a[212:213], v83 offset:2048\n"
"v_mfma_f32_16x16x32_bf16 a[152:155], a[216:219], v[8:11], 0\n"
"ds_read_b64_tr_b16 a[214:215], v83 offset:4608\n"
"v_mfma_f32_16x16x32_bf16 a[156:159], a[216:219], v[12:15], 0\n"
"v_mfma_f32_16x16x32_bf16 a[0:3], a[180:183], v[20:23], a[0:3]\n"
"ds_read_b64_tr_b16 a[216:217], v82 offset:2048\n"
"v_mfma_f32_16x16x32_bf16 a[4:7], a[180:183], v[24:27], a[4:7]\n"
"ds_read_b64_tr_b16 a[218:219], v82 offset:4608\n"
"s_add_u32 s78, s80, 1\n"
"s_and_b32 s78, s78, 3\n"
"s_mul_i32 s78, s78, 327680\n"
"v_mfma_f32_16x16x32_bf16 a[8:11], a[180:183], v[28:31], a[8:11]\n"
"s_add_u32 s78, s78, s75\n"
"s_add_u32 m0, s76, 0x1000\n"
"s_add_u32 s79, s78, 0x1e000\n"
"v_mfma_f32_16x16x32_bf16 a[12:15], a[180:183], v[32:35], a[12:15]\n"
"buffer_load_dwordx4 v84, s[24:27], s79 offen lds\n"
"v_mfma_f32_16x16x32_bf16 a[160:163], a[180:183], v[36:39], a[160:163]\n"
"v_mfma_f32_16x16x32_bf16 a[16:19], a[184:187], v[20:23], a[16:19]\n"
"v_mfma_f32_16x16x32_bf16 a[20:23], a[184:187], v[24:27], a[20:23]\n"
"s_add_u32 m0, s76, 0x1400\n"
"s_add_u32 s79, s78, 0x1e080\n"
"v_mfma_f32_16x16x32_bf16 a[24:27], a[184:187], v[28:31], a[24:27]\n"
"buffer_load_dwordx4 v84, s[24:27], s79 offen lds\n"
"v_mfma_f32_16x16x32_bf16 a[28:31], a[184:187], v[32:35], a[28:31]\n"
"v_mfma_f32_16x16x32_bf16 a[164:167], a[184:187], v[36:39], a[164:167]\n"
"s_waitcnt lgkmcnt(2)\n"
"v_mfma_f32_16x16x32_bf16 a[32:35], a[188:191], v[20:23], a[32:35]\n"
"s_add_u32 m0, s76, 0x1800\n"
"s_add_u32 s79, s78, 0x1e100\n"
"v_mfma_f32_16x16x32_bf16 a[36:39], a[188:191], v[24:27], a[36:39]\n"
"buffer_load_dwordx4 v85, s[24:27], s79 offen lds\n"
"v_mfma_f32_16x16x32_bf16 a[40:43], a[188:191], v[28:31], a[40:43]\n"
"v_mfma_f32_16x16x32_bf16 a[44:47], a[188:191], v[32:35], a[44:47]\n"
"v_mfma_f32_16x16x32_bf16 a[168:171], a[188:191], v[36:39], a[168:171]\n"
"v_mfma_f32_16x16x32_bf16 a[48:51], a[192:195], v[20:23], a[48:51]\n"
"s_add_u32 m0, s76, 0x1c00\n"
"s_add_u32 s79, s78, 0x20840\n"
"buffer_load_dwordx4 v84, s[24:27], s79 offen lds\n"
"v_mfma_f32_16x16x32_bf16 a[52:55], a[192:195], v[24:27], a[52:55]\n"
"v_mfma_f32_16x16x32_bf16 a[56:59], a[192:195], v[28:31], a[56:59]\n"
"v_mfma_f32_16x16x32_bf16 a[60:63], a[192:195], v[32:35], a[60:63]\n"
"v_mfma_f32_16x16x32_bf16 a[172:175], a[192:195], v[36:39], a[172:175]\n"
"s_add_u32 m0, s76, 0x2000\n"
"s_add_u32 s79, s78, 0x208c0\n"
"v_mfma_f32_16x16x32_bf16 a[64:67], a[196:199], v[20:23], a[64:67]\n"
"buffer_load_dwordx4 v84, s[24:27], s79 offen lds\n"
"v_mfma_f32_16x16x32_bf16 a[68:71], a[196:199], v[24:27], a[68:71]\n"
"v_mfma_f32_16x16x32_bf16 a[72:75], a[196:199], v[28:31], a[72:75]\n"
"v_mfma_f32_16x16x32_bf16 a[76:79], a[196:199], v[32:35], a[76:79]\n"
"v_mfma_f32_16x16x32_bf16 a[176:179], a[196:199], v[36:39], a[176:179]\n"
"s_waitcnt vmcnt(63) lgkmcnt(0)\n"
"s_barrier\n"
"v_add_u32 v80, 0x5000, v80\n"
"v_add_u32 v81, 0x5000, v81\n"
"ds_read_b64_tr_b16 a[180:181], v80 offset:0\n"
"v_mfma_f32_16x16x32_bf16 a[80:83], a[200:203], v[20:23], a[80:83]\n"
"ds_read_b64_tr_b16 a[182:183], v80 offset:2560\n"
"v_mfma_f32_16x16x32_bf16 a[84:87], a[200:203], v[24:27], a[84:87]\n"
"ds_read_b64_tr_b16 a[184:185], v81 offset:0\n"
"v_add_u32 v82, 0x5000, v82\n"
"v_add_u32 v83, 0x5000, v83\n"
"v_mfma_f32_16x16x32_bf16 a[88:91], a[200:203], v[28:31], a[88:91]\n"
"ds_read_b64_tr_b16 a[186:187], v81 offset:2560\n"
"v_mfma_f32_16x16x32_bf16 a[92:95], a[200:203], v[32:35], a[92:95]\n"
"ds_read_b64_tr_b16 a[188:189], v80 offset:512\n"
"v_mfma_f32_16x16x32_bf16 a[96:99], a[204:207], v[20:23], a[96:99]\n"
"ds_read_b64_tr_b16 a[190:191], v80 offset:3072\n"
"v_mfma_f32_16x16x32_bf16 a[100:103], a[204:207], v[24:27], a[100:103]\n"
"ds_read_b64_tr_b16 a[192:193], v81 offset:512\n"
"v_mfma_f32_16x16x32_bf16 a[104:107], a[204:207], v[28:31], a[104:107]\n"
"ds_read_b64_tr_b16 a[194:195], v81 offset:3072\n"
"v_mfma_f32_16x16x32_bf16 a[108:111], a[204:207], v[32:35], a[108:111]\n"
"ds_read_b64_tr_b16 a[196:197], v80 offset:1024\n"
"v_mfma_f32_16x16x32_bf16 a[112:115], a[208:211], v[20:23], a[112:115]\n"
"ds_read_b64_tr_b16 a[198:199], v80 offset:3584\n"
"v_mfma_f32_16x16x32_bf16 a[116:119], a[208:211], v[24:27], a[116:119]\n"
"ds_read_b64_tr_b16 a[200:201], v82 offset:1024\n"
"v_mfma_f32_16x16x32_bf16 a[120:123], a[208:211], v[28:31], a[120:123]\n"
"ds_read_b64_tr_b16 a[202:203], v82 offset:3584\n"
"v_mfma_f32_16x16x32_bf16 a[124:127], a[208:211], v[32:35], a[124:127]\n"
"ds_read_b64_tr_b16 a[204:205], v83 offset:1536\n"
"v_mfma_f32_16x16x32_bf16 a[128:131], a[212:215], v[20:23], a[128:131]\n"
"ds_read_b64_tr_b16 a[206:207], v83 offset:4096\n"
"v_mfma_f32_16x16x32_bf16 a[132:135], a[212:215], v[24:27], a[132:135]\n"
"ds_read_b64_tr_b16 a[208:209], v82 offset:1536\n"
"v_mfma_f32_16x16x32_bf16 a[136:139], a[212:215], v[28:31], a[136:139]\n"
"s_waitcnt lgkmcnt(11)\n"
"ds_read_b64_tr_b16 a[210:211], v82 offset:4096\n"
"v_mfma_f32_16x16x32_bf16 a[140:143], a[212:215], v[32:35], a[140:143]\n"
"v_mfma_f32_16x16x32_bf16 a[144:147], a[216:219], v[20:23], a[144:147]\n"
"v_mfma_f32_16x16x32_bf16 a[148:151], a[216:219], v[24:27], a[148:151]\n"
"ds_read_b64_tr_b16 a[212:213], v83 offset:2048\n"
"v_mfma_f32_16x16x32_bf16 a[152:155], a[216:219], v[28:31], a[152:155]\n"
"ds_read_b64_tr_b16 a[214:215], v83 offset:4608\n"
"v_mfma_f32_16x16x32_bf16 a[156:159], a[216:219], v[32:35], a[156:159]\n"
"v_mfma_f32_16x16x32_bf16 a[0:3], a[180:183], v[40:43], a[0:3]\n"
"ds_read_b64_tr_b16 a[216:217], v82 offset:2048\n"
"v_mfma_f32_16x16x32_bf16 a[4:7], a[180:183], v[44:47], a[4:7]\n"
"s_waitcnt lgkmcnt(8)\n"
"ds_read_b64_tr_b16 a[218:219], v82 offset:4608\n"
"s_add_u32 s78, s80, 2\n"
"s_and_b32 s78, s78, 3\n"
"s_mul_i32 s78, s78, 327680\n"
"v_mfma_f32_16x16x32_bf16 a[8:11], a[180:183], v[48:51], a[8:11]\n"
"s_add_u32 s78, s78, s75\n"
"s_add_u32 m0, s76, 0x6000\n"
"s_add_u32 s79, s78, 0x0\n"
"v_mfma_f32_16x16x32_bf16 a[12:15], a[180:183], v[52:55], a[12:15]\n"
"buffer_load_dwordx4 v84, s[24:27], s79 offen lds\n"
"v_mfma_f32_16x16x32_bf16 a[160:163], a[180:183], v[56:59], a[160:163]\n"
"v_mfma_f32_16x16x32_bf16 a[16:19], a[184:187], v[40:43], a[16:19]\n"
"v_mfma_f32_16x16x32_bf16 a[20:23], a[184:187], v[44:47], a[20:23]\n"
"s_add_u32 m0, s76, 0x6400\n"
"s_add_u32 s79, s78, 0x80\n"
"v_mfma_f32_16x16x32_bf16 a[24:27], a[184:187], v[48:51], a[24:27]\n"
"buffer_load_dwordx4 v84, s[24:27], s79 offen lds\n"
"v_mfma_f32_16x16x32_bf16 a[28:31], a[184:187], v[52:55], a[28:31]\n"
"v_mfma_f32_16x16x32_bf16 a[164:167], a[184:187], v[56:59], a[164:167]\n"
"v_mfma_f32_16x16x32_bf16 a[32:35], a[188:191], v[40:43], a[32:35]\n"
"s_add_u32 m0, s76, 0x6800\n"
"s_add_u32 s79, s78, 0x100\n"
"v_mfma_f32_16x16x32_bf16 a[36:39], a[188:191], v[44:47], a[36:39]\n"
"buffer_load_dwordx4 v85, s[24:27], s79 offen lds\n"
"v_mfma_f32_16x16x32_bf16 a[40:43], a[188:191], v[48:51], a[40:43]\n"
"v_mfma_f32_16x16x32_bf16 a[44:47], a[188:191], v[52:55], a[44:47]\n"
"v_mfma_f32_16x16x32_bf16 a[168:171], a[188:191], v[56:59], a[168:171]\n"
"v_mfma_f32_16x16x32_bf16 a[48:51], a[192:195], v[40:43], a[48:51]\n"
"s_add_u32 m0, s76, 0x6c00\n"
"s_add_u32 s79, s78, 0x2840\n"
"buffer_load_dwordx4 v84, s[24:27], s79 offen lds\n"
"v_mfma_f32_16x16x32_bf16 a[52:55], a[192:195], v[44:47], a[52:55]\n"
"v_mfma_f32_16x16x32_bf16 a[56:59], a[192:195], v[48:51], a[56:59]\n"
"v_mfma_f32_16x16x32_bf16 a[60:63], a[192:195], v[52:55], a[60:63]\n"
"v_mfma_f32_16x16x32_bf16 a[172:175], a[192:195], v[56:59], a[172:175]\n"
"s_add_u32 m0, s76, 0x7000\n"
"s_add_u32 s79, s78, 0x28c0\n"
"v_mfma_f32_16x16x32_bf16 a[64:67], a[196:199], v[40:43], a[64:67]\n"
"buffer_load_dwordx4 v84, s[24:27], s79 offen lds\n"
"v_mfma_f32_16x16x32_bf16 a[68:71], a[196:199], v[44:47], a[68:71]\n"
"v_mfma_f32_16x16x32_bf16 a[72:75], a[196:199], v[48:51], a[72:75]\n"
"v_mfma_f32_16x16x32_bf16 a[76:79], a[196:199], v[52:55], a[76:79]\n"
"v_mfma_f32_16x16x32_bf16 a[176:179], a[196:199], v[56:59], a[176:179]\n"
"s_waitcnt vmcnt(63) lgkmcnt(0)\n"
"s_barrier\n"
"v_add_u32 v80, 0x5000, v80\n"
"v_add_u32 v81, 0x5000, v81\n"
"ds_read_b64_tr_b16 a[180:181], v80 offset:0\n"
"v_mfma_f32_16x16x32_bf16 a[80:83], a[200:203], v[40:43], a[80:83]\n"
"ds_read_b64_tr_b16 a[182:183], v80 offset:2560\n"
"v_mfma_f32_16x16x32_bf16 a[84:87], a[200:203], v[44:47], a[84:87]\n"
"ds_read_b64_tr_b16 a[184:185], v81 offset:0\n"
"v_add_u32 v82, 0x5000, v82\n"
"v_add_u32 v83, 0x5000, v83\n"
"v_mfma_f32_16x16x32_bf16 a[88:91], a[200:203], v[48:51], a[88:91]\n"
"ds_read_b64_tr_b16 a[186:187], v81 offset:2560\n"
"v_mfma_f32_16x16x32_bf16 a[92:95], a[200:203], v[52:55], a[92:95]\n"
"ds_read_b64_tr_b16 a[188:189], v80 offset:512\n"
"v_mfma_f32_16x16x32_bf16 a[96:99], a[204:207], v[40:43], a[96:99]\n"
"ds_read_b64_tr_b16 a[190:191], v80 offset:3072\n"
"v_mfma_f32_16x16x32_bf16 a[100:103], a[204:207], v[44:47], a[100:103]\n"
"ds_read_b64_tr_b16 a[192:193], v81 offset:512\n"
"v_mfma_f32_16x16x32_bf16 a[104:107], a[204:207], v[48:51], a[104:107]\n"
"ds_read_b64_tr_b16 a[194:195], v81 offset:3072\n"
"v_mfma_f32_16x16x32_bf16 a[108:111], a[204:207], v[52:55], a[108:111]\n"
"ds_read_b64_tr_b16 a[196:197], v80 offset:1024\n"
"v_mfma_f32_16x16x32_bf16 a[112:115], a[208:211], v[40:43], a[112:115]\n"
"ds_read_b64_tr_b16 a[198:199], v80 offset:3584\n"
"v_mfma_f32_16x16x32_bf16 a[116:119], a[208:211], v[44:47], a[116:119]\n"
"ds_read_b64_tr_b16 a[200:201], v82 offset:1024\n"
"v_mfma_f32_16x16x32_bf16 a[120:123], a[208:211], v[48:51], a[120:123]\n"
"ds_read_b64_tr_b16 a[202:203], v82 offset:3584\n"
"v_mfma_f32_16x16x32_bf16 a[124:127], a[208:211], v[52:55], a[124:127]\n"
"ds_read_b64_tr_b16 a[204:205], v83 offset:1536\n"
"v_mfma_f32_16x16x32_bf16 a[128:131], a[212:215], v[40:43], a[128:131]\n"
"ds_read_b64_tr_b16 a[206:207], v83 offset:4096\n"
"v_mfma_f32_16x16x32_bf16 a[132:135], a[212:215], v[44:47], a[132:135]\n"
"ds_read_b64_tr_b16 a[208:209], v82 offset:1536\n"
"v_mfma_f32_16x16x32_bf16 a[136:139], a[212:215], v[48:51], a[136:139]\n"
"s_waitcnt lgkmcnt(11)\n"
"ds_read_b64_tr_b16 a[210:211], v82 offset:4096\n"
"v_mfma_f32_16x16x32_bf16 a[140:143], a[212:215], v[52:55], a[140:143]\n"
"v_mfma_f32_16x16x32_bf16 a[144:147], a[216:219], v[40:43], a[144:147]\n"
"v_mfma_f32_16x16x32_bf16 a[148:151], a[216:219], v[44:47], a[148:151]\n"
"ds_read_b64_tr_b16 a[212:213], v83 offset:2048\n"
"v_mfma_f32_16x16x32_bf16 a[152:155], a[216:219], v[48:51], a[152:155]\n"
"ds_read_b64_tr_b16 a[214:215], v83 offset:4608\n"
"v_mfma_f32_16x16x32_bf16 a[156:159], a[216:219], v[52:55], a[156:159]\n"
"s_lshl_b32 s21, s18, 3\n"
"s_add_u32 s21, s21, s19\n"
"s_lshl_b32 s21, s21, 2\n"
"s_add_u32 s21, s21, s81\n"
"s_lshl_b32 s21, s21, 5\n"
"v_and_b32 v218, 3, v247\n"
"v_lshlrev_b32 v218, 3, v218\n"
"v_add_u32 v218, s21, v218\n"
"s_mov_b32 s73, 131072\n"
"Lpw_%=:\n"
"global_load_dwordx2 v[220:221], v218, s[94:95] sc1\n"
"s_waitcnt vmcnt(0)\n"
"v_cmp_eq_u64 vcc, s[92:93], v[220:221]\n"
"s_nop 1\n"
"s_mov_b64 s[90:91], vcc\n"
"s_cmp_eq_u64 s[90:91], exec\n"
"s_cbranch_scc1 Lpwd_%=\n"
"s_sleep 2\n"
"s_sub_u32 s73, s73, 1\n"
"s_cmp_lg_u32 s73, 0\n"
"s_cbranch_scc1 Lpw_%=\n"
"Lpwd_%=:\n"
"s_add_u32 s77, s80, 1\n"
"s_and_b32 s77, s77, 3\n"
"s_mul_i32 s77, s77, 8192\n"
"s_add_u32 s77, s77, s74\n"
"buffer_load_dwordx4 v[0:3], v86, s[40:43], s77 offen offset:0 sc1\n"
"buffer_load_dwordx4 v[4:7], v87, s[40:43], s77 offen offset:0 sc1\n"
"buffer_load_dwordx4 v[8:11], v88, s[40:43], s77 offen offset:0 sc1\n"
"buffer_load_dwordx4 v[12:15], v89, s[40:43], s77 offen offset:0 sc1\n"
"buffer_load_dwordx4 v[16:19], v90, s[40:43], s77 offen offset:0 sc1\n"
"s_add_u32 s77, s80, 1\n"
"s_and_b32 s77, s77, 3\n"
"s_mul_i32 s77, s77, 8192\n"
"s_add_u32 s77, s77, s74\n"
"buffer_load_dwordx4 v[20:23], v86, s[40:43], s77 offen offset:1024 sc1\n"
"buffer_load_dwordx4 v[24:27], v87, s[40:43], s77 offen offset:1024 sc1\n"
"buffer_load_dwordx4 v[28:31], v88, s[40:43], s77 offen offset:1024 sc1\n"
"buffer_load_dwordx4 v[32:35], v89, s[40:43], s77 offen offset:1024 sc1\n"
"buffer_load_dwordx4 v[36:39], v90, s[40:43], s77 offen offset:1024 sc1\n"
"v_mfma_f32_16x16x32_bf16 a[0:3], a[180:183], v[60:63], a[0:3]\n"
"ds_read_b64_tr_b16 a[216:217], v82 offset:2048\n"
"v_mfma_f32_16x16x32_bf16 a[4:7], a[180:183], v[64:67], a[4:7]\n"
"s_waitcnt lgkmcnt(4)\n"
"ds_read_b64_tr_b16 a[218:219], v82 offset:4608\n"
"s_add_u32 s78, s80, 2\n"
"s_and_b32 s78, s78, 3\n"
"s_mul_i32 s78, s78, 327680\n"
"v_mfma_f32_16x16x32_bf16 a[8:11], a[180:183], v[68:71], a[8:11]\n"
"s_add_u32 s78, s78, s75\n"
"s_add_u32 m0, s76, 0xb000\n"
"s_add_u32 s79, s78, 0xa000\n"
"s_add_u32 s77, s80, 1\n"
"v_mfma_f32_16x16x32_bf16 a[12:15], a[180:183], v[72:75], a[12:15]\n"
"s_and_b32 s77, s77, 3\n"
"buffer_load_dwordx4 v84, s[24:27], s79 offen lds\n"
"s_mul_i32 s77, s77, 8192\n"
"v_mfma_f32_16x16x32_bf16 a[160:163], a[180:183], v[76:79], a[160:163]\n"
"s_add_u32 s77, s77, s74\n"
"v_mfma_f32_16x16x32_bf16 a[16:19], a[184:187], v[60:63], a[16:19]\n"
"buffer_load_dwordx4 v[40:43], v86, s[40:43], s77 offen offset:2048 sc1\n"
"v_mfma_f32_16x16x32_bf16 a[20:23], a[184:187], v[64:67], a[20:23]\n"
"s_add_u32 m0, s76, 0xb400\n"
"s_add_u32 s79, s78, 0xa080\n"
"v_mfma_f32_16x16x32_bf16 a[24:27], a[184:187], v[68:71], a[24:27]\n"
"buffer_load_dwordx4 v84, s[24:27], s79 offen lds\n"
"v_mfma_f32_16x16x32_bf16 a[28:31], a[184:187], v[72:75], a[28:31]\n"
"v_mfma_f32_16x16x32_bf16 a[164:167], a[184:187], v[76:79], a[164:167]\n"
"buffer_load_dwordx4 v[44:47], v87, s[40:43], s77 offen offset:2048 sc1\n"
"v_mfma_f32_16x16x32_bf16 a[32:35], a[188:191], v[60:63], a[32:35]\n"
"s_add_u32 m0, s76, 0xb800\n"
"s_add_u32 s79, s78, 0xa100\n"
"v_mfma_f32_16x16x32_bf16 a[36:39], a[188:191], v[64:67], a[36:39]\n"
"buffer_load_dwordx4 v85, s[24:27], s79 offen lds\n"
"v_mfma_f32_16x16x32_bf16 a[40:43], a[188:191], v[68:71], a[40:43]\n"
"buffer_load_dwordx4 v[48:51], v88, s[40:43], s77 offen offset:2048 sc1\n"
"v_mfma_f32_16x16x32_bf16 a[44:47], a[188:191], v[72:75], a[44:47]\n"
"v_mfma_f32_16x16x32_bf16 a[168:171], a[188:191], v[76:79], a[168:171]\n"
"v_mfma_f32_16x16x32_bf16 a[48:51], a[192:195], v[60:63], a[48:51]\n"
"s_add_u32 m0, s76, 0xbc00\n"
"s_add_u32 s79, s78, 0xc840\n"
"buffer_load_dwordx4 v84, s[24:27], s79 offen lds\n"
"v_mfma_f32_16x16x32_bf16 a[52:55], a[192:195], v[64:67], a[52:55]\n"
"buffer_load_dwordx4 v[52:55], v89, s[40:43], s77 offen offset:2048 sc1\n"
"v_mfma_f32_16x16x32_bf16 a[56:59], a[192:195], v[68:71], a[56:59]\n"
"v_mfma_f32_16x16x32_bf16 a[60:63], a[192:195], v[72:75], a[60:63]\n"
"v_mfma_f32_16x16x32_bf16 a[172:175], a[192:195], v[76:79], a[172:175]\n"
"s_add_u32 m0, s76, 0xc000\n"
"s_add_u32 s79, s78, 0xc8c0\n"
"v_mfma_f32_16x16x32_bf16 a[64:67], a[196:199], v[60:63], a[64:67]\n"
"buffer_load_dwordx4 v84, s[24:27], s79 offen lds\n"
"buffer_load_dwordx4 v[56:59], v90, s[40:43], s77 offen offset:2048 sc1\n"
"v_mfma_f32_16x16x32_bf16 a[68:71], a[196:199], v[64:67], a[68:71]\n"
"v_mfma_f32_16x16x32_bf16 a[72:75], a[196:199], v[68:71], a[72:75]\n"
"v_mfma_f32_16x16x32_bf16 a[76:79], a[196:199], v[72:75], a[76:79]\n"
"v_mfma_f32_16x16x32_bf16 a[176:179], a[196:199], v[76:79], a[176:179]\n"
"s_waitcnt vmcnt(63) lgkmcnt(0)\n"
"s_barrier\n"
"v_add_u32 v80, 0x5000, v80\n"
"v_add_u32 v81, 0x5000, v81\n"
"ds_read_b64_tr_b16 a[180:181], v80 offset:0\n"
"v_mfma_f32_16x16x32_bf16 a[80:83], a[200:203], v[60:63], a[80:83]\n"
"ds_read_b64_tr_b16 a[182:183], v80 offset:2560\n"
"v_mfma_f32_16x16x32_bf16 a[84:87], a[200:203], v[64:67], a[84:87]\n"
"ds_read_b64_tr_b16 a[184:185], v81 offset:0\n"
"v_add_u32 v82, 0x5000, v82\n"
"v_add_u32 v83, 0x5000, v83\n"
"v_mfma_f32_16x16x32_bf16 a[88:91], a[200:203], v[68:71], a[88:91]\n"
"ds_read_b64_tr_b16 a[186:187], v81 offset:2560\n"
"v_mfma_f32_16x16x32_bf16 a[92:95], a[200:203], v[72:75], a[92:95]\n"
"ds_read_b64_tr_b16 a[188:189], v80 offset:512\n"
"v_mfma_f32_16x16x32_bf16 a[96:99], a[204:207], v[60:63], a[96:99]\n"
"ds_read_b64_tr_b16 a[190:191], v80 offset:3072\n"
"v_mfma_f32_16x16x32_bf16 a[100:103], a[204:207], v[64:67], a[100:103]\n"
"ds_read_b64_tr_b16 a[192:193], v81 offset:512\n"
"v_mfma_f32_16x16x32_bf16 a[104:107], a[204:207], v[68:71], a[104:107]\n"
"ds_read_b64_tr_b16 a[194:195], v81 offset:3072\n"
"v_mfma_f32_16x16x32_bf16 a[108:111], a[204:207], v[72:75], a[108:111]\n"
"ds_read_b64_tr_b16 a[196:197], v80 offset:1024\n"
"v_mfma_f32_16x16x32_bf16 a[112:115], a[208:211], v[60:63], a[112:115]\n"
"ds_read_b64_tr_b16 a[198:199], v80 offset:3584\n"
"v_mfma_f32_16x16x32_bf16 a[116:119], a[208:211], v[64:67], a[116:119]\n"
"ds_read_b64_tr_b16 a[200:201], v82 offset:1024\n"
"v_mfma_f32_16x16x32_bf16 a[120:123], a[208:211], v[68:71], a[120:123]\n"
"ds_read_b64_tr_b16 a[202:203], v82 offset:3584\n"
"v_mfma_f32_16x16x32_bf16 a[124:127], a[208:211], v[72:75], a[124:127]\n"
"ds_read_b64_tr_b16 a[204:205], v83 offset:1536\n"
"v_mfma_f32_16x16x32_bf16 a[128:131], a[212:215], v[60:63], a[128:131]\n"
"ds_read_b64_tr_b16 a[206:207], v83 offset:4096\n"
"v_mfma_f32_16x16x32_bf16 a[132:135], a[212:215], v[64:67], a[132:135]\n"
"ds_read_b64_tr_b16 a[208:209], v82 offset:1536\n"
"v_mfma_f32_16x16x32_bf16 a[136:139], a[212:215], v[68:71], a[136:139]\n"
"s_waitcnt lgkmcnt(11)\n"
"ds_read_b64_tr_b16 a[210:211], v82 offset:4096\n"
"v_mfma_f32_16x16x32_bf16 a[140:143], a[212:215], v[72:75], a[140:143]\n"
"v_mfma_f32_16x16x32_bf16 a[144:147], a[216:219], v[60:63], a[144:147]\n"
"v_mfma_f32_16x16x32_bf16 a[148:151], a[216:219], v[64:67], a[148:151]\n"
"ds_read_b64_tr_b16 a[212:213], v83 offset:2048\n"
"v_mfma_f32_16x16x32_bf16 a[152:155], a[216:219], v[68:71], a[152:155]\n"
"ds_read_b64_tr_b16 a[214:215], v83 offset:4608\n"
"v_mfma_f32_16x16x32_bf16 a[156:159], a[216:219], v[72:75], a[156:159]\n"
"s_waitcnt vmcnt(19)\n"
"v_mfma_f32_16x16x32_bf16 a[0:3], a[180:183], v[0:3], a[0:3]\n"
"ds_read_b64_tr_b16 a[216:217], v82 offset:2048\n"
"s_waitcnt vmcnt(18)\n"
"v_mfma_f32_16x16x32_bf16 a[4:7], a[180:183], v[4:7], a[4:7]\n"
"s_waitcnt lgkmcnt(8)\n"
"ds_read_b64_tr_b16 a[218:219], v82 offset:4608\n"
"s_add_u32 s78, s80, 2\n"
"s_and_b32 s78, s78, 3\n"
"s_mul_i32 s78, s78, 327680\n"
"s_waitcnt vmcnt(17)\n"
"v_mfma_f32_16x16x32_bf16 a[8:11], a[180:183], v[8:11], a[8:11]\n"
"s_add_u32 s78, s78, s75\n"
"s_add_u32 m0, s76, 0x10000\n"
"s_add_u32 s79, s78, 0x14000\n"
"s_add_u32 s77, s80, 1\n"
"s_waitcnt vmcnt(16)\n"
"v_mfma_f32_16x16x32_bf16 a[12:15], a[180:183], v[12:15], a[12:15]\n"
"s_and_b32 s77, s77, 3\n"
"buffer_load_dwordx4 v84, s[24:27], s79 offen lds\n"
"s_mul_i32 s77, s77, 8192\n"
"s_waitcnt vmcnt(16)\n"
"v_mfma_f32_16x16x32_bf16 a[160:163], a[180:183], v[16:19], a[160:163]\n"
"s_add_u32 s77, s77, s74\n"
"v_mfma_f32_16x16x32_bf16 a[16:19], a[184:187], v[0:3], a[16:19]\n"
"buffer_load_dwordx4 v[60:63], v86, s[40:43], s77 offen offset:3072 sc1\n"
"v_mfma_f32_16x16x32_bf16 a[20:23], a[184:187], v[4:7], a[20:23]\n"
"s_add_u32 m0, s76, 0x10400\n"
"s_add_u32 s79, s78, 0x14080\n"
"v_mfma_f32_16x16x32_bf16 a[24:27], a[184:187], v[8:11], a[24:27]\n"
"buffer_load_dwordx4 v84, s[24:27], s79 offen lds\n"
"v_mfma_f32_16x16x32_bf16 a[28:31], a[184:187], v[12:15], a[28:31]\n"
"v_mfma_f32_16x16x32_bf16 a[164:167], a[184:187], v[16:19], a[164:167]\n"
"buffer_load_dwordx4 v[64:67], v87, s[40:43], s77 offen offset:3072 sc1\n"
"v_mfma_f32_16x16x32_bf16 a[32:35], a[188:191], v[0:3], a[32:35]\n"
"s_add_u32 m0, s76, 0x10800\n"
"s_add_u32 s79, s78, 0x14100\n"
"v_mfma_f32_16x16x32_bf16 a[36:39], a[188:191], v[4:7], a[36:39]\n"
"buffer_load_dwordx4 v85, s[24:27], s79 offen lds\n"
"v_mfma_f32_16x16x32_bf16 a[40:43], a[188:191], v[8:11], a[40:43]\n"
"buffer_load_dwordx4 v[68:71], v88, s[40:43], s77 offen offset:3072 sc1\n"
"v_mfma_f32_16x16x32_bf16 a[44:47], a[188:191], v[12:15], a[44:47]\n"
"v_mfma_f32_16x16x32_bf16 a[168:171], a[188:191], v[16:19], a[168:171]\n"
"v_mfma_f32_16x16x32_bf16 a[48:51], a[192:195], v[0:3], a[48:51]\n"
"s_add_u32 m0, s76, 0x10c00\n"
"s_add_u32 s79, s78, 0x16840\n"
"buffer_load_dwordx4 v84, s[24:27], s79 offen lds\n"
"v_mfma_f32_16x16x32_bf16 a[52:55], a[192:195], v[4:7], a[52:55]\n"
"buffer_load_dwordx4 v[72:75], v89, s[40:43], s77 offen offset:3072 sc1\n"
"v_mfma_f32_16x16x32_bf16 a[56:59], a[192:195], v[8:11], a[56:59]\n"
"v_mfma_f32_16x16x32_bf16 a[60:63], a[192:195], v[12:15], a[60:63]\n"
"v_mfma_f32_16x16x32_bf16 a[172:175], a[192:195], v[16:19], a[172:175]\n"
"s_add_u32 m0, s76, 0x11000\n"
"s_add_u32 s79, s78, 0x168c0\n"
"v_mfma_f32_16x16x32_bf16 a[64:67], a[196:199], v[0:3], a[64:67]\n"
"buffer_load_dwordx4 v84, s[24:27], s79 offen lds\n"
"buffer_load_dwordx4 v[76:79], v90, s[40:43], s77 offen offset:3072 sc1\n"
"v_mfma_f32_16x16x32_bf16 a[68:71], a[196:199], v[4:7], a[68:71]\n"
"v_mfma_f32_16x16x32_bf16 a[72:75], a[196:199], v[8:11], a[72:75]\n"
"v_mfma_f32_16x16x32_bf16 a[76:79], a[196:199], v[12:15], a[76:79]\n"
"v_mfma_f32_16x16x32_bf16 a[176:179], a[196:199], v[16:19], a[176:179]\n"
"s_waitcnt vmcnt(63) lgkmcnt(0)\n"
"s_barrier\n"
"v_add_u32 v80, 0x5000, v80\n"
"v_add_u32 v81, 0x5000, v81\n"
"ds_read_b64_tr_b16 a[180:181], v80 offset:0\n"
"v_mfma_f32_16x16x32_bf16 a[80:83], a[200:203], v[0:3], a[80:83]\n"
"ds_read_b64_tr_b16 a[182:183], v80 offset:2560\n"
"v_mfma_f32_16x16x32_bf16 a[84:87], a[200:203], v[4:7], a[84:87]\n"
"ds_read_b64_tr_b16 a[184:185], v81 offset:0\n"
"v_add_u32 v82, 0x5000, v82\n"
"v_add_u32 v83, 0x5000, v83\n"
"v_mfma_f32_16x16x32_bf16 a[88:91], a[200:203], v[8:11], a[88:91]\n"
"ds_read_b64_tr_b16 a[186:187], v81 offset:2560\n"
"v_mfma_f32_16x16x32_bf16 a[92:95], a[200:203], v[12:15], a[92:95]\n"
"ds_read_b64_tr_b16 a[188:189], v80 offset:512\n"
"v_mfma_f32_16x16x32_bf16 a[96:99], a[204:207], v[0:3], a[96:99]\n"
"ds_read_b64_tr_b16 a[190:191], v80 offset:3072\n"
"v_mfma_f32_16x16x32_bf16 a[100:103], a[204:207], v[4:7], a[100:103]\n"
"ds_read_b64_tr_b16 a[192:193], v81 offset:512\n"
"v_mfma_f32_16x16x32_bf16 a[104:107], a[204:207], v[8:11], a[104:107]\n"
"ds_read_b64_tr_b16 a[194:195], v81 offset:3072\n"
"v_mfma_f32_16x16x32_bf16 a[108:111], a[204:207], v[12:15], a[108:111]\n"
"ds_read_b64_tr_b16 a[196:197], v80 offset:1024\n"
"v_mfma_f32_16x16x32_bf16 a[112:115], a[208:211], v[0:3], a[112:115]\n"
"ds_read_b64_tr_b16 a[198:199], v80 offset:3584\n"
"v_mfma_f32_16x16x32_bf16 a[116:119], a[208:211], v[4:7], a[116:119]\n"
"ds_read_b64_tr_b16 a[200:201], v82 offset:1024\n"
"v_mfma_f32_16x16x32_bf16 a[120:123], a[208:211], v[8:11], a[120:123]\n"
"ds_read_b64_tr_b16 a[202:203], v82 offset:3584\n"
"v_mfma_f32_16x16x32_bf16 a[124:127], a[208:211], v[12:15], a[124:127]\n"
"ds_read_b64_tr_b16 a[204:205], v83 offset:1536\n"
"v_mfma_f32_16x16x32_bf16 a[128:131], a[212:215], v[0:3], a[128:131]\n"
"ds_read_b64_tr_b16 a[206:207], v83 offset:4096\n"
"v_mfma_f32_16x16x32_bf16 a[132:135], a[212:215], v[4:7], a[132:135]\n"
"ds_read_b64_tr_b16 a[208:209], v82 offset:1536\n"
"v_mfma_f32_16x16x32_bf16 a[136:139], a[212:215], v[8:11], a[136:139]\n"
"s_waitcnt lgkmcnt(11)\n"
"ds_read_b64_tr_b16 a[210:211], v82 offset:4096\n"
"v_mfma_f32_16x16x32_bf16 a[140:143], a[212:215], v[12:15], a[140:143]\n"
"v_mfma_f32_16x16x32_bf16 a[144:147], a[216:219], v[0:3], a[144:147]\n"
"v_mfma_f32_16x16x32_bf16 a[148:151], a[216:219], v[4:7], a[148:151]\n"
"ds_read_b64_tr_b16 a[212:213], v83 offset:2048\n"
"v_mfma_f32_16x16x32_bf16 a[152:155], a[216:219], v[8:11], a[152:155]\n"
"ds_read_b64_tr_b16 a[214:215], v83 offset:4608\n"
"v_mfma_f32_16x16x32_bf16 a[156:159], a[216:219], v[12:15], a[156:159]\n"
"s_waitcnt vmcnt(24)\n"
"v_mfma_f32_16x16x32_bf16 a[0:3], a[180:183], v[20:23], a[0:3]\n"
"ds_read_b64_tr_b16 a[216:217], v82 offset:2048\n"
"s_waitcnt vmcnt(23)\n"
"v_mfma_f32_16x16x32_bf16 a[4:7], a[180:183], v[24:27], a[4:7]\n"
"s_waitcnt lgkmcnt(8)\n"
"ds_read_b64_tr_b16 a[218:219], v82 offset:4608\n"
"s_add_u32 s78, s80, 2\n"
"s_and_b32 s78, s78, 3\n"
"s_mul_i32 s78, s78, 327680\n"
"s_waitcnt vmcnt(22)\n"
"v_mfma_f32_16x16x32_bf16 a[8:11], a[180:183], v[28:31], a[8:11]\n"
"s_add_u32 s78, s78, s75\n"
"s_add_u32 m0, s76, 0x15000\n"
"s_add_u32 s79, s78, 0x1e000\n"
"s_add_u32 s77, s80, 2\n"
"s_waitcnt vmcnt(21)\n"
"v_mfma_f32_16x16x32_bf16 a[12:15], a[180:183], v[32:35], a[12:15]\n"
"s_and_b32 s77, s77, 3\n"
"buffer_load_dwordx4 v84, s[24:27], s79 offen lds\n"
"s_mul_i32 s77, s77, 8192\n"
"s_waitcnt vmcnt(21)\n"
"v_mfma_f32_16x16x32_bf16 a[160:163], a[180:183], v[36:39], a[160:163]\n"
"s_add_u32 s77, s77, s74\n"
"v_mfma_f32_16x16x32_bf16 a[16:19], a[184:187], v[20:23], a[16:19]\n"
"buffer_load_dwordx4 v[0:3], v86, s[40:43], s77 offen offset:0 sc1\n"
"v_mfma_f32_16x16x32_bf16 a[20:23], a[184:187], v[24:27], a[20:23]\n"
"s_add_u32 m0, s76, 0x15400\n"
"s_add_u32 s79, s78, 0x1e080\n"
"v_mfma_f32_16x16x32_bf16 a[24:27], a[184:187], v[28:31], a[24:27]\n"
"buffer_load_dwordx4 v84, s[24:27], s79 offen lds\n"
"v_mfma_f32_16x16x32_bf16 a[28:31], a[184:187], v[32:35], a[28:31]\n"
"v_mfma_f32_16x16x32_bf16 a[164:167], a[184:187], v[36:39], a[164:167]\n"
"buffer_load_dwordx4 v[4:7], v87, s[40:43], s77 offen offset:0 sc1\n"
"v_mfma_f32_16x16x32_bf16 a[32:35], a[188:191], v[20:23], a[32:35]\n"
"s_add_u32 m0, s76, 0x15800\n"
"s_add_u32 s79, s78, 0x1e100\n"
"v_mfma_f32_16x16x32_bf16 a[36:39], a[188:191], v[24:27], a[36:39]\n"
"buffer_load_dwordx4 v85, s[24:27], s79 offen lds\n"
"v_mfma_f32_16x16x32_bf16 a[40:43], a[188:191], v[28:31], a[40:43]\n"
"buffer_load_dwordx4 v[8:11], v88, s[40:43], s77 offen offset:0 sc1\n"
"v_mfma_f32_16x16x32_bf16 a[44:47], a[188:191], v[32:35], a[44:47]\n"
"v_mfma_f32_16x16x32_bf16 a[168:171], a[188:191], v[36:39], a[168:171]\n"
"v_mfma_f32_16x16x32_bf16 a[48:51], a[192:195], v[20:23], a[48:51]\n"
"s_add_u32 m0, s76, 0x15c00\n"
"s_add_u32 s79, s78, 0x20840\n"
"buffer_load_dwordx4 v84, s[24:27], s79 offen lds\n"
"v_mfma_f32_16x16x32_bf16 a[52:55], a[192:195], v[24:27], a[52:55]\n"
"buffer_load_dwordx4 v[12:15], v89, s[40:43], s77 offen offset:0 sc1\n"
"v_mfma_f32_16x16x32_bf16 a[56:59], a[192:195], v[28:31], a[56:59]\n"
"v_mfma_f32_16x16x32_bf16 a[60:63], a[192:195], v[32:35], a[60:63]\n"
"v_mfma_f32_16x16x32_bf16 a[172:175], a[192:195], v[36:39], a[172:175]\n"
"s_add_u32 m0, s76, 0x16000\n"
"s_add_u32 s79, s78, 0x208c0\n"
"v_mfma_f32_16x16x32_bf16 a[64:67], a[196:199], v[20:23], a[64:67]\n"
"buffer_load_dwordx4 v84, s[24:27], s79 offen lds\n"
"buffer_load_dwordx4 v[16:19], v90, s[40:43], s77 offen offset:0 sc1\n"
"v_mfma_f32_16x16x32_bf16 a[68:71], a[196:199], v[24:27], a[68:71]\n"
"v_mfma_f32_16x16x32_bf16 a[72:75], a[196:199], v[28:31], a[72:75]\n"
"v_mfma_f32_16x16x32_bf16 a[76:79], a[196:199], v[32:35], a[76:79]\n"
"v_mfma_f32_16x16x32_bf16 a[176:179], a[196:199], v[36:39], a[176:179]\n"
"s_waitcnt vmcnt(63) lgkmcnt(0)\n"
"s_barrier\n"
"v_add_u32 v80, 0x5000, v80\n"
"v_add_u32 v81, 0x5000, v81\n"
"ds_read_b64_tr_b16 a[180:181], v80 offset:0\n"
"v_mfma_f32_16x16x32_bf16 a[80:83], a[200:203], v[20:23], a[80:83]\n"
"ds_read_b64_tr_b16 a[182:183], v80 offset:2560\n"
"v_mfma_f32_16x16x32_bf16 a[84:87], a[200:203], v[24:27], a[84:87]\n"
"ds_read_b64_tr_b16 a[184:185], v81 offset:0\n"
"v_add_u32 v82, 0x5000, v82\n"
"v_add_u32 v83, 0x5000, v83\n"
"v_mfma_f32_16x16x32_bf16 a[88:91], a[200:203], v[28:31], a[88:91]\n"
"ds_read_b64_tr_b16 a[186:187], v81 offset:2560\n"
"v_mfma_f32_16x16x32_bf16 a[92:95], a[200:203], v[32:35], a[92:95]\n"
"ds_read_b64_tr_b16 a[188:189], v80 offset:512\n"
"v_mfma_f32_16x16x32_bf16 a[96:99], a[204:207], v[20:23], a[96:99]\n"
"ds_read_b64_tr_b16 a[190:191], v80 offset:3072\n"
"v_mfma_f32_16x16x32_bf16 a[100:103], a[204:207], v[24:27], a[100:103]\n"
"ds_read_b64_tr_b16 a[192:193], v81 offset:512\n"
"v_mfma_f32_16x16x32_bf16 a[104:107], a[204:207], v[28:31], a[104:107]\n"
"ds_read_b64_tr_b16 a[194:195], v81 offset:3072\n"
"v_mfma_f32_16x16x32_bf16 a[108:111], a[204:207], v[32:35], a[108:111]\n"
"ds_read_b64_tr_b16 a[196:197], v80 offset:1024\n"
"v_mfma_f32_16x16x32_bf16 a[112:115], a[208:211], v[20:23], a[112:115]\n"
"ds_read_b64_tr_b16 a[198:199], v80 offset:3584\n"
"v_mfma_f32_16x16x32_bf16 a[116:119], a[208:211], v[24:27], a[116:119]\n"
"ds_read_b64_tr_b16 a[200:201], v82 offset:1024\n"
"v_mfma_f32_16x16x32_bf16 a[120:123], a[208:211], v[28:31], a[120:123]\n"
"ds_read_b64_tr_b16 a[202:203], v82 offset:3584\n"
"v_mfma_f32_16x16x32_bf16 a[124:127], a[208:211], v[32:35], a[124:127]\n"
"ds_read_b64_tr_b16 a[204:205], v83 offset:1536\n"
"v_mfma_f32_16x16x32_bf16 a[128:131], a[212:215], v[20:23], a[128:131]\n"
"ds_read_b64_tr_b16 a[206:207], v83 offset:4096\n"
"v_mfma_f32_16x16x32_bf16 a[132:135], a[212:215], v[24:27], a[132:135]\n"
"ds_read_b64_tr_b16 a[208:209], v82 offset:1536\n"
"v_mfma_f32_16x16x32_bf16 a[136:139], a[212:215], v[28:31], a[136:139]\n"
"s_waitcnt lgkmcnt(11)\n"
"ds_read_b64_tr_b16 a[210:211], v82 offset:4096\n"
"v_mfma_f32_16x16x32_bf16 a[140:143], a[212:215], v[32:35], a[140:143]\n"
"v_mfma_f32_16x16x32_bf16 a[144:147], a[216:219], v[20:23], a[144:147]\n"
"v_mfma_f32_16x16x32_bf16 a[148:151], a[216:219], v[24:27], a[148:151]\n"
"ds_read_b64_tr_b16 a[212:213], v83 offset:2048\n"
"v_mfma_f32_16x16x32_bf16 a[152:155], a[216:219], v[28:31], a[152:155]\n"
"ds_read_b64_tr_b16 a[214:215], v83 offset:4608\n"
"v_mfma_f32_16x16x32_bf16 a[156:159], a[216:219], v[32:35], a[156:159]\n"
"s_waitcnt vmcnt(28)\n"
"v_mfma_f32_16x16x32_bf16 a[0:3], a[180:183], v[40:43], a[0:3]\n"
"ds_read_b64_tr_b16 a[216:217], v82 offset:2048\n"
"s_waitcnt vmcnt(26)\n"
"v_mfma_f32_16x16x32_bf16 a[4:7], a[180:183], v[44:47], a[4:7]\n"
"s_waitcnt lgkmcnt(8)\n"
"ds_read_b64_tr_b16 a[218:219], v82 offset:4608\n"
"s_add_u32 s78, s80, 3\n"
"s_and_b32 s78, s78, 3\n"
"s_mul_i32 s78, s78, 327680\n"
"s_waitcnt vmcnt(24)\n"
"v_mfma_f32_16x16x32_bf16 a[8:11], a[180:183], v[48:51], a[8:11]\n"
"s_add_u32 s78, s78, s75\n"
"s_add_u32 m0, s76, 0x1a000\n"
"s_add_u32 s79, s78, 0x0\n"
"s_add_u32 s77, s80, 2\n"
"s_waitcnt vmcnt(22)\n"
"v_mfma_f32_16x16x32_bf16 a[12:15], a[180:183], v[52:55], a[12:15]\n"
"s_and_b32 s77, s77, 3\n"
"buffer_load_dwordx4 v84, s[24:27], s79 offen lds\n"
"s_mul_i32 s77, s77, 8192\n"
"s_waitcnt vmcnt(21)\n"
"v_mfma_f32_16x16x32_bf16 a[160:163], a[180:183], v[56:59], a[160:163]\n"
"s_add_u32 s77, s77, s74\n"
"v_mfma_f32_16x16x32_bf16 a[16:19], a[184:187], v[40:43], a[16:19]\n"
"buffer_load_dwordx4 v[20:23], v86, s[40:43], s77 offen offset:1024 sc1\n"
"v_mfma_f32_16x16x32_bf16 a[20:23], a[184:187], v[44:47], a[20:23]\n"
"s_add_u32 m0, s76, 0x1a400\n"
"s_add_u32 s79, s78, 0x80\n"
"v_mfma_f32_16x16x32_bf16 a[24:27], a[184:187], v[48:51], a[24:27]\n"
"buffer_load_dwordx4 v84, s[24:27], s79 offen lds\n"
"v_mfma_f32_16x16x32_bf16 a[28:31], a[184:187], v[52:55], a[28:31]\n"
"v_mfma_f32_16x16x32_bf16 a[164:167], a[184:187], v[56:59], a[164:167]\n"
"buffer_load_dwordx4 v[24:27], v87, s[40:43], s77 offen offset:1024 sc1\n"
"v_mfma_f32_16x16x32_bf16 a[32:35], a[188:191], v[40:43], a[32:35]\n"
"s_add_u32 m0, s76, 0x1a800\n"
"s_add_u32 s79, s78, 0x100\n"
"v_mfma_f32_16x16x32_bf16 a[36:39], a[188:191], v[44:47], a[36:39]\n"
"buffer_load_dwordx4 v85, s[24:27], s79 offen lds\n"
"v_mfma_f32_16x16x32_bf16 a[40:43], a[188:191], v[48:51], a[40:43]\n"
"buffer_load_dwordx4 v[28:31], v88, s[40:43], s77 offen offset:1024 sc1\n"
"v_mfma_f32_16x16x32_bf16 a[44:47], a[188:191], v[52:55], a[44:47]\n"
"v_mfma_f32_16x16x32_bf16 a[168:171], a[188:191], v[56:59], a[168:171]\n"
"v_mfma_f32_16x16x32_bf16 a[48:51], a[192:195], v[40:43], a[48:51]\n"
"s_add_u32 m0, s76, 0x1ac00\n"
"s_add_u32 s79, s78, 0x2840\n"
"buffer_load_dwordx4 v84, s[24:27], s79 offen lds\n"
"v_mfma_f32_16x16x32_bf16 a[52:55], a[192:195], v[44:47], a[52:55]\n"
"buffer_load_dwordx4 v[32:35], v89, s[40:43], s77 offen offset:1024 sc1\n"
"v_mfma_f32_16x16x32_bf16 a[56:59], a[192:195], v[48:51], a[56:59]\n"
"v_mfma_f32_16x16x32_bf16 a[60:63], a[192:195], v[52:55], a[60:63]\n"
"v_mfma_f32_16x16x32_bf16 a[172:175], a[192:195], v[56:59], a[172:175]\n"
"s_add_u32 m0, s76, 0x1b000\n"
"s_add_u32 s79, s78, 0x28c0\n"
"v_mfma_f32_16x16x32_bf16 a[64:67], a[196:199], v[40:43], a[64:67]\n"
"buffer_load_dwordx4 v84, s[24:27], s79 offen lds\n"
"buffer_load_dwordx4 v[36:39], v90, s[40:43], s77 offen offset:1024 sc1\n"
"v_mfma_f32_16x16x32_bf16 a[68:71], a[196:199], v[44:47], a[68:71]\n"
"v_mfma_f32_16x16x32_bf16 a[72:75], a[196:199], v[48:51], a[72:75]\n"
"v_mfma_f32_16x16x32_bf16 a[76:79], a[196:199], v[52:55], a[76:79]\n"
"v_mfma_f32_16x16x32_bf16 a[176:179], a[196:199], v[56:59], a[176:179]\n"
"s_waitcnt vmcnt(63) lgkmcnt(0)\n"
"s_barrier\n"
"v_add_u32 v80, 0xfffe2000, v80\n"
"v_add_u32 v81, 0xfffe2000, v81\n"
"ds_read_b64_tr_b16 a[180:181], v80 offset:0\n"
"v_mfma_f32_16x16x32_bf16 a[80:83], a[200:203], v[40:43], a[80:83]\n"
"ds_read_b64_tr_b16 a[182:183], v80 offset:2560\n"
"v_mfma_f32_16x16x32_bf16 a[84:87], a[200:203], v[44:47], a[84:87]\n"
"ds_read_b64_tr_b16 a[184:185], v81 offset:0\n"
"v_add_u32 v82, 0xfffe2000, v82\n"
"v_add_u32 v83, 0xfffe2000, v83\n"
"v_mfma_f32_16x16x32_bf16 a[88:91], a[200:203], v[48:51], a[88:91]\n"
"ds_read_b64_tr_b16 a[186:187], v81 offset:2560\n"
"v_mfma_f32_16x16x32_bf16 a[92:95], a[200:203], v[52:55], a[92:95]\n"
"ds_read_b64_tr_b16 a[188:189], v80 offset:512\n"
"v_mfma_f32_16x16x32_bf16 a[96:99], a[204:207], v[40:43], a[96:99]\n"
"ds_read_b64_tr_b16 a[190:191], v80 offset:3072\n"
"v_mfma_f32_16x16x32_bf16 a[100:103], a[204:207], v[44:47], a[100:103]\n"
"ds_read_b64_tr_b16 a[192:193], v81 offset:512\n"
"v_mfma_f32_16x16x32_bf16 a[104:107], a[204:207], v[48:51], a[104:107]\n"
"ds_read_b64_tr_b16 a[194:195], v81 offset:3072\n"
"v_mfma_f32_16x16x32_bf16 a[108:111], a[204:207], v[52:55], a[108:111]\n"
"ds_read_b64_tr_b16 a[196:197], v80 offset:1024\n"
"v_mfma_f32_16x16x32_bf16 a[112:115], a[208:211], v[40:43], a[112:115]\n"
"ds_read_b64_tr_b16 a[198:199], v80 offset:3584\n"
"v_mfma_f32_16x16x32_bf16 a[116:119], a[208:211], v[44:47], a[116:119]\n"
"ds_read_b64_tr_b16 a[200:201], v82 offset:1024\n"
"v_mfma_f32_16x16x32_bf16 a[120:123], a[208:211], v[48:51], a[120:123]\n"
"ds_read_b64_tr_b16 a[202:203], v82 offset:3584\n"
"v_mfma_f32_16x16x32_bf16 a[124:127], a[208:211], v[52:55], a[124:127]\n"
"ds_read_b64_tr_b16 a[204:205], v83 offset:1536\n"
"v_mfma_f32_16x16x32_bf16 a[128:131], a[212:215], v[40:43], a[128:131]\n"
"ds_read_b64_tr_b16 a[206:207], v83 offset:4096\n"
"v_mfma_f32_16x16x32_bf16 a[132:135], a[212:215], v[44:47], a[132:135]\n"
"ds_read_b64_tr_b16 a[208:209], v82 offset:1536\n"
"v_mfma_f32_16x16x32_bf16 a[136:139], a[212:215], v[48:51], a[136:139]\n"
"s_waitcnt lgkmcnt(11)\n"
"ds_read_b64_tr_b16 a[210:211], v82 offset:4096\n"
"v_mfma_f32_16x16x32_bf16 a[140:143], a[212:215], v[52:55], a[140:143]\n"
"v_mfma_f32_16x16x32_bf16 a[144:147], a[216:219], v[40:43], a[144:147]\n"
"v_mfma_f32_16x16x32_bf16 a[148:151], a[216:219], v[44:47], a[148:151]\n"
"ds_read_b64_tr_b16 a[212:213], v83 offset:2048\n"
"v_mfma_f32_16x16x32_bf16 a[152:155], a[216:219], v[48:51], a[152:155]\n"
"ds_read_b64_tr_b16 a[214:215], v83 offset:4608\n"
"v_mfma_f32_16x16x32_bf16 a[156:159], a[216:219], v[52:55], a[156:159]\n"
"s_waitcnt vmcnt(28)\n"
"v_mfma_f32_16x16x32_bf16 a[0:3], a[180:183], v[60:63], a[0:3]\n"
"ds_read_b64_tr_b16 a[216:217], v82 offset:2048\n"
"s_waitcnt vmcnt(26)\n"
"v_mfma_f32_16x16x32_bf16 a[4:7], a[180:183], v[64:67], a[4:7]\n"
"s_waitcnt lgkmcnt(8)\n"
"ds_read_b64_tr_b16 a[218:219], v82 offset:4608\n"
"s_add_u32 s78, s80, 3\n"
"s_and_b32 s78, s78, 3\n"
"s_mul_i32 s78, s78, 327680\n"
"s_waitcnt vmcnt(24)\n"
"v_mfma_f32_16x16x32_bf16 a[8:11], a[180:183], v[68:71], a[8:11]\n"
"s_add_u32 s78, s78, s75\n"
"s_add_u32 m0, s76, 0x1f000\n"
"s_add_u32 s79, s78, 0xa000\n"
"s_add_u32 s77, s80, 2\n"
"s_waitcnt vmcnt(22)\n"
"v_mfma_f32_16x16x32_bf16 a[12:15], a[180:183], v[72:75], a[12:15]\n"
"s_and_b32 s77, s77, 3\n"
"buffer_load_dwordx4 v84, s[24:27], s79 offen lds\n"
"s_mul_i32 s77, s77, 8192\n"
"s_waitcnt vmcnt(21)\n"
"v_mfma_f32_16x16x32_bf16 a[160:163], a[180:183], v[76:79], a[160:163]\n"
"s_add_u32 s77, s77, s74\n"
"v_mfma_f32_16x16x32_bf16 a[16:19], a[184:187], v[60:63], a[16:19]\n"
"buffer_load_dwordx4 v[40:43], v86, s[40:43], s77 offen offset:2048 sc1\n"
"v_mfma_f32_16x16x32_bf16 a[20:23], a[184:187], v[64:67], a[20:23]\n"
"s_add_u32 m0, s76, 0x1f400\n"
"s_add_u32 s79, s78, 0xa080\n"
"v_mfma_f32_16x16x32_bf16 a[24:27], a[184:187], v[68:71], a[24:27]\n"
"buffer_load_dwordx4 v84, s[24:27], s79 offen lds\n"
"v_mfma_f32_16x16x32_bf16 a[28:31], a[184:187], v[72:75], a[28:31]\n"
"v_mfma_f32_16x16x32_bf16 a[164:167], a[184:187], v[76:79], a[164:167]\n"
"buffer_load_dwordx4 v[44:47], v87, s[40:43], s77 offen offset:2048 sc1\n"
"v_mfma_f32_16x16x32_bf16 a[32:35], a[188:191], v[60:63], a[32:35]\n"
"s_add_u32 m0, s76, 0x1f800\n"
"s_add_u32 s79, s78, 0xa100\n"
"v_mfma_f32_16x16x32_bf16 a[36:39], a[188:191], v[64:67], a[36:39]\n"
"buffer_load_dwordx4 v85, s[24:27], s79 offen lds\n"
"v_mfma_f32_16x16x32_bf16 a[40:43], a[188:191], v[68:71], a[40:43]\n"
"buffer_load_dwordx4 v[48:51], v88, s[40:43], s77 offen offset:2048 sc1\n"
"v_mfma_f32_16x16x32_bf16 a[44:47], a[188:191], v[72:75], a[44:47]\n"
"v_mfma_f32_16x16x32_bf16 a[168:171], a[188:191], v[76:79], a[168:171]\n"
"v_mfma_f32_16x16x32_bf16 a[48:51], a[192:195], v[60:63], a[48:51]\n"
"s_add_u32 m0, s76, 0x1fc00\n"
"s_add_u32 s79, s78, 0xc840\n"
"buffer_load_dwordx4 v84, s[24:27], s79 offen lds\n"
"v_mfma_f32_16x16x32_bf16 a[52:55], a[192:195], v[64:67], a[52:55]\n"
"buffer_load_dwordx4 v[52:55], v89, s[40:43], s77 offen offset:2048 sc1\n"
"v_mfma_f32_16x16x32_bf16 a[56:59], a[192:195], v[68:71], a[56:59]\n"
"v_mfma_f32_16x16x32_bf16 a[60:63], a[192:195], v[72:75], a[60:63]\n"
"v_mfma_f32_16x16x32_bf16 a[172:175], a[192:195], v[76:79], a[172:175]\n"
"s_add_u32 m0, s76, 0x20000\n"
"s_add_u32 s79, s78, 0xc8c0\n"
"v_mfma_f32_16x16x32_bf16 a[64:67], a[196:199], v[60:63], a[64:67]\n"
"buffer_load_dwordx4 v84, s[24:27], s79 offen lds\n"
"buffer_load_dwordx4 v[56:59], v90, s[40:43], s77 offen offset:2048 sc1\n"
"v_mfma_f32_16x16x32_bf16 a[68:71], a[196:199], v[64:67], a[68:71]\n"
"v_mfma_f32_16x16x32_bf16 a[72:75], a[196:199], v[68:71], a[72:75]\n"
"v_mfma_f32_16x16x32_bf16 a[76:79], a[196:199], v[72:75], a[76:79]\n"
"v_mfma_f32_16x16x32_bf16 a[176:179], a[196:199], v[76:79], a[176:179]\n"
"s_waitcnt vmcnt(63) lgkmcnt(0)\n"
"s_barrier\n"
"v_add_u32 v80, 0x5000, v80\n"
"v_add_u32 v81, 0x5000, v81\n"
"ds_read_b64_tr_b16 a[180:181], v80 offset:0\n"
"v_mfma_f32_16x16x32_bf16 a[80:83], a[200:203], v[60:63], a[80:83]\n"
"ds_read_b64_tr_b16 a[182:183], v80 offset:2560\n"
"v_mfma_f32_16x16x32_bf16 a[84:87], a[200:203], v[64:67], a[84:87]\n"
"ds_read_b64_tr_b16 a[184:185], v81 offset:0\n"
"v_add_u32 v82, 0x5000, v82\n"
"v_add_u32 v83, 0x5000, v83\n"
"v_mfma_f32_16x16x32_bf16 a[88:91], a[200:203], v[68:71], a[88:91]\n"
"ds_read_b64_tr_b16 a[186:187], v81 offset:2560\n"
"v_mfma_f32_16x16x32_bf16 a[92:95], a[200:203], v[72:75], a[92:95]\n"
"ds_read_b64_tr_b16 a[188:189], v80 offset:512\n"
"v_mfma_f32_16x16x32_bf16 a[96:99], a[204:207], v[60:63], a[96:99]\n"
"ds_read_b64_tr_b16 a[190:191], v80 offset:3072\n"
"v_mfma_f32_16x16x32_bf16 a[100:103], a[204:207], v[64:67], a[100:103]\n"
"ds_read_b64_tr_b16 a[192:193], v81 offset:512\n"
"v_mfma_f32_16x16x32_bf16 a[104:107], a[204:207], v[68:71], a[104:107]\n"
"ds_read_b64_tr_b16 a[194:195], v81 offset:3072\n"
"v_mfma_f32_16x16x32_bf16 a[108:111], a[204:207], v[72:75], a[108:111]\n"
"ds_read_b64_tr_b16 a[196:197], v80 offset:1024\n"
"v_mfma_f32_16x16x32_bf16 a[112:115], a[208:211], v[60:63], a[112:115]\n"
"ds_read_b64_tr_b16 a[198:199], v80 offset:3584\n"
"v_mfma_f32_16x16x32_bf16 a[116:119], a[208:211], v[64:67], a[116:119]\n"
"ds_read_b64_tr_b16 a[200:201], v82 offset:1024\n"
"v_mfma_f32_16x16x32_bf16 a[120:123], a[208:211], v[68:71], a[120:123]\n"
"ds_read_b64_tr_b16 a[202:203], v82 offset:3584\n"
"v_mfma_f32_16x16x32_bf16 a[124:127], a[208:211], v[72:75], a[124:127]\n"
"ds_read_b64_tr_b16 a[204:205], v83 offset:1536\n"
"v_mfma_f32_16x16x32_bf16 a[128:131], a[212:215], v[60:63], a[128:131]\n"
"ds_read_b64_tr_b16 a[206:207], v83 offset:4096\n"
"v_mfma_f32_16x16x32_bf16 a[132:135], a[212:215], v[64:67], a[132:135]\n"
"ds_read_b64_tr_b16 a[208:209], v82 offset:1536\n"
"v_mfma_f32_16x16x32_bf16 a[136:139], a[212:215], v[68:71], a[136:139]\n"
"s_waitcnt lgkmcnt(11)\n"
"ds_read_b64_tr_b16 a[210:211], v82 offset:4096\n"
"v_mfma_f32_16x16x32_bf16 a[140:143], a[212:215], v[72:75], a[140:143]\n"
"v_mfma_f32_16x16x32_bf16 a[144:147], a[216:219], v[60:63], a[144:147]\n"
"v_mfma_f32_16x16x32_bf16 a[148:151], a[216:219], v[64:67], a[148:151]\n"
"ds_read_b64_tr_b16 a[212:213], v83 offset:2048\n"
"v_mfma_f32_16x16x32_bf16 a[152:155], a[216:219], v[68:71], a[152:155]\n"
"ds_read_b64_tr_b16 a[214:215], v83 offset:4608\n"
"v_mfma_f32_16x16x32_bf16 a[156:159], a[216:219], v[72:75], a[156:159]\n"
"s_waitcnt vmcnt(28)\n"
"v_mfma_f32_16x16x32_bf16 a[0:3], a[180:183], v[0:3], a[0:3]\n"
"ds_read_b64_tr_b16 a[216:217], v82 offset:2048\n"
"s_waitcnt vmcnt(26)\n"
"v_mfma_f32_16x16x32_bf16 a[4:7], a[180:183], v[4:7], a[4:7]\n"
"s_waitcnt lgkmcnt(8)\n"
"ds_read_b64_tr_b16 a[218:219], v82 offset:4608\n"
"s_add_u32 s78, s80, 3\n"
"s_and_b32 s78, s78, 3\n"
"s_mul_i32 s78, s78, 327680\n"
"s_waitcnt vmcnt(24)\n"
"v_mfma_f32_16x16x32_bf16 a[8:11], a[180:183], v[8:11], a[8:11]\n"
"s_add_u32 s78, s78, s75\n"
"s_add_u32 m0, s76, 0x1000\n"
"s_add_u32 s79, s78, 0x14000\n"
"s_add_u32 s77, s80, 2\n"
"s_waitcnt vmcnt(22)\n"
"v_mfma_f32_16x16x32_bf16 a[12:15], a[180:183], v[12:15], a[12:15]\n"
"s_and_b32 s77, s77, 3\n"
"buffer_load_dwordx4 v84, s[24:27], s79 offen lds\n"
"s_mul_i32 s77, s77, 8192\n"
"s_waitcnt vmcnt(21)\n"
"v_mfma_f32_16x16x32_bf16 a[160:163], a[180:183], v[16:19], a[160:163]\n"
"s_add_u32 s77, s77, s74\n"
"v_mfma_f32_16x16x32_bf16 a[16:19], a[184:187], v[0:3], a[16:19]\n"
"buffer_load_dwordx4 v[60:63], v86, s[40:43], s77 offen offset:3072 sc1\n"
"v_mfma_f32_16x16x32_bf16 a[20:23], a[184:187], v[4:7], a[20:23]\n"
"s_add_u32 m0, s76, 0x1400\n"
"s_add_u32 s79, s78, 0x14080\n"
"v_mfma_f32_16x16x32_bf16 a[24:27], a[184:187], v[8:11], a[24:27]\n"
"buffer_load_dwordx4 v84, s[24:27], s79 offen lds\n"
"v_mfma_f32_16x16x32_bf16 a[28:31], a[184:187], v[12:15], a[28:31]\n"
"v_mfma_f32_16x16x32_bf16 a[164:167], a[184:187], v[16:19], a[164:167]\n"
"buffer_load_dwordx4 v[64:67], v87, s[40:43], s77 offen offset:3072 sc1\n"
"v_mfma_f32_16x16x32_bf16 a[32:35], a[188:191], v[0:3], a[32:35]\n"
"s_add_u32 m0, s76, 0x1800\n"
"s_add_u32 s79, s78, 0x14100\n"
"v_mfma_f32_16x16x32_bf16 a[36:39], a[188:191], v[4:7], a[36:39]\n"
"buffer_load_dwordx4 v85, s[24:27], s79 offen lds\n"
"v_mfma_f32_16x16x32_bf16 a[40:43], a[188:191], v[8:11], a[40:43]\n"
"buffer_load_dwordx4 v[68:71], v88, s[40:43], s77 offen offset:3072 sc1\n"
"v_mfma_f32_16x16x32_bf16 a[44:47], a[188:191], v[12:15], a[44:47]\n"
"v_mfma_f32_16x16x32_bf16 a[168:171], a[188:191], v[16:19], a[168:171]\n"
"v_mfma_f32_16x16x32_bf16 a[48:51], a[192:195], v[0:3], a[48:51]\n"
"s_add_u32 m0, s76, 0x1c00\n"
"s_add_u32 s79, s78, 0x16840\n"
"buffer_load_dwordx4 v84, s[24:27], s79 offen lds\n"
"v_mfma_f32_16x16x32_bf16 a[52:55], a[192:195], v[4:7], a[52:55]\n"
"buffer_load_dwordx4 v[72:75], v89, s[40:43], s77 offen offset:3072 sc1\n"
"v_mfma_f32_16x16x32_bf16 a[56:59], a[192:195], v[8:11], a[56:59]\n"
"v_mfma_f32_16x16x32_bf16 a[60:63], a[192:195], v[12:15], a[60:63]\n"
"v_mfma_f32_16x16x32_bf16 a[172:175], a[192:195], v[16:19], a[172:175]\n"
"s_add_u32 m0, s76, 0x2000\n"
"s_add_u32 s79, s78, 0x168c0\n"
"v_mfma_f32_16x16x32_bf16 a[64:67], a[196:199], v[0:3], a[64:67]\n"
"buffer_load_dwordx4 v84, s[24:27], s79 offen lds\n"
"buffer_load_dwordx4 v[76:79], v90, s[40:43], s77 offen offset:3072 sc1\n"
"v_mfma_f32_16x16x32_bf16 a[68:71], a[196:199], v[4:7], a[68:71]\n"
"v_mfma_f32_16x16x32_bf16 a[72:75], a[196:199], v[8:11], a[72:75]\n"
"v_mfma_f32_16x16x32_bf16 a[76:79], a[196:199], v[12:15], a[76:79]\n"
"v_mfma_f32_16x16x32_bf16 a[176:179], a[196:199], v[16:19], a[176:179]\n"
"s_waitcnt vmcnt(63) lgkmcnt(0)\n"
"s_barrier\n"
"v_add_u32 v80, 0x5000, v80\n"
"v_add_u32 v81, 0x5000, v81\n"
"ds_read_b64_tr_b16 a[180:181], v80 offset:0\n"
"v_mfma_f32_16x16x32_bf16 a[80:83], a[200:203], v[0:3], a[80:83]\n"
"ds_read_b64_tr_b16 a[182:183], v80 offset:2560\n"
"v_mfma_f32_16x16x32_bf16 a[84:87], a[200:203], v[4:7], a[84:87]\n"
"ds_read_b64_tr_b16 a[184:185], v81 offset:0\n"
"v_add_u32 v82, 0x5000, v82\n"
"v_add_u32 v83, 0x5000, v83\n"
"v_mfma_f32_16x16x32_bf16 a[88:91], a[200:203], v[8:11], a[88:91]\n"
"ds_read_b64_tr_b16 a[186:187], v81 offset:2560\n"
"v_mfma_f32_16x16x32_bf16 a[92:95], a[200:203], v[12:15], a[92:95]\n"
"ds_read_b64_tr_b16 a[188:189], v80 offset:512\n"
"v_mfma_f32_16x16x32_bf16 a[96:99], a[204:207], v[0:3], a[96:99]\n"
"ds_read_b64_tr_b16 a[190:191], v80 offset:3072\n"
"v_mfma_f32_16x16x32_bf16 a[100:103], a[204:207], v[4:7], a[100:103]\n"
"ds_read_b64_tr_b16 a[192:193], v81 offset:512\n"
"v_mfma_f32_16x16x32_bf16 a[104:107], a[204:207], v[8:11], a[104:107]\n"
"ds_read_b64_tr_b16 a[194:195], v81 offset:3072\n"
"v_mfma_f32_16x16x32_bf16 a[108:111], a[204:207], v[12:15], a[108:111]\n"
"ds_read_b64_tr_b16 a[196:197], v80 offset:1024\n"
"v_mfma_f32_16x16x32_bf16 a[112:115], a[208:211], v[0:3], a[112:115]\n"
"ds_read_b64_tr_b16 a[198:199], v80 offset:3584\n"
"v_mfma_f32_16x16x32_bf16 a[116:119], a[208:211], v[4:7], a[116:119]\n"
"ds_read_b64_tr_b16 a[200:201], v82 offset:1024\n"
"v_mfma_f32_16x16x32_bf16 a[120:123], a[208:211], v[8:11], a[120:123]\n"
"ds_read_b64_tr_b16 a[202:203], v82 offset:3584\n"
"v_mfma_f32_16x16x32_bf16 a[124:127], a[208:211], v[12:15], a[124:127]\n"
"ds_read_b64_tr_b16 a[204:205], v83 offset:1536\n"
"v_mfma_f32_16x16x32_bf16 a[128:131], a[212:215], v[0:3], a[128:131]\n"
"ds_read_b64_tr_b16 a[206:207], v83 offset:4096\n"
"v_mfma_f32_16x16x32_bf16 a[132:135], a[212:215], v[4:7], a[132:135]\n"
"ds_read_b64_tr_b16 a[208:209], v82 offset:1536\n"
"v_mfma_f32_16x16x32_bf16 a[136:139], a[212:215], v[8:11], a[136:139]\n"
"s_waitcnt lgkmcnt(11)\n"
"ds_read_b64_tr_b16 a[210:211], v82 offset:4096\n"
"v_mfma_f32_16x16x32_bf16 a[140:143], a[212:215], v[12:15], a[140:143]\n"
"v_mfma_f32_16x16x32_bf16 a[144:147], a[216:219], v[0:3], a[144:147]\n"
"v_mfma_f32_16x16x32_bf16 a[148:151], a[216:219], v[4:7], a[148:151]\n"
"ds_read_b64_tr_b16 a[212:213], v83 offset:2048\n"
"v_mfma_f32_16x16x32_bf16 a[152:155], a[216:219], v[8:11], a[152:155]\n"
"ds_read_b64_tr_b16 a[214:215], v83 offset:4608\n"
"v_mfma_f32_16x16x32_bf16 a[156:159], a[216:219], v[12:15], a[156:159]\n"
"s_waitcnt vmcnt(28)\n"
"v_mfma_f32_16x16x32_bf16 a[0:3], a[180:183], v[20:23], a[0:3]\n"
"ds_read_b64_tr_b16 a[216:217], v82 offset:2048\n"
"s_waitcnt vmcnt(26)\n"
"v_mfma_f32_16x16x32_bf16 a[4:7], a[180:183], v[24:27], a[4:7]\n"
"s_waitcnt lgkmcnt(8)\n"
"ds_read_b64_tr_b16 a[218:219], v82 offset:4608\n"
"s_add_u32 s78, s80, 3\n"
"s_and_b32 s78, s78, 3\n"
"s_mul_i32 s78, s78, 327680\n"
"s_waitcnt vmcnt(24)\n"
"v_mfma_f32_16x16x32_bf16 a[8:11], a[180:183], v[28:31], a[8:11]\n"
"s_add_u32 s78, s78, s75\n"
"s_add_u32 m0, s76, 0x6000\n"
"s_add_u32 s79, s78, 0x1e000\n"
"s_add_u32 s77, s80, 3\n"
"s_waitcnt vmcnt(22)\n"
"v_mfma_f32_16x16x32_bf16 a[12:15], a[180:183], v[32:35], a[12:15]\n"
"s_and_b32 s77, s77, 3\n"
"buffer_load_dwordx4 v84, s[24:27], s79 offen lds\n"
"s_mul_i32 s77, s77, 8192\n"
"s_waitcnt vmcnt(21)\n"
"v_mfma_f32_16x16x32_bf16 a[160:163], a[180:183], v[36:39], a[160:163]\n"
"s_add_u32 s77, s77, s74\n"
"v_mfma_f32_16x16x32_bf16 a[16:19], a[184:187], v[20:23], a[16:19]\n"
"buffer_load_dwordx4 v[0:3], v86, s[40:43], s77 offen offset:0 sc1\n"
"v_mfma_f32_16x16x32_bf16 a[20:23], a[184:187], v[24:27], a[20:23]\n"
"s_add_u32 m0, s76, 0x6400\n"
"s_add_u32 s79, s78, 0x1e080\n"
"v_mfma_f32_16x16x32_bf16 a[24:27], a[184:187], v[28:31], a[24:27]\n"
"buffer_load_dwordx4 v84, s[24:27], s79 offen lds\n"
"v_mfma_f32_16x16x32_bf16 a[28:31], a[184:187], v[32:35], a[28:31]\n"
"v_mfma_f32_16x16x32_bf16 a[164:167], a[184:187], v[36:39], a[164:167]\n"
"buffer_load_dwordx4 v[4:7], v87, s[40:43], s77 offen offset:0 sc1\n"
"v_mfma_f32_16x16x32_bf16 a[32:35], a[188:191], v[20:23], a[32:35]\n"
"s_add_u32 m0, s76, 0x6800\n"
"s_add_u32 s79, s78, 0x1e100\n"
"v_mfma_f32_16x16x32_bf16 a[36:39], a[188:191], v[24:27], a[36:39]\n"
"buffer_load_dwordx4 v85, s[24:27], s79 offen lds\n"
"v_mfma_f32_16x16x32_bf16 a[40:43], a[188:191], v[28:31], a[40:43]\n"
"buffer_load_dwordx4 v[8:11], v88, s[40:43], s77 offen offset:0 sc1\n"
"v_mfma_f32_16x16x32_bf16 a[44:47], a[188:191], v[32:35], a[44:47]\n"
"v_mfma_f32_16x16x32_bf16 a[168:171], a[188:191], v[36:39], a[168:171]\n"
"v_mfma_f32_16x16x32_bf16 a[48:51], a[192:195], v[20:23], a[48:51]\n"
"s_add_u32 m0, s76, 0x6c00\n"
"s_add_u32 s79, s78, 0x20840\n"
"buffer_load_dwordx4 v84, s[24:27], s79 offen lds\n"
"v_mfma_f32_16x16x32_bf16 a[52:55], a[192:195], v[24:27], a[52:55]\n"
"buffer_load_dwordx4 v[12:15], v89, s[40:43], s77 offen offset:0 sc1\n"
"v_mfma_f32_16x16x32_bf16 a[56:59], a[192:195], v[28:31], a[56:59]\n"
"v_mfma_f32_16x16x32_bf16 a[60:63], a[192:195], v[32:35], a[60:63]\n"
"v_mfma_f32_16x16x32_bf16 a[172:175], a[192:195], v[36:39], a[172:175]\n"
"s_add_u32 m0, s76, 0x7000\n"
"s_add_u32 s79, s78, 0x208c0\n"
"v_mfma_f32_16x16x32_bf16 a[64:67], a[196:199], v[20:23], a[64:67]\n"
"buffer_load_dwordx4 v84, s[24:27], s79 offen lds\n"
"buffer_load_dwordx4 v[16:19], v90, s[40:43], s77 offen offset:0 sc1\n"
"v_mfma_f32_16x16x32_bf16 a[68:71], a[196:199], v[24:27], a[68:71]\n"
"v_mfma_f32_16x16x32_bf16 a[72:75], a[196:199], v[28:31], a[72:75]\n"
"v_mfma_f32_16x16x32_bf16 a[76:79], a[196:199], v[32:35], a[76:79]\n"
"v_mfma_f32_16x16x32_bf16 a[176:179], a[196:199], v[36:39], a[176:179]\n"
"s_waitcnt vmcnt(63) lgkmcnt(0)\n"
"s_barrier\n"
"v_add_u32 v80, 0x5000, v80\n"
"v_add_u32 v81, 0x5000, v81\n"
"ds_read_b64_tr_b16 a[180:181], v80 offset:0\n"
"v_mfma_f32_16x16x32_bf16 a[80:83], a[200:203], v[20:23], a[80:83]\n"
"ds_read_b64_tr_b16 a[182:183], v80 offset:2560\n"
"v_mfma_f32_16x16x32_bf16 a[84:87], a[200:203], v[24:27], a[84:87]\n"
"ds_read_b64_tr_b16 a[184:185], v81 offset:0\n"
"v_add_u32 v82, 0x5000, v82\n"
"v_add_u32 v83, 0x5000, v83\n"
"v_mfma_f32_16x16x32_bf16 a[88:91], a[200:203], v[28:31], a[88:91]\n"
"ds_read_b64_tr_b16 a[186:187], v81 offset:2560\n"
"v_mfma_f32_16x16x32_bf16 a[92:95], a[200:203], v[32:35], a[92:95]\n"
"ds_read_b64_tr_b16 a[188:189], v80 offset:512\n"
"v_mfma_f32_16x16x32_bf16 a[96:99], a[204:207], v[20:23], a[96:99]\n"
"ds_read_b64_tr_b16 a[190:191], v80 offset:3072\n"
"v_mfma_f32_16x16x32_bf16 a[100:103], a[204:207], v[24:27], a[100:103]\n"
"ds_read_b64_tr_b16 a[192:193], v81 offset:512\n"
"v_mfma_f32_16x16x32_bf16 a[104:107], a[204:207], v[28:31], a[104:107]\n"
"ds_read_b64_tr_b16 a[194:195], v81 offset:3072\n"
"v_mfma_f32_16x16x32_bf16 a[108:111], a[204:207], v[32:35], a[108:111]\n"
"ds_read_b64_tr_b16 a[196:197], v80 offset:1024\n"
"v_mfma_f32_16x16x32_bf16 a[112:115], a[208:211], v[20:23], a[112:115]\n"
"ds_read_b64_tr_b16 a[198:199], v80 offset:3584\n"
"v_mfma_f32_16x16x32_bf16 a[116:119], a[208:211], v[24:27], a[116:119]\n"
"ds_read_b64_tr_b16 a[200:201], v82 offset:1024\n"
"v_mfma_f32_16x16x32_bf16 a[120:123], a[208:211], v[28:31], a[120:123]\n"
"ds_read_b64_tr_b16 a[202:203], v82 offset:3584\n"
"v_mfma_f32_16x16x32_bf16 a[124:127], a[208:211], v[32:35], a[124:127]\n"
"ds_read_b64_tr_b16 a[204:205], v83 offset:1536\n"
"v_mfma_f32_16x16x32_bf16 a[128:131], a[212:215], v[20:23], a[128:131]\n"
"ds_read_b64_tr_b16 a[206:207], v83 offset:4096\n"
"v_mfma_f32_16x16x32_bf16 a[132:135], a[212:215], v[24:27], a[132:135]\n"
"ds_read_b64_tr_b16 a[208:209], v82 offset:1536\n"
"v_mfma_f32_16x16x32_bf16 a[136:139], a[212:215], v[28:31], a[136:139]\n"
"s_waitcnt lgkmcnt(11)\n"
"ds_read_b64_tr_b16 a[210:211], v82 offset:4096\n"
"v_mfma_f32_16x16x32_bf16 a[140:143], a[212:215], v[32:35], a[140:143]\n"
"v_mfma_f32_16x16x32_bf16 a[144:147], a[216:219], v[20:23], a[144:147]\n"
"v_mfma_f32_16x16x32_bf16 a[148:151], a[216:219], v[24:27], a[148:151]\n"
"ds_read_b64_tr_b16 a[212:213], v83 offset:2048\n"
"v_mfma_f32_16x16x32_bf16 a[152:155], a[216:219], v[28:31], a[152:155]\n"
"ds_read_b64_tr_b16 a[214:215], v83 offset:4608\n"
"v_mfma_f32_16x16x32_bf16 a[156:159], a[216:219], v[32:35], a[156:159]\n"
"s_waitcnt vmcnt(28)\n"
"v_mfma_f32_16x16x32_bf16 a[0:3], a[180:183], v[40:43], a[0:3]\n"
"ds_read_b64_tr_b16 a[216:217], v82 offset:2048\n"
"s_waitcnt vmcnt(26)\n"
"v_mfma_f32_16x16x32_bf16 a[4:7], a[180:183], v[44:47], a[4:7]\n"
"s_waitcnt lgkmcnt(8)\n"
"ds_read_b64_tr_b16 a[218:219], v82 offset:4608\n"
"s_waitcnt vmcnt(24)\n"
"v_mfma_f32_16x16x32_bf16 a[8:11], a[180:183], v[48:51], a[8:11]\n"
"s_add_u32 s77, s80, 3\n"
"s_waitcnt vmcnt(22)\n"
"v_mfma_f32_16x16x32_bf16 a[12:15], a[180:183], v[52:55], a[12:15]\n"
"s_and_b32 s77, s77, 3\n"
"s_mul_i32 s77, s77, 8192\n"
"s_waitcnt vmcnt(20)\n"
"v_mfma_f32_16x16x32_bf16 a[160:163], a[180:183], v[56:59], a[160:163]\n"
"s_add_u32 s77, s77, s74\n"
"v_mfma_f32_16x16x32_bf16 a[16:19], a[184:187], v[40:43], a[16:19]\n"
"buffer_load_dwordx4 v[20:23], v86, s[40:43], s77 offen offset:1024 sc1\n"
"v_mfma_f32_16x16x32_bf16 a[20:23], a[184:187], v[44:47], a[20:23]\n"
"v_mfma_f32_16x16x32_bf16 a[24:27], a[184:187], v[48:51], a[24:27]\n"
"v_mfma_f32_16x16x32_bf16 a[28:31], a[184:187], v[52:55], a[28:31]\n"
"v_mfma_f32_16x16x32_bf16 a[164:167], a[184:187], v[56:59], a[164:167]\n"
"buffer_load_dwordx4 v[24:27], v87, s[40:43], s77 offen offset:1024 sc1\n"
"v_mfma_f32_16x16x32_bf16 a[32:35], a[188:191], v[40:43], a[32:35]\n"
"v_mfma_f32_16x16x32_bf16 a[36:39], a[188:191], v[44:47], a[36:39]\n"
"v_mfma_f32_16x16x32_bf16 a[40:43], a[188:191], v[48:51], a[40:43]\n"
"buffer_load_dwordx4 v[28:31], v88, s[40:43], s77 offen offset:1024 sc1\n"
"v_mfma_f32_16x16x32_bf16 a[44:47], a[188:191], v[52:55], a[44:47]\n"
"v_mfma_f32_16x16x32_bf16 a[168:171], a[188:191], v[56:59], a[168:171]\n"
"v_mfma_f32_16x16x32_bf16 a[48:51], a[192:195], v[40:43], a[48:51]\n"
"v_mfma_f32_16x16x32_bf16 a[52:55], a[192:195], v[44:47], a[52:55]\n"
"buffer_load_dwordx4 v[32:35], v89, s[40:43], s77 offen offset:1024 sc1\n"
"v_mfma_f32_16x16x32_bf16 a[56:59], a[192:195], v[48:51], a[56:59]\n"
"v_mfma_f32_16x16x32_bf16 a[60:63], a[192:195], v[52:55], a[60:63]\n"
"v_mfma_f32_16x16x32_bf16 a[172:175], a[192:195], v[56:59], a[172:175]\n"
"v_mfma_f32_16x16x32_bf16 a[64:67], a[196:199], v[40:43], a[64:67]\n"
"buffer_load_dwordx4 v[36:39], v90, s[40:43], s77 offen offset:1024 sc1\n"
"v_mfma_f32_16x16x32_bf16 a[68:71], a[196:199], v[44:47], a[68:71]\n"
"v_mfma_f32_16x16x32_bf16 a[72:75], a[196:199], v[48:51], a[72:75]\n"
"v_mfma_f32_16x16x32_bf16 a[76:79], a[196:199], v[52:55], a[76:79]\n"
"v_mfma_f32_16x16x32_bf16 a[176:179], a[196:199], v[56:59], a[176:179]\n"
"s_waitcnt vmcnt(63) lgkmcnt(0)\n"
"s_barrier\n"
"v_add_u32 v80, 0x5000, v80\n"
"v_add_u32 v81, 0x5000, v81\n"
"ds_read_b64_tr_b16 a[180:181], v80 offset:0\n"
"v_mfma_f32_16x16x32_bf16 a[80:83], a[200:203], v[40:43], a[80:83]\n"
"ds_read_b64_tr_b16 a[182:183], v80 offset:2560\n"
"v_mfma_f32_16x16x32_bf16 a[84:87], a[200:203], v[44:47], a[84:87]\n"
"ds_read_b64_tr_b16 a[184:185], v81 offset:0\n"
"v_add_u32 v82, 0x5000, v82\n"
"v_add_u32 v83, 0x5000, v83\n"
"v_mfma_f32_16x16x32_bf16 a[88:91], a[200:203], v[48:51], a[88:91]\n"
"ds_read_b64_tr_b16 a[186:187], v81 offset:2560\n"
"v_mfma_f32_16x16x32_bf16 a[92:95], a[200:203], v[52:55], a[92:95]\n"
"ds_read_b64_tr_b16 a[188:189], v80 offset:512\n"
"v_mfma_f32_16x16x32_bf16 a[96:99], a[204:207], v[40:43], a[96:99]\n"
"ds_read_b64_tr_b16 a[190:191], v80 offset:3072\n"
"v_mfma_f32_16x16x32_bf16 a[100:103], a[204:207], v[44:47], a[100:103]\n"
"ds_read_b64_tr_b16 a[192:193], v81 offset:512\n"
"v_mfma_f32_16x16x32_bf16 a[104:107], a[204:207], v[48:51], a[104:107]\n"
"ds_read_b64_tr_b16 a[194:195], v81 offset:3072\n"
"v_mfma_f32_16x16x32_bf16 a[108:111], a[204:207], v[52:55], a[108:111]\n"
"ds_read_b64_tr_b16 a[196:197], v80 offset:1024\n"
"v_mfma_f32_16x16x32_bf16 a[112:115], a[208:211], v[40:43], a[112:115]\n"
"ds_read_b64_tr_b16 a[198:199], v80 offset:3584\n"
"v_mfma_f32_16x16x32_bf16 a[116:119], a[208:211], v[44:47], a[116:119]\n"
"ds_read_b64_tr_b16 a[200:201], v82 offset:1024\n"
"v_mfma_f32_16x16x32_bf16 a[120:123], a[208:211], v[48:51], a[120:123]\n"
"ds_read_b64_tr_b16 a[202:203], v82 offset:3584\n"
"v_mfma_f32_16x16x32_bf16 a[124:127], a[208:211], v[52:55], a[124:127]\n"
"ds_read_b64_tr_b16 a[204:205], v83 offset:1536\n"
"v_mfma_f32_16x16x32_bf16 a[128:131], a[212:215], v[40:43], a[128:131]\n"
"ds_read_b64_tr_b16 a[206:207], v83 offset:4096\n"
"v_mfma_f32_16x16x32_bf16 a[132:135], a[212:215], v[44:47], a[132:135]\n"
"ds_read_b64_tr_b16 a[208:209], v82 offset:1536\n"
"v_mfma_f32_16x16x32_bf16 a[136:139], a[212:215], v[48:51], a[136:139]\n"
"s_waitcnt lgkmcnt(11)\n"
"ds_read_b64_tr_b16 a[210:211], v82 offset:4096\n"
"v_mfma_f32_16x16x32_bf16 a[140:143], a[212:215], v[52:55], a[140:143]\n"
"v_mfma_f32_16x16x32_bf16 a[144:147], a[216:219], v[40:43], a[144:147]\n"
"v_mfma_f32_16x16x32_bf16 a[148:151], a[216:219], v[44:47], a[148:151]\n"
"ds_read_b64_tr_b16 a[212:213], v83 offset:2048\n"
"v_mfma_f32_16x16x32_bf16 a[152:155], a[216:219], v[48:51], a[152:155]\n"
"ds_read_b64_tr_b16 a[214:215], v83 offset:4608\n"
"v_mfma_f32_16x16x32_bf16 a[156:159], a[216:219], v[52:55], a[156:159]\n"
"s_waitcnt vmcnt(23)\n"
"v_mfma_f32_16x16x32_bf16 a[0:3], a[180:183], v[60:63], a[0:3]\n"
"ds_read_b64_tr_b16 a[216:217], v82 offset:2048\n"
"s_waitcnt vmcnt(21)\n"
"v_mfma_f32_16x16x32_bf16 a[4:7], a[180:183], v[64:67], a[4:7]\n"
"s_waitcnt lgkmcnt(8)\n"
"ds_read_b64_tr_b16 a[218:219], v82 offset:4608\n"
"s_waitcnt vmcnt(19)\n"
"v_mfma_f32_16x16x32_bf16 a[8:11], a[180:183], v[68:71], a[8:11]\n"
"s_add_u32 s77, s80, 3\n"
"s_waitcnt vmcnt(17)\n"
"v_mfma_f32_16x16x32_bf16 a[12:15], a[180:183], v[72:75], a[12:15]\n"
"s_and_b32 s77, s77, 3\n"
"s_mul_i32 s77, s77, 8192\n"
"s_waitcnt vmcnt(15)\n"
"v_mfma_f32_16x16x32_bf16 a[160:163], a[180:183], v[76:79], a[160:163]\n"
"s_add_u32 s77, s77, s74\n"
"v_mfma_f32_16x16x32_bf16 a[16:19], a[184:187], v[60:63], a[16:19]\n"
"buffer_load_dwordx4 v[40:43], v86, s[40:43], s77 offen offset:2048 sc1\n"
"v_mfma_f32_16x16x32_bf16 a[20:23], a[184:187], v[64:67], a[20:23]\n"
"v_mfma_f32_16x16x32_bf16 a[24:27], a[184:187], v[68:71], a[24:27]\n"
"v_mfma_f32_16x16x32_bf16 a[28:31], a[184:187], v[72:75], a[28:31]\n"
"v_mfma_f32_16x16x32_bf16 a[164:167], a[184:187], v[76:79], a[164:167]\n"
"buffer_load_dwordx4 v[44:47], v87, s[40:43], s77 offen offset:2048 sc1\n"
"v_mfma_f32_16x16x32_bf16 a[32:35], a[188:191], v[60:63], a[32:35]\n"
"v_mfma_f32_16x16x32_bf16 a[36:39], a[188:191], v[64:67], a[36:39]\n"
"v_mfma_f32_16x16x32_bf16 a[40:43], a[188:191], v[68:71], a[40:43]\n"
"buffer_load_dwordx4 v[48:51], v88, s[40:43], s77 offen offset:2048 sc1\n"
"v_mfma_f32_16x16x32_bf16 a[44:47], a[188:191], v[72:75], a[44:47]\n"
"v_mfma_f32_16x16x32_bf16 a[168:171], a[188:191], v[76:79], a[168:171]\n"
"v_mfma_f32_16x16x32_bf16 a[48:51], a[192:195], v[60:63], a[48:51]\n"
"v_mfma_f32_16x16x32_bf16 a[52:55], a[192:195], v[64:67], a[52:55]\n"
"buffer_load_dwordx4 v[52:55], v89, s[40:43], s77 offen offset:2048 sc1\n"
"v_mfma_f32_16x16x32_bf16 a[56:59], a[192:195], v[68:71], a[56:59]\n"
"v_mfma_f32_16x16x32_bf16 a[60:63], a[192:195], v[72:75], a[60:63]\n"
"v_mfma_f32_16x16x32_bf16 a[172:175], a[192:195], v[76:79], a[172:175]\n"
"v_mfma_f32_16x16x32_bf16 a[64:67], a[196:199], v[60:63], a[64:67]\n"
"buffer_load_dwordx4 v[56:59], v90, s[40:43], s77 offen offset:2048 sc1\n"
"v_mfma_f32_16x16x32_bf16 a[68:71], a[196:199], v[64:67], a[68:71]\n"
"v_mfma_f32_16x16x32_bf16 a[72:75], a[196:199], v[68:71], a[72:75]\n"
"v_mfma_f32_16x16x32_bf16 a[76:79], a[196:199], v[72:75], a[76:79]\n"
"v_mfma_f32_16x16x32_bf16 a[176:179], a[196:199], v[76:79], a[176:179]\n"
"s_waitcnt vmcnt(63) lgkmcnt(0)\n"
"s_barrier\n"
"v_add_u32 v80, 0x5000, v80\n"
"v_add_u32 v81, 0x5000, v81\n"
"ds_read_b64_tr_b16 a[180:181], v80 offset:0\n"
"v_mfma_f32_16x16x32_bf16 a[80:83], a[200:203], v[60:63], a[80:83]\n"
"ds_read_b64_tr_b16 a[182:183], v80 offset:2560\n"
"v_mfma_f32_16x16x32_bf16 a[84:87], a[200:203], v[64:67], a[84:87]\n"
"ds_read_b64_tr_b16 a[184:185], v81 offset:0\n"
"v_add_u32 v82, 0x5000, v82\n"
"v_add_u32 v83, 0x5000, v83\n"
"v_mfma_f32_16x16x32_bf16 a[88:91], a[200:203], v[68:71], a[88:91]\n"
"ds_read_b64_tr_b16 a[186:187], v81 offset:2560\n"
"v_mfma_f32_16x16x32_bf16 a[92:95], a[200:203], v[72:75], a[92:95]\n"
"ds_read_b64_tr_b16 a[188:189], v80 offset:512\n"
"v_mfma_f32_16x16x32_bf16 a[96:99], a[204:207], v[60:63], a[96:99]\n"
"ds_read_b64_tr_b16 a[190:191], v80 offset:3072\n"
"v_mfma_f32_16x16x32_bf16 a[100:103], a[204:207], v[64:67], a[100:103]\n"
"ds_read_b64_tr_b16 a[192:193], v81 offset:512\n"
"v_mfma_f32_16x16x32_bf16 a[104:107], a[204:207], v[68:71], a[104:107]\n"
"ds_read_b64_tr_b16 a[194:195], v81 offset:3072\n"
"v_mfma_f32_16x16x32_bf16 a[108:111], a[204:207], v[72:75], a[108:111]\n"
"ds_read_b64_tr_b16 a[196:197], v80 offset:1024\n"
"v_mfma_f32_16x16x32_bf16 a[112:115], a[208:211], v[60:63], a[112:115]\n"
"ds_read_b64_tr_b16 a[198:199], v80 offset:3584\n"
"v_mfma_f32_16x16x32_bf16 a[116:119], a[208:211], v[64:67], a[116:119]\n"
"ds_read_b64_tr_b16 a[200:201], v82 offset:1024\n"
"v_mfma_f32_16x16x32_bf16 a[120:123], a[208:211], v[68:71], a[120:123]\n"
"ds_read_b64_tr_b16 a[202:203], v82 offset:3584\n"
"v_mfma_f32_16x16x32_bf16 a[124:127], a[208:211], v[72:75], a[124:127]\n"
"ds_read_b64_tr_b16 a[204:205], v83 offset:1536\n"
"v_mfma_f32_16x16x32_bf16 a[128:131], a[212:215], v[60:63], a[128:131]\n"
"ds_read_b64_tr_b16 a[206:207], v83 offset:4096\n"
"v_mfma_f32_16x16x32_bf16 a[132:135], a[212:215], v[64:67], a[132:135]\n"
"ds_read_b64_tr_b16 a[208:209], v82 offset:1536\n"
"v_mfma_f32_16x16x32_bf16 a[136:139], a[212:215], v[68:71], a[136:139]\n"
"s_waitcnt lgkmcnt(11)\n"
"ds_read_b64_tr_b16 a[210:211], v82 offset:4096\n"
"v_mfma_f32_16x16x32_bf16 a[140:143], a[212:215], v[72:75], a[140:143]\n"
"v_mfma_f32_16x16x32_bf16 a[144:147], a[216:219], v[60:63], a[144:147]\n"
"v_mfma_f32_16x16x32_bf16 a[148:151], a[216:219], v[64:67], a[148:151]\n"
"ds_read_b64_tr_b16 a[212:213], v83 offset:2048\n"
"v_mfma_f32_16x16x32_bf16 a[152:155], a[216:219], v[68:71], a[152:155]\n"
"ds_read_b64_tr_b16 a[214:215], v83 offset:4608\n"
"v_mfma_f32_16x16x32_bf16 a[156:159], a[216:219], v[72:75], a[156:159]\n"
"s_waitcnt vmcnt(18)\n"
"v_mfma_f32_16x16x32_bf16 a[0:3], a[180:183], v[0:3], a[0:3]\n"
"ds_read_b64_tr_b16 a[216:217], v82 offset:2048\n"
"s_waitcnt vmcnt(16)\n"
"v_mfma_f32_16x16x32_bf16 a[4:7], a[180:183], v[4:7], a[4:7]\n"
"s_waitcnt lgkmcnt(8)\n"
"ds_read_b64_tr_b16 a[218:219], v82 offset:4608\n"
"s_waitcnt vmcnt(14)\n"
"v_mfma_f32_16x16x32_bf16 a[8:11], a[180:183], v[8:11], a[8:11]\n"
"s_add_u32 s77, s80, 3\n"
"s_waitcnt vmcnt(12)\n"
"v_mfma_f32_16x16x32_bf16 a[12:15], a[180:183], v[12:15], a[12:15]\n"
"s_and_b32 s77, s77, 3\n"
"s_mul_i32 s77, s77, 8192\n"
"s_waitcnt vmcnt(10)\n"
"v_mfma_f32_16x16x32_bf16 a[160:163], a[180:183], v[16:19], a[160:163]\n"
"s_add_u32 s77, s77, s74\n"
"v_mfma_f32_16x16x32_bf16 a[16:19], a[184:187], v[0:3], a[16:19]\n"
"buffer_load_dwordx4 v[60:63], v86, s[40:43], s77 offen offset:3072 sc1\n"
"v_mfma_f32_16x16x32_bf16 a[20:23], a[184:187], v[4:7], a[20:23]\n"
"v_mfma_f32_16x16x32_bf16 a[24:27], a[184:187], v[8:11], a[24:27]\n"
"v_mfma_f32_16x16x32_bf16 a[28:31], a[184:187], v[12:15], a[28:31]\n"
"v_mfma_f32_16x16x32_bf16 a[164:167], a[184:187], v[16:19], a[164:167]\n"
"buffer_load_dwordx4 v[64:67], v87, s[40:43], s77 offen offset:3072 sc1\n"
"v_mfma_f32_16x16x32_bf16 a[32:35], a[188:191], v[0:3], a[32:35]\n"
"v_mfma_f32_16x16x32_bf16 a[36:39], a[188:191], v[4:7], a[36:39]\n"
"v_mfma_f32_16x16x32_bf16 a[40:43], a[188:191], v[8:11], a[40:43]\n"
"buffer_load_dwordx4 v[68:71], v88, s[40:43], s77 offen offset:3072 sc1\n"
"v_mfma_f32_16x16x32_bf16 a[44:47], a[188:191], v[12:15], a[44:47]\n"
"v_mfma_f32_16x16x32_bf16 a[168:171], a[188:191], v[16:19], a[168:171]\n"
"v_mfma_f32_16x16x32_bf16 a[48:51], a[192:195], v[0:3], a[48:51]\n"
"v_mfma_f32_16x16x32_bf16 a[52:55], a[192:195], v[4:7], a[52:55]\n"
"buffer_load_dwordx4 v[72:75], v89, s[40:43], s77 offen offset:3072 sc1\n"
"v_mfma_f32_16x16x32_bf16 a[56:59], a[192:195], v[8:11], a[56:59]\n"
"v_mfma_f32_16x16x32_bf16 a[60:63], a[192:195], v[12:15], a[60:63]\n"
"v_mfma_f32_16x16x32_bf16 a[172:175], a[192:195], v[16:19], a[172:175]\n"
"v_mfma_f32_16x16x32_bf16 a[64:67], a[196:199], v[0:3], a[64:67]\n"
"buffer_load_dwordx4 v[76:79], v90, s[40:43], s77 offen offset:3072 sc1\n"
"v_mfma_f32_16x16x32_bf16 a[68:71], a[196:199], v[4:7], a[68:71]\n"
"v_mfma_f32_16x16x32_bf16 a[72:75], a[196:199], v[8:11], a[72:75]\n"
"v_mfma_f32_16x16x32_bf16 a[76:79], a[196:199], v[12:15], a[76:79]\n"
"v_mfma_f32_16x16x32_bf16 a[176:179], a[196:199], v[16:19], a[176:179]\n"
"s_waitcnt vmcnt(63) lgkmcnt(0)\n"
"s_barrier\n"
"v_add_u32 v80, 0x5000, v80\n"
"v_add_u32 v81, 0x5000, v81\n"
"ds_read_b64_tr_b16 a[180:181], v80 offset:0\n"
"v_mfma_f32_16x16x32_bf16 a[80:83], a[200:203], v[0:3], a[80:83]\n"
"ds_read_b64_tr_b16 a[182:183], v80 offset:2560\n"
"v_mfma_f32_16x16x32_bf16 a[84:87], a[200:203], v[4:7], a[84:87]\n"
"ds_read_b64_tr_b16 a[184:185], v81 offset:0\n"
"v_add_u32 v82, 0x5000, v82\n"
"v_add_u32 v83, 0x5000, v83\n"
"v_mfma_f32_16x16x32_bf16 a[88:91], a[200:203], v[8:11], a[88:91]\n"
"ds_read_b64_tr_b16 a[186:187], v81 offset:2560\n"
"v_mfma_f32_16x16x32_bf16 a[92:95], a[200:203], v[12:15], a[92:95]\n"
"ds_read_b64_tr_b16 a[188:189], v80 offset:512\n"
"v_mfma_f32_16x16x32_bf16 a[96:99], a[204:207], v[0:3], a[96:99]\n"
"ds_read_b64_tr_b16 a[190:191], v80 offset:3072\n"
"v_mfma_f32_16x16x32_bf16 a[100:103], a[204:207], v[4:7], a[100:103]\n"
"ds_read_b64_tr_b16 a[192:193], v81 offset:512\n"
"v_mfma_f32_16x16x32_bf16 a[104:107], a[204:207], v[8:11], a[104:107]\n"
"ds_read_b64_tr_b16 a[194:195], v81 offset:3072\n"
"v_mfma_f32_16x16x32_bf16 a[108:111], a[204:207], v[12:15], a[108:111]\n"
"ds_read_b64_tr_b16 a[196:197], v80 offset:1024\n"
"v_mfma_f32_16x16x32_bf16 a[112:115], a[208:211], v[0:3], a[112:115]\n"
"ds_read_b64_tr_b16 a[198:199], v80 offset:3584\n"
"v_mfma_f32_16x16x32_bf16 a[116:119], a[208:211], v[4:7], a[116:119]\n"
"ds_read_b64_tr_b16 a[200:201], v82 offset:1024\n"
"v_mfma_f32_16x16x32_bf16 a[120:123], a[208:211], v[8:11], a[120:123]\n"
"ds_read_b64_tr_b16 a[202:203], v82 offset:3584\n"
"v_mfma_f32_16x16x32_bf16 a[124:127], a[208:211], v[12:15], a[124:127]\n"
"ds_read_b64_tr_b16 a[204:205], v83 offset:1536\n"
"v_mfma_f32_16x16x32_bf16 a[128:131], a[212:215], v[0:3], a[128:131]\n"
"ds_read_b64_tr_b16 a[206:207], v83 offset:4096\n"
"v_mfma_f32_16x16x32_bf16 a[132:135], a[212:215], v[4:7], a[132:135]\n"
"ds_read_b64_tr_b16 a[208:209], v82 offset:1536\n"
"v_mfma_f32_16x16x32_bf16 a[136:139], a[212:215], v[8:11], a[136:139]\n"
"s_waitcnt lgkmcnt(11)\n"
"ds_read_b64_tr_b16 a[210:211], v82 offset:4096\n"
"v_mfma_f32_16x16x32_bf16 a[140:143], a[212:215], v[12:15], a[140:143]\n"
"v_mfma_f32_16x16x32_bf16 a[144:147], a[216:219], v[0:3], a[144:147]\n"
"v_mfma_f32_16x16x32_bf16 a[148:151], a[216:219], v[4:7], a[148:151]\n"
"ds_read_b64_tr_b16 a[212:213], v83 offset:2048\n"
"v_mfma_f32_16x16x32_bf16 a[152:155], a[216:219], v[8:11], a[152:155]\n"
"ds_read_b64_tr_b16 a[214:215], v83 offset:4608\n"
"v_mfma_f32_16x16x32_bf16 a[156:159], a[216:219], v[12:15], a[156:159]\n"
"s_waitcnt vmcnt(14)\n"
"v_mfma_f32_16x16x32_bf16 a[0:3], a[180:183], v[20:23], a[0:3]\n"
"ds_read_b64_tr_b16 a[216:217], v82 offset:2048\n"
"s_waitcnt vmcnt(13)\n"
"v_mfma_f32_16x16x32_bf16 a[4:7], a[180:183], v[24:27], a[4:7]\n"
"s_waitcnt lgkmcnt(8)\n"
"ds_read_b64_tr_b16 a[218:219], v82 offset:4608\n"
"s_waitcnt vmcnt(12)\n"
"v_mfma_f32_16x16x32_bf16 a[8:11], a[180:183], v[28:31], a[8:11]\n"
"s_waitcnt vmcnt(11)\n"
"v_mfma_f32_16x16x32_bf16 a[12:15], a[180:183], v[32:35], a[12:15]\n"
"s_waitcnt vmcnt(10)\n"
"v_mfma_f32_16x16x32_bf16 a[160:163], a[180:183], v[36:39], a[160:163]\n"
"v_mfma_f32_16x16x32_bf16 a[16:19], a[184:187], v[20:23], a[16:19]\n"
"v_mfma_f32_16x16x32_bf16 a[20:23], a[184:187], v[24:27], a[20:23]\n"
"v_mfma_f32_16x16x32_bf16 a[24:27], a[184:187], v[28:31], a[24:27]\n"
"v_mfma_f32_16x16x32_bf16 a[28:31], a[184:187], v[32:35], a[28:31]\n"
"v_mfma_f32_16x16x32_bf16 a[164:167], a[184:187], v[36:39], a[164:167]\n"
"v_mfma_f32_16x16x32_bf16 a[32:35], a[188:191], v[20:23], a[32:35]\n"
"v_mfma_f32_16x16x32_bf16 a[36:39], a[188:191], v[24:27], a[36:39]\n"
"v_mfma_f32_16x16x32_bf16 a[40:43], a[188:191], v[28:31], a[40:43]\n"
"v_mfma_f32_16x16x32_bf16 a[44:47], a[188:191], v[32:35], a[44:47]\n"
"v_mfma_f32_16x16x32_bf16 a[168:171], a[188:191], v[36:39], a[168:171]\n"
"v_mfma_f32_16x16x32_bf16 a[48:51], a[192:195], v[20:23], a[48:51]\n"
"v_mfma_f32_16x16x32_bf16 a[52:55], a[192:195], v[24:27], a[52:55]\n"
"v_mfma_f32_16x16x32_bf16 a[56:59], a[192:195], v[28:31], a[56:59]\n"
"v_mfma_f32_16x16x32_bf16 a[60:63], a[192:195], v[32:35], a[60:63]\n"
"v_mfma_f32_16x16x32_bf16 a[172:175], a[192:195], v[36:39], a[172:175]\n"
"v_mfma_f32_16x16x32_bf16 a[64:67], a[196:199], v[20:23], a[64:67]\n"
"v_mfma_f32_16x16x32_bf16 a[68:71], a[196:199], v[24:27], a[68:71]\n"
"v_mfma_f32_16x16x32_bf16 a[72:75], a[196:199], v[28:31], a[72:75]\n"
"v_mfma_f32_16x16x32_bf16 a[76:79], a[196:199], v[32:35], a[76:79]\n"
"v_mfma_f32_16x16x32_bf16 a[176:179], a[196:199], v[36:39], a[176:179]\n"
"s_waitcnt vmcnt(63) lgkmcnt(0)\n"
"s_barrier\n"
"v_add_u32 v80, 0xfffe2000, v80\n"
"v_add_u32 v81, 0xfffe2000, v81\n"
"ds_read_b64_tr_b16 a[180:181], v80 offset:0\n"
"v_mfma_f32_16x16x32_bf16 a[80:83], a[200:203], v[20:23], a[80:83]\n"
"ds_read_b64_tr_b16 a[182:183], v80 offset:2560\n"
"v_mfma_f32_16x16x32_bf16 a[84:87], a[200:203], v[24:27], a[84:87]\n"
"ds_read_b64_tr_b16 a[184:185], v81 offset:0\n"
"v_add_u32 v82, 0xfffe2000, v82\n"
"v_add_u32 v83, 0xfffe2000, v83\n"
"v_mfma_f32_16x16x32_bf16 a[88:91], a[200:203], v[28:31], a[88:91]\n"
"ds_read_b64_tr_b16 a[186:187], v81 offset:2560\n"
"v_mfma_f32_16x16x32_bf16 a[92:95], a[200:203], v[32:35], a[92:95]\n"
"ds_read_b64_tr_b16 a[188:189], v80 offset:512\n"
"v_mfma_f32_16x16x32_bf16 a[96:99], a[204:207], v[20:23], a[96:99]\n"
"ds_read_b64_tr_b16 a[190:191], v80 offset:3072\n"
"v_mfma_f32_16x16x32_bf16 a[100:103], a[204:207], v[24:27], a[100:103]\n"
"ds_read_b64_tr_b16 a[192:193], v81 offset:512\n"
"v_mfma_f32_16x16x32_bf16 a[104:107], a[204:207], v[28:31], a[104:107]\n"
"ds_read_b64_tr_b16 a[194:195], v81 offset:3072\n"
"v_mfma_f32_16x16x32_bf16 a[108:111], a[204:207], v[32:35], a[108:111]\n"
"ds_read_b64_tr_b16 a[196:197], v80 offset:1024\n"
"v_mfma_f32_16x16x32_bf16 a[112:115], a[208:211], v[20:23], a[112:115]\n"
"ds_read_b64_tr_b16 a[198:199], v80 offset:3584\n"
"v_mfma_f32_16x16x32_bf16 a[116:119], a[208:211], v[24:27], a[116:119]\n"
"ds_read_b64_tr_b16 a[200:201], v82 offset:1024\n"
"v_mfma_f32_16x16x32_bf16 a[120:123], a[208:211], v[28:31], a[120:123]\n"
"ds_read_b64_tr_b16 a[202:203], v82 offset:3584\n"
"v_mfma_f32_16x16x32_bf16 a[124:127], a[208:211], v[32:35], a[124:127]\n"
"ds_read_b64_tr_b16 a[204:205], v83 offset:1536\n"
"v_mfma_f32_16x16x32_bf16 a[128:131], a[212:215], v[20:23], a[128:131]\n"
"ds_read_b64_tr_b16 a[206:207], v83 offset:4096\n"
"v_mfma_f32_16x16x32_bf16 a[132:135], a[212:215], v[24:27], a[132:135]\n"
"ds_read_b64_tr_b16 a[208:209], v82 offset:1536\n"
"v_mfma_f32_16x16x32_bf16 a[136:139], a[212:215], v[28:31], a[136:139]\n"
"s_waitcnt lgkmcnt(11)\n"
"ds_read_b64_tr_b16 a[210:211], v82 offset:4096\n"
"v_mfma_f32_16x16x32_bf16 a[140:143], a[212:215], v[32:35], a[140:143]\n"
"v_mfma_f32_16x16x32_bf16 a[144:147], a[216:219], v[20:23], a[144:147]\n"
"v_mfma_f32_16x16x32_bf16 a[148:151], a[216:219], v[24:27], a[148:151]\n"
"ds_read_b64_tr_b16 a[212:213], v83 offset:2048\n"
"v_mfma_f32_16x16x32_bf16 a[152:155], a[216:219], v[28:31], a[152:155]\n"
"ds_read_b64_tr_b16 a[214:215], v83 offset:4608\n"
"v_mfma_f32_16x16x32_bf16 a[156:159], a[216:219], v[32:35], a[156:159]\n"
"s_waitcnt vmcnt(9)\n"
"v_mfma_f32_16x16x32_bf16 a[0:3], a[180:183], v[40:43], a[0:3]\n"
"ds_read_b64_tr_b16 a[216:217], v82 offset:2048\n"
"s_waitcnt vmcnt(8)\n"
"v_mfma_f32_16x16x32_bf16 a[4:7], a[180:183], v[44:47], a[4:7]\n"
"s_waitcnt lgkmcnt(8)\n"
"ds_read_b64_tr_b16 a[218:219], v82 offset:4608\n"
"s_waitcnt vmcnt(7)\n"
"v_mfma_f32_16x16x32_bf16 a[8:11], a[180:183], v[48:51], a[8:11]\n"
"s_waitcnt vmcnt(6)\n"
"v_mfma_f32_16x16x32_bf16 a[12:15], a[180:183], v[52:55], a[12:15]\n"
"s_waitcnt vmcnt(5)\n"
"v_mfma_f32_16x16x32_bf16 a[160:163], a[180:183], v[56:59], a[160:163]\n"
"v_mfma_f32_16x16x32_bf16 a[16:19], a[184:187], v[40:43], a[16:19]\n"
"v_mfma_f32_16x16x32_bf16 a[20:23], a[184:187], v[44:47], a[20:23]\n"
"v_mfma_f32_16x16x32_bf16 a[24:27], a[184:187], v[48:51], a[24:27]\n"
"v_mfma_f32_16x16x32_bf16 a[28:31], a[184:187], v[52:55], a[28:31]\n"
"v_mfma_f32_16x16x32_bf16 a[164:167], a[184:187], v[56:59], a[164:167]\n"
"v_mfma_f32_16x16x32_bf16 a[32:35], a[188:191], v[40:43], a[32:35]\n"
"v_mfma_f32_16x16x32_bf16 a[36:39], a[188:191], v[44:47], a[36:39]\n"
"v_mfma_f32_16x16x32_bf16 a[40:43], a[188:191], v[48:51], a[40:43]\n"
"v_mfma_f32_16x16x32_bf16 a[44:47], a[188:191], v[52:55], a[44:47]\n"
"v_mfma_f32_16x16x32_bf16 a[168:171], a[188:191], v[56:59], a[168:171]\n"
"v_mfma_f32_16x16x32_bf16 a[48:51], a[192:195], v[40:43], a[48:51]\n"
"v_mfma_f32_16x16x32_bf16 a[52:55], a[192:195], v[44:47], a[52:55]\n"
"v_mfma_f32_16x16x32_bf16 a[56:59], a[192:195], v[48:51], a[56:59]\n"
"v_mfma_f32_16x16x32_bf16 a[60:63], a[192:195], v[52:55], a[60:63]\n"
"v_mfma_f32_16x16x32_bf16 a[172:175], a[192:195], v[56:59], a[172:175]\n"
"v_mfma_f32_16x16x32_bf16 a[64:67], a[196:199], v[40:43], a[64:67]\n"
"v_mfma_f32_16x16x32_bf16 a[68:71], a[196:199], v[44:47], a[68:71]\n"
"v_mfma_f32_16x16x32_bf16 a[72:75], a[196:199], v[48:51], a[72:75]\n"
"v_mfma_f32_16x16x32_bf16 a[76:79], a[196:199], v[52:55], a[76:79]\n"
"v_mfma_f32_16x16x32_bf16 a[176:179], a[196:199], v[56:59], a[176:179]\n"
"s_waitcnt vmcnt(63) lgkmcnt(0)\n"
"s_barrier\n"
"v_add_u32 v80, 0x5000, v80\n"
"v_add_u32 v81, 0x5000, v81\n"
"ds_read_b64_tr_b16 a[180:181], v80 offset:0\n"
"v_mfma_f32_16x16x32_bf16 a[80:83], a[200:203], v[40:43], a[80:83]\n"
"ds_read_b64_tr_b16 a[182:183], v80 offset:2560\n"
"v_mfma_f32_16x16x32_bf16 a[84:87], a[200:203], v[44:47], a[84:87]\n"
"ds_read_b64_tr_b16 a[184:185], v81 offset:0\n"
"v_add_u32 v82, 0x5000, v82\n"
"v_add_u32 v83, 0x5000, v83\n"
"v_mfma_f32_16x16x32_bf16 a[88:91], a[200:203], v[48:51], a[88:91]\n"
"ds_read_b64_tr_b16 a[186:187], v81 offset:2560\n"
"v_mfma_f32_16x16x32_bf16 a[92:95], a[200:203], v[52:55], a[92:95]\n"
"ds_read_b64_tr_b16 a[188:189], v80 offset:512\n"
"v_mfma_f32_16x16x32_bf16 a[96:99], a[204:207], v[40:43], a[96:99]\n"
"ds_read_b64_tr_b16 a[190:191], v80 offset:3072\n"
"v_mfma_f32_16x16x32_bf16 a[100:103], a[204:207], v[44:47], a[100:103]\n"
"ds_read_b64_tr_b16 a[192:193], v81 offset:512\n"
"v_mfma_f32_16x16x32_bf16 a[104:107], a[204:207], v[48:51], a[104:107]\n"
"ds_read_b64_tr_b16 a[194:195], v81 offset:3072\n"
"v_mfma_f32_16x16x32_bf16 a[108:111], a[204:207], v[52:55], a[108:111]\n"
"ds_read_b64_tr_b16 a[196:197], v80 offset:1024\n"
"v_mfma_f32_16x16x32_bf16 a[112:115], a[208:211], v[40:43], a[112:115]\n"
"ds_read_b64_tr_b16 a[198:199], v80 offset:3584\n"
"v_mfma_f32_16x16x32_bf16 a[116:119], a[208:211], v[44:47], a[116:119]\n"
"ds_read_b64_tr_b16 a[200:201], v82 offset:1024\n"
"v_mfma_f32_16x16x32_bf16 a[120:123], a[208:211], v[48:51], a[120:123]\n"
"ds_read_b64_tr_b16 a[202:203], v82 offset:3584\n"
"v_mfma_f32_16x16x32_bf16 a[124:127], a[208:211], v[52:55], a[124:127]\n"
"ds_read_b64_tr_b16 a[204:205], v83 offset:1536\n"
"v_mfma_f32_16x16x32_bf16 a[128:131], a[212:215], v[40:43], a[128:131]\n"
"ds_read_b64_tr_b16 a[206:207], v83 offset:4096\n"
"v_mfma_f32_16x16x32_bf16 a[132:135], a[212:215], v[44:47], a[132:135]\n"
"ds_read_b64_tr_b16 a[208:209], v82 offset:1536\n"
"v_mfma_f32_16x16x32_bf16 a[136:139], a[212:215], v[48:51], a[136:139]\n"
"s_waitcnt lgkmcnt(11)\n"
"ds_read_b64_tr_b16 a[210:211], v82 offset:4096\n"
"v_mfma_f32_16x16x32_bf16 a[140:143], a[212:215], v[52:55], a[140:143]\n"
"v_mfma_f32_16x16x32_bf16 a[144:147], a[216:219], v[40:43], a[144:147]\n"
"v_mfma_f32_16x16x32_bf16 a[148:151], a[216:219], v[44:47], a[148:151]\n"
"ds_read_b64_tr_b16 a[212:213], v83 offset:2048\n"
"v_mfma_f32_16x16x32_bf16 a[152:155], a[216:219], v[48:51], a[152:155]\n"
"ds_read_b64_tr_b16 a[214:215], v83 offset:4608\n"
"v_mfma_f32_16x16x32_bf16 a[156:159], a[216:219], v[52:55], a[156:159]\n"
"s_waitcnt vmcnt(4)\n"
"v_mfma_f32_16x16x32_bf16 a[0:3], a[180:183], v[60:63], a[0:3]\n"
"ds_read_b64_tr_b16 a[216:217], v82 offset:2048\n"
"s_waitcnt vmcnt(3)\n"
"v_mfma_f32_16x16x32_bf16 a[4:7], a[180:183], v[64:67], a[4:7]\n"
"s_waitcnt lgkmcnt(8)\n"
"ds_read_b64_tr_b16 a[218:219], v82 offset:4608\n"
"s_waitcnt vmcnt(2)\n"
"v_mfma_f32_16x16x32_bf16 a[8:11], a[180:183], v[68:71], a[8:11]\n"
"s_waitcnt vmcnt(1)\n"
"v_mfma_f32_16x16x32_bf16 a[12:15], a[180:183], v[72:75], a[12:15]\n"
"s_waitcnt vmcnt(0)\n"
"v_mfma_f32_16x16x32_bf16 a[160:163], a[180:183], v[76:79], a[160:163]\n"
"v_mfma_f32_16x16x32_bf16 a[16:19], a[184:187], v[60:63], a[16:19]\n"
"v_mfma_f32_16x16x32_bf16 a[20:23], a[184:187], v[64:67], a[20:23]\n"
"v_mfma_f32_16x16x32_bf16 a[24:27], a[184:187], v[68:71], a[24:27]\n"
"v_mfma_f32_16x16x32_bf16 a[28:31], a[184:187], v[72:75], a[28:31]\n"
"v_mfma_f32_16x16x32_bf16 a[164:167], a[184:187], v[76:79], a[164:167]\n"
"v_mfma_f32_16x16x32_bf16 a[32:35], a[188:191], v[60:63], a[32:35]\n"
"v_mfma_f32_16x16x32_bf16 a[36:39], a[188:191], v[64:67], a[36:39]\n"
"v_mfma_f32_16x16x32_bf16 a[40:43], a[188:191], v[68:71], a[40:43]\n"
"v_mfma_f32_16x16x32_bf16 a[44:47], a[188:191], v[72:75], a[44:47]\n"
"v_mfma_f32_16x16x32_bf16 a[168:171], a[188:191], v[76:79], a[168:171]\n"
"v_mfma_f32_16x16x32_bf16 a[48:51], a[192:195], v[60:63], a[48:51]\n"
"v_mfma_f32_16x16x32_bf16 a[52:55], a[192:195], v[64:67], a[52:55]\n"
"v_mfma_f32_16x16x32_bf16 a[56:59], a[192:195], v[68:71], a[56:59]\n"
"v_mfma_f32_16x16x32_bf16 a[60:63], a[192:195], v[72:75], a[60:63]\n"
"v_mfma_f32_16x16x32_bf16 a[172:175], a[192:195], v[76:79], a[172:175]\n"
"v_mfma_f32_16x16x32_bf16 a[64:67], a[196:199], v[60:63], a[64:67]\n"
"v_mfma_f32_16x16x32_bf16 a[68:71], a[196:199], v[64:67], a[68:71]\n"
"v_mfma_f32_16x16x32_bf16 a[72:75], a[196:199], v[68:71], a[72:75]\n"
"v_mfma_f32_16x16x32_bf16 a[76:79], a[196:199], v[72:75], a[76:79]\n"
"v_mfma_f32_16x16x32_bf16 a[176:179], a[196:199], v[76:79], a[176:179]\n"
"s_waitcnt lgkmcnt(0)\n"
"v_mfma_f32_16x16x32_bf16 a[80:83], a[200:203], v[60:63], a[80:83]\n"
"v_mfma_f32_16x16x32_bf16 a[84:87], a[200:203], v[64:67], a[84:87]\n"
"v_mfma_f32_16x16x32_bf16 a[88:91], a[200:203], v[68:71], a[88:91]\n"
"v_mfma_f32_16x16x32_bf16 a[92:95], a[200:203], v[72:75], a[92:95]\n"
"v_mfma_f32_16x16x32_bf16 a[96:99], a[204:207], v[60:63], a[96:99]\n"
"v_mfma_f32_16x16x32_bf16 a[100:103], a[204:207], v[64:67], a[100:103]\n"
"v_mfma_f32_16x16x32_bf16 a[104:107], a[204:207], v[68:71], a[104:107]\n"
"v_mfma_f32_16x16x32_bf16 a[108:111], a[204:207], v[72:75], a[108:111]\n"
"v_mfma_f32_16x16x32_bf16 a[112:115], a[208:211], v[60:63], a[112:115]\n"
"v_mfma_f32_16x16x32_bf16 a[116:119], a[208:211], v[64:67], a[116:119]\n"
"v_mfma_f32_16x16x32_bf16 a[120:123], a[208:211], v[68:71], a[120:123]\n"
"v_mfma_f32_16x16x32_bf16 a[124:127], a[208:211], v[72:75], a[124:127]\n"
"v_mfma_f32_16x16x32_bf16 a[128:131], a[212:215], v[60:63], a[128:131]\n"
"v_mfma_f32_16x16x32_bf16 a[132:135], a[212:215], v[64:67], a[132:135]\n"
"v_mfma_f32_16x16x32_bf16 a[136:139], a[212:215], v[68:71], a[136:139]\n"
"v_mfma_f32_16x16x32_bf16 a[140:143], a[212:215], v[72:75], a[140:143]\n"
"v_mfma_f32_16x16x32_bf16 a[144:147], a[216:219], v[60:63], a[144:147]\n"
"v_mfma_f32_16x16x32_bf16 a[148:151], a[216:219], v[64:67], a[148:151]\n"
"v_mfma_f32_16x16x32_bf16 a[152:155], a[216:219], v[68:71], a[152:155]\n"
"v_mfma_f32_16x16x32_bf16 a[156:159], a[216:219], v[72:75], a[156:159]\n"
"s_mul_i32 s21, s15, 23552\n"
"s_xor_b32 s22, s15, 2\n"
"s_mul_i32 s22, s22, 23552\n"
"v_lshlrev_b32 v100, 4, v247\n"
"v_add_u32 v101, s22, v100\n"
"v_add_u32 v100, s21, v100\n"
"s_lshl_b32 s21, s18, 3\n"
"s_add_u32 s21, s21, s19\n"
"s_lshl_b32 s21, s21, 2\n"
"s_add_u32 s21, s21, s80\n"
"s_lshl_b32 s74, s21, 4\n"
"s_lshl_b32 s21, s17, 1\n"
"s_add_u32 s21, s21, s16\n"
"s_lshl_b32 s21, s21, 2\n"
"s_add_u32 s21, s21, s81\n"
"s_add_u32 s21, s21, s74\n"
"s_mul_i32 s77, s21, 12288\n"
"s_mul_i32 s21, s17, 5120\n"
"s_add_u32 s78, s77, s21\n"
"s_sub_u32 s79, s77, s21\n"
"v_lshlrev_b32 v102, 4, v247\n"
"v_lshlrev_b32 v103, 3, v247\n"
"s_barrier\n"
"s_nop 7\n"
"s_cmp_eq_u32 s16, 0\n"
"s_cbranch_scc1 Lr1a_%=\n"
"ds_write_b128 v100, a[0:3] offset:0\n"
"ds_write_b128 v100, a[4:7] offset:1024\n"
"ds_write_b128 v100, a[16:19] offset:2048\n"
"ds_write_b128 v100, a[20:23] offset:3072\n"
"ds_write_b128 v100, a[32:35] offset:4096\n"
"ds_write_b128 v100, a[36:39] offset:5120\n"
"ds_write_b128 v100, a[48:51] offset:6144\n"
"ds_write_b128 v100, a[52:55] offset:7168\n"
"ds_write_b128 v100, a[64:67] offset:8192\n"
"ds_write_b128 v100, a[68:71] offset:9216\n"
"ds_write_b128 v100, a[80:83] offset:10240\n"
"ds_write_b128 v100, a[84:87] offset:11264\n"
"ds_write_b128 v100, a[96:99] offset:12288\n"
"ds_write_b128 v100, a[100:103] offset:13312\n"
"ds_write_b128 v100, a[112:115] offset:14336\n"
"s_waitcnt lgkmcnt(14)\n"
"ds_write_b128 v100, a[116:119] offset:15360\n"
"s_waitcnt lgkmcnt(14)\n"
"ds_write_b128 v100, a[128:131] offset:16384\n"
"s_waitcnt lgkmcnt(14)\n"
"ds_write_b128 v100, a[132:135] offset:17408\n"
"s_waitcnt lgkmcnt(14)\n"
"ds_write_b128 v100, a[144:147] offset:18432\n"
"s_waitcnt lgkmcnt(14)\n"
"ds_write_b128 v100, a[148:151] offset:19456\n"
"s_waitcnt lgkmcnt(14)\n"
"ds_write_b128 v100, a[160:163] offset:20480\n"
"s_waitcnt lgkmcnt(14)\n"
"ds_write_b128 v100, a[164:167] offset:21504\n"
"s_waitcnt lgkmcnt(14)\n"
"ds_write_b128 v100, a[168:171] offset:22528\n"
"v_accvgpr_read_b32 v0, a8\n"
"v_accvgpr_read_b32 v1, a9\n"
"v_accvgpr_read_b32 v2, a10\n"
"v_accvgpr_read_b32 v3, a11\n"
"v_accvgpr_read_b32 v4, a12\n"
"v_accvgpr_read_b32 v5, a13\n"
"v_accvgpr_read_b32 v6, a14\n"
"v_accvgpr_read_b32 v7, a15\n"
"v_accvgpr_read_b32 v8, a24\n"
"v_accvgpr_read_b32 v9, a25\n"
"v_accvgpr_read_b32 v10, a26\n"
"v_accvgpr_read_b32 v11, a27\n"
"v_accvgpr_read_b32 v12, a28\n"
"v_accvgpr_read_b32 v13, a29\n"
"v_accvgpr_read_b32 v14, a30\n"
"v_accvgpr_read_b32 v15, a31\n"
"v_accvgpr_read_b32 v16, a40\n"
"v_accvgpr_read_b32 v17, a41\n"
"v_accvgpr_read_b32 v18, a42\n"
"v_accvgpr_read_b32 v19, a43\n"
"v_accvgpr_read_b32 v20, a44\n"
"v_accvgpr_read_b32 v21, a45\n"
"v_accvgpr_read_b32 v22, a46\n"
"v_accvgpr_read_b32 v23, a47\n"
"v_accvgpr_read_b32 v24, a56\n"
"v_accvgpr_read_b32 v25, a57\n"
"v_accvgpr_read_b32 v26, a58\n"
"v_accvgpr_read_b32 v27, a59\n"
"v_accvgpr_read_b32 v28, a60\n"
"v_accvgpr_read_b32 v29, a61\n"
"v_accvgpr_read_b32 v30, a62\n"
"v_accvgpr_read_b32 v31, a63\n"
"v_accvgpr_read_b32 v32, a72\n"
"v_accvgpr_read_b32 v33, a73\n"
"v_accvgpr_read_b32 v34, a74\n"
"v_accvgpr_read_b32 v35, a75\n"
"v_accvgpr_read_b32 v36, a76\n"
"v_accvgpr_read_b32 v37, a77\n"
"v_accvgpr_read_b32 v38, a78\n"
"v_accvgpr_read_b32 v39, a79\n"
"v_accvgpr_read_b32 v40, a88\n"
"v_accvgpr_read_b32 v41, a89\n"
"v_accvgpr_read_b32 v42, a90\n"
"v_accvgpr_read_b32 v43, a91\n"
"v_accvgpr_read_b32 v44, a92\n"
"v_accvgpr_read_b32 v45, a93\n"
"v_accvgpr_read_b32 v46, a94\n"
"v_accvgpr_read_b32 v47, a95\n"
"v_accvgpr_read_b32 v48, a104\n"
"v_accvgpr_read_b32 v49, a105\n"
"v_accvgpr_read_b32 v50, a106\n"
"v_accvgpr_read_b32 v51, a107\n"
"v_accvgpr_read_b32 v52, a108\n"
"v_accvgpr_read_b32 v53, a109\n"
"v_accvgpr_read_b32 v54, a110\n"
"v_accvgpr_read_b32 v55, a111\n"
"v_accvgpr_read_b32 v56, a120\n"
"v_accvgpr_read_b32 v57, a121\n"
"v_accvgpr_read_b32 v58, a122\n"
"v_accvgpr_read_b32 v59, a123\n"
"v_accvgpr_read_b32 v60, a124\n"
"v_accvgpr_read_b32 v61, a125\n"
"v_accvgpr_read_b32 v62, a126\n"
"v_accvgpr_read_b32 v63, a127\n"
"v_accvgpr_read_b32 v64, a136\n"
"v_accvgpr_read_b32 v65, a137\n"
"v_accvgpr_read_b32 v66, a138\n"
"v_accvgpr_read_b32 v67, a139\n"
"v_accvgpr_read_b32 v68, a140\n"
"v_accvgpr_read_b32 v69, a141\n"
"v_accvgpr_read_b32 v70, a142\n"
"v_accvgpr_read_b32 v71, a143\n"
"v_accvgpr_read_b32 v72, a152\n"
"v_accvgpr_read_b32 v73, a153\n"
"v_accvgpr_read_b32 v74, a154\n"
"v_accvgpr_read_b32 v75, a155\n"
"v_accvgpr_read_b32 v76, a156\n"
"v_accvgpr_read_b32 v77, a157\n"
"v_accvgpr_read_b32 v78, a158\n"
"v_accvgpr_read_b32 v79, a159\n"
"v_accvgpr_read_b32 v80, a172\n"
"v_accvgpr_read_b32 v81, a173\n"
"v_accvgpr_read_b32 v82, a174\n"
"v_accvgpr_read_b32 v83, a175\n"
"v_accvgpr_read_b32 v84, a176\n"
"v_accvgpr_read_b32 v85, a177\n"
"v_accvgpr_read_b32 v86, a178\n"
"v_accvgpr_read_b32 v87, a179\n"
"s_waitcnt lgkmcnt(0)\n"
"s_barrier\n"
"ds_read_b128 v[112:115], v101 offset:0\n"
"ds_read_b128 v[116:119], v101 offset:1024\n"
"ds_read_b128 v[120:123], v101 offset:2048\n"
"ds_read_b128 v[124:127], v101 offset:3072\n"
"ds_read_b128 v[128:131], v101 offset:4096\n"
"ds_read_b128 v[132:135], v101 offset:5120\n"
"ds_read_b128 v[136:139], v101 offset:6144\n"
"ds_read_b128 v[140:143], v101 offset:7168\n"
"ds_read_b128 v[144:147], v101 offset:8192\n"
"ds_read_b128 v[148:151], v101 offset:9216\n"
"ds_read_b128 v[152:155], v101 offset:10240\n"
"ds_read_b128 v[156:159], v101 offset:11264\n"
"ds_read_b128 v[160:163], v101 offset:12288\n"
"ds_read_b128 v[164:167], v101 offset:13312\n"
"ds_read_b128 v[168:171], v101 offset:14336\n"
"s_waitcnt lgkmcnt(14)\n"
"ds_read_b128 v[172:175], v101 offset:15360\n"
"s_waitcnt lgkmcnt(14)\n"
"ds_read_b128 v[176:179], v101 offset:16384\n"
"s_waitcnt lgkmcnt(14)\n"
"ds_read_b128 v[180:183], v101 offset:17408\n"
"s_waitcnt lgkmcnt(14)\n"
"ds_read_b128 v[184:187], v101 offset:18432\n"
"s_waitcnt lgkmcnt(14)\n"
"ds_read_b128 v[188:191], v101 offset:19456\n"
"s_waitcnt lgkmcnt(14)\n"
"ds_read_b128 v[192:195], v101 offset:20480\n"
"s_waitcnt lgkmcnt(14)\n"
"ds_read_b128 v[196:199], v101 offset:21504\n"
"v_pk_add_f32 v[0:1], v[0:1], v[112:113]\n"
"v_pk_add_f32 v[2:3], v[2:3], v[114:115]\n"
"v_pk_add_f32 v[4:5], v[4:5], v[116:117]\n"
"v_pk_add_f32 v[6:7], v[6:7], v[118:119]\n"
"v_cvt_pk_bf16_f32 v104, v0, v1\n"
"v_cvt_pk_bf16_f32 v105, v2, v3\n"
"v_cvt_pk_bf16_f32 v106, v4, v5\n"
"v_cvt_pk_bf16_f32 v107, v6, v7\n"
"s_add_u32 s72, s78, 0\n"
"buffer_store_dwordx4 v[104:107], v102, s[32:35], s72 offen sc1\n"
"v_pk_add_f32 v[8:9], v[8:9], v[120:121]\n"
"v_pk_add_f32 v[10:11], v[10:11], v[122:123]\n"
"v_pk_add_f32 v[12:13], v[12:13], v[124:125]\n"
"v_pk_add_f32 v[14:15], v[14:15], v[126:127]\n"
"v_cvt_pk_bf16_f32 v108, v8, v9\n"
"v_cvt_pk_bf16_f32 v109, v10, v11\n"
"v_cvt_pk_bf16_f32 v110, v12, v13\n"
"v_cvt_pk_bf16_f32 v111, v14, v15\n"
"s_add_u32 s72, s78, 1024\n"
"buffer_store_dwordx4 v[108:111], v102, s[32:35], s72 offen sc1\n"
"v_pk_add_f32 v[16:17], v[16:17], v[128:129]\n"
"v_pk_add_f32 v[18:19], v[18:19], v[130:131]\n"
"v_pk_add_f32 v[20:21], v[20:21], v[132:133]\n"
"v_pk_add_f32 v[22:23], v[22:23], v[134:135]\n"
"v_cvt_pk_bf16_f32 v104, v16, v17\n"
"v_cvt_pk_bf16_f32 v105, v18, v19\n"
"v_cvt_pk_bf16_f32 v106, v20, v21\n"
"v_cvt_pk_bf16_f32 v107, v22, v23\n"
"s_add_u32 s72, s78, 2048\n"
"buffer_store_dwordx4 v[104:107], v102, s[32:35], s72 offen sc1\n"
"v_pk_add_f32 v[24:25], v[24:25], v[136:137]\n"
"v_pk_add_f32 v[26:27], v[26:27], v[138:139]\n"
"s_waitcnt lgkmcnt(14)\n"
"v_pk_add_f32 v[28:29], v[28:29], v[140:141]\n"
"v_pk_add_f32 v[30:31], v[30:31], v[142:143]\n"
"v_cvt_pk_bf16_f32 v108, v24, v25\n"
"v_cvt_pk_bf16_f32 v109, v26, v27\n"
"v_cvt_pk_bf16_f32 v110, v28, v29\n"
"v_cvt_pk_bf16_f32 v111, v30, v31\n"
"s_add_u32 s72, s78, 3072\n"
"buffer_store_dwordx4 v[108:111], v102, s[32:35], s72 offen sc1\n"
"s_waitcnt lgkmcnt(13)\n"
"v_pk_add_f32 v[32:33], v[32:33], v[144:145]\n"
"v_pk_add_f32 v[34:35], v[34:35], v[146:147]\n"
"s_waitcnt lgkmcnt(12)\n"
"v_pk_add_f32 v[36:37], v[36:37], v[148:149]\n"
"v_pk_add_f32 v[38:39], v[38:39], v[150:151]\n"
"v_cvt_pk_bf16_f32 v104, v32, v33\n"
"v_cvt_pk_bf16_f32 v105, v34, v35\n"
"v_cvt_pk_bf16_f32 v106, v36, v37\n"
"v_cvt_pk_bf16_f32 v107, v38, v39\n"
"s_add_u32 s72, s78, 4096\n"
"buffer_store_dwordx4 v[104:107], v102, s[32:35], s72 offen sc1\n"
"s_waitcnt lgkmcnt(11)\n"
"v_pk_add_f32 v[40:41], v[40:41], v[152:153]\n"
"v_pk_add_f32 v[42:43], v[42:43], v[154:155]\n"
"s_waitcnt lgkmcnt(10)\n"
"v_pk_add_f32 v[44:45], v[44:45], v[156:157]\n"
"v_pk_add_f32 v[46:47], v[46:47], v[158:159]\n"
"v_cvt_pk_bf16_f32 v108, v40, v41\n"
"v_cvt_pk_bf16_f32 v109, v42, v43\n"
"v_cvt_pk_bf16_f32 v110, v44, v45\n"
"v_cvt_pk_bf16_f32 v111, v46, v47\n"
"s_add_u32 s72, s79, 5120\n"
"buffer_store_dwordx4 v[108:111], v102, s[32:35], s72 offen sc1\n"
"s_waitcnt lgkmcnt(9)\n"
"v_pk_add_f32 v[48:49], v[48:49], v[160:161]\n"
"v_pk_add_f32 v[50:51], v[50:51], v[162:163]\n"
"s_waitcnt lgkmcnt(8)\n"
"v_pk_add_f32 v[52:53], v[52:53], v[164:165]\n"
"v_pk_add_f32 v[54:55], v[54:55], v[166:167]\n"
"v_cvt_pk_bf16_f32 v104, v48, v49\n"
"v_cvt_pk_bf16_f32 v105, v50, v51\n"
"v_cvt_pk_bf16_f32 v106, v52, v53\n"
"v_cvt_pk_bf16_f32 v107, v54, v55\n"
"s_add_u32 s72, s79, 6144\n"
"buffer_store_dwordx4 v[104:107], v102, s[32:35], s72 offen sc1\n"
"s_waitcnt lgkmcnt(7)\n"
"v_pk_add_f32 v[56:57], v[56:57], v[168:169]\n"
"v_pk_add_f32 v[58:59], v[58:59], v[170:171]\n"
"s_waitcnt lgkmcnt(6)\n"
"v_pk_add_f32 v[60:61], v[60:61], v[172:173]\n"
"v_pk_add_f32 v[62:63], v[62:63], v[174:175]\n"
"v_cvt_pk_bf16_f32 v108, v56, v57\n"
"v_cvt_pk_bf16_f32 v109, v58, v59\n"
"v_cvt_pk_bf16_f32 v110, v60, v61\n"
"v_cvt_pk_bf16_f32 v111, v62, v63\n"
"s_add_u32 s72, s79, 7168\n"
"buffer_store_dwordx4 v[108:111], v102, s[32:35], s72 offen sc1\n"
"s_waitcnt lgkmcnt(5)\n"
"v_pk_add_f32 v[64:65], v[64:65], v[176:177]\n"
"v_pk_add_f32 v[66:67], v[66:67], v[178:179]\n"
"s_waitcnt lgkmcnt(4)\n"
"v_pk_add_f32 v[68:69], v[68:69], v[180:181]\n"
"v_pk_add_f32 v[70:71], v[70:71], v[182:183]\n"
"v_cvt_pk_bf16_f32 v104, v64, v65\n"
"v_cvt_pk_bf16_f32 v105, v66, v67\n"
"v_cvt_pk_bf16_f32 v106, v68, v69\n"
"v_cvt_pk_bf16_f32 v107, v70, v71\n"
"s_add_u32 s72, s79, 8192\n"
"buffer_store_dwordx4 v[104:107], v102, s[32:35], s72 offen sc1\n"
"s_waitcnt lgkmcnt(3)\n"
"v_pk_add_f32 v[72:73], v[72:73], v[184:185]\n"
"v_pk_add_f32 v[74:75], v[74:75], v[186:187]\n"
"s_waitcnt lgkmcnt(2)\n"
"v_pk_add_f32 v[76:77], v[76:77], v[188:189]\n"
"v_pk_add_f32 v[78:79], v[78:79], v[190:191]\n"
"v_cvt_pk_bf16_f32 v108, v72, v73\n"
"v_cvt_pk_bf16_f32 v109, v74, v75\n"
"v_cvt_pk_bf16_f32 v110, v76, v77\n"
"v_cvt_pk_bf16_f32 v111, v78, v79\n"
"s_add_u32 s72, s79, 9216\n"
"buffer_store_dwordx4 v[108:111], v102, s[32:35], s72 offen sc1\n"
"s_waitcnt lgkmcnt(0)\n"
"v_pk_add_f32 v[80:81], v[80:81], v[192:193]\n"
"v_pk_add_f32 v[82:83], v[82:83], v[194:195]\n"
"v_cvt_pk_bf16_f32 v96, v80, v81\n"
"v_cvt_pk_bf16_f32 v97, v82, v83\n"
"s_mov_b32 s72, 0x26800\n"
"s_cmp_eq_u32 s17, 0\n"
"s_cselect_b32 s72, 0xe800, s72\n"
"s_add_u32 s21, s74, s81\n"
"s_mul_i32 s21, s21, 12288\n"
"s_add_u32 s72, s72, s21\n"
"buffer_store_dwordx2 v[96:97], v103, s[32:35], s72 offen sc1\n"
"v_pk_add_f32 v[84:85], v[84:85], v[196:197]\n"
"v_pk_add_f32 v[86:87], v[86:87], v[198:199]\n"
"v_cvt_pk_bf16_f32 v98, v84, v85\n"
"v_cvt_pk_bf16_f32 v99, v86, v87\n"
"s_mov_b32 s72, 0x26a00\n"
"s_cmp_eq_u32 s17, 0\n"
"s_cselect_b32 s72, 0xea00, s72\n"
"s_add_u32 s21, s74, s81\n"
"s_mul_i32 s21, s21, 12288\n"
"s_add_u32 s72, s72, s21\n"
"buffer_store_dwordx2 v[98:99], v103, s[32:35], s72 offen sc1\n"
"s_branch Lr1b_%=\n"
"Lr1a_%=:\n"
"ds_write_b128 v100, a[8:11] offset:0\n"
"ds_write_b128 v100, a[12:15] offset:1024\n"
"ds_write_b128 v100, a[24:27] offset:2048\n"
"ds_write_b128 v100, a[28:31] offset:3072\n"
"ds_write_b128 v100, a[40:43] offset:4096\n"
"ds_write_b128 v100, a[44:47] offset:5120\n"
"ds_write_b128 v100, a[56:59] offset:6144\n"
"ds_write_b128 v100, a[60:63] offset:7168\n"
"ds_write_b128 v100, a[72:75] offset:8192\n"
"ds_write_b128 v100, a[76:79] offset:9216\n"
"ds_write_b128 v100, a[88:91] offset:10240\n"
"ds_write_b128 v100, a[92:95] offset:11264\n"
"ds_write_b128 v100, a[104:107] offset:12288\n"
"ds_write_b128 v100, a[108:111] offset:13312\n"
"ds_write_b128 v100, a[120:123] offset:14336\n"
"s_waitcnt lgkmcnt(14)\n"
"ds_write_b128 v100, a[124:127] offset:15360\n"
"s_waitcnt lgkmcnt(14)\n"
"ds_write_b128 v100, a[136:139] offset:16384\n"
"s_waitcnt lgkmcnt(14)\n"
"ds_write_b128 v100, a[140:143] offset:17408\n"
"s_waitcnt lgkmcnt(14)\n"
"ds_write_b128 v100, a[152:155] offset:18432\n"
"s_waitcnt lgkmcnt(14)\n"
"ds_write_b128 v100, a[156:159] offset:19456\n"
"s_waitcnt lgkmcnt(14)\n"
"ds_write_b128 v100, a[172:175] offset:20480\n"
"s_waitcnt lgkmcnt(14)\n"
"ds_write_b128 v100, a[176:179] offset:21504\n"
"v_accvgpr_read_b32 v0, a0\n"
"v_accvgpr_read_b32 v1, a1\n"
"v_accvgpr_read_b32 v2, a2\n"
"v_accvgpr_read_b32 v3, a3\n"
"v_accvgpr_read_b32 v4, a4\n"
"v_accvgpr_read_b32 v5, a5\n"
"v_accvgpr_read_b32 v6, a6\n"
"v_accvgpr_read_b32 v7, a7\n"
"v_accvgpr_read_b32 v8, a16\n"
"v_accvgpr_read_b32 v9, a17\n"
"v_accvgpr_read_b32 v10, a18\n"
"v_accvgpr_read_b32 v11, a19\n"
"v_accvgpr_read_b32 v12, a20\n"
"v_accvgpr_read_b32 v13, a21\n"
"v_accvgpr_read_b32 v14, a22\n"
"v_accvgpr_read_b32 v15, a23\n"
"v_accvgpr_read_b32 v16, a32\n"
"v_accvgpr_read_b32 v17, a33\n"
"v_accvgpr_read_b32 v18, a34\n"
"v_accvgpr_read_b32 v19, a35\n"
"v_accvgpr_read_b32 v20, a36\n"
"v_accvgpr_read_b32 v21, a37\n"
"v_accvgpr_read_b32 v22, a38\n"
"v_accvgpr_read_b32 v23, a39\n"
"v_accvgpr_read_b32 v24, a48\n"
"v_accvgpr_read_b32 v25, a49\n"
"v_accvgpr_read_b32 v26, a50\n"
"v_accvgpr_read_b32 v27, a51\n"
"v_accvgpr_read_b32 v28, a52\n"
"v_accvgpr_read_b32 v29, a53\n"
"v_accvgpr_read_b32 v30, a54\n"
"v_accvgpr_read_b32 v31, a55\n"
"v_accvgpr_read_b32 v32, a64\n"
"v_accvgpr_read_b32 v33, a65\n"
"v_accvgpr_read_b32 v34, a66\n"
"v_accvgpr_read_b32 v35, a67\n"
"v_accvgpr_read_b32 v36, a68\n"
"v_accvgpr_read_b32 v37, a69\n"
"v_accvgpr_read_b32 v38, a70\n"
"v_accvgpr_read_b32 v39, a71\n"
"v_accvgpr_read_b32 v40, a80\n"
"v_accvgpr_read_b32 v41, a81\n"
"v_accvgpr_read_b32 v42, a82\n"
"v_accvgpr_read_b32 v43, a83\n"
"v_accvgpr_read_b32 v44, a84\n"
"v_accvgpr_read_b32 v45, a85\n"
"v_accvgpr_read_b32 v46, a86\n"
"v_accvgpr_read_b32 v47, a87\n"
"v_accvgpr_read_b32 v48, a96\n"
"v_accvgpr_read_b32 v49, a97\n"
"v_accvgpr_read_b32 v50, a98\n"
"v_accvgpr_read_b32 v51, a99\n"
"v_accvgpr_read_b32 v52, a100\n"
"v_accvgpr_read_b32 v53, a101\n"
"v_accvgpr_read_b32 v54, a102\n"
"v_accvgpr_read_b32 v55, a103\n"
"v_accvgpr_read_b32 v56, a112\n"
"v_accvgpr_read_b32 v57, a113\n"
"v_accvgpr_read_b32 v58, a114\n"
"v_accvgpr_read_b32 v59, a115\n"
"v_accvgpr_read_b32 v60, a116\n"
"v_accvgpr_read_b32 v61, a117\n"
"v_accvgpr_read_b32 v62, a118\n"
"v_accvgpr_read_b32 v63, a119\n"
"v_accvgpr_read_b32 v64, a128\n"
"v_accvgpr_read_b32 v65, a129\n"
"v_accvgpr_read_b32 v66, a130\n"
"v_accvgpr_read_b32 v67, a131\n"
"v_accvgpr_read_b32 v68, a132\n"
"v_accvgpr_read_b32 v69, a133\n"
"v_accvgpr_read_b32 v70, a134\n"
"v_accvgpr_read_b32 v71, a135\n"
"v_accvgpr_read_b32 v72, a144\n"
"v_accvgpr_read_b32 v73, a145\n"
"v_accvgpr_read_b32 v74, a146\n"
"v_accvgpr_read_b32 v75, a147\n"
"v_accvgpr_read_b32 v76, a148\n"
"v_accvgpr_read_b32 v77, a149\n"
"v_accvgpr_read_b32 v78, a150\n"
"v_accvgpr_read_b32 v79, a151\n"
"v_accvgpr_read_b32 v80, a160\n"
"v_accvgpr_read_b32 v81, a161\n"
"v_accvgpr_read_b32 v82, a162\n"
"v_accvgpr_read_b32 v83, a163\n"
"v_accvgpr_read_b32 v84, a164\n"
"v_accvgpr_read_b32 v85, a165\n"
"v_accvgpr_read_b32 v86, a166\n"
"v_accvgpr_read_b32 v87, a167\n"
"v_accvgpr_read_b32 v88, a168\n"
"v_accvgpr_read_b32 v89, a169\n"
"v_accvgpr_read_b32 v90, a170\n"
"v_accvgpr_read_b32 v91, a171\n"
"s_waitcnt lgkmcnt(0)\n"
"s_barrier\n"
"ds_read_b128 v[112:115], v101 offset:0\n"
"ds_read_b128 v[116:119], v101 offset:1024\n"
"ds_read_b128 v[120:123], v101 offset:2048\n"
"ds_read_b128 v[124:127], v101 offset:3072\n"
"ds_read_b128 v[128:131], v101 offset:4096\n"
"ds_read_b128 v[132:135], v101 offset:5120\n"
"ds_read_b128 v[136:139], v101 offset:6144\n"
"ds_read_b128 v[140:143], v101 offset:7168\n"
"ds_read_b128 v[144:147], v101 offset:8192\n"
"ds_read_b128 v[148:151], v101 offset:9216\n"
"ds_read_b128 v[152:155], v101 offset:10240\n"
"ds_read_b128 v[156:159], v101 offset:11264\n"
"ds_read_b128 v[160:163], v101 offset:12288\n"
"ds_read_b128 v[164:167], v101 offset:13312\n"
"ds_read_b128 v[168:171], v101 offset:14336\n"
"s_waitcnt lgkmcnt(14)\n"
"ds_read_b128 v[172:175], v101 offset:15360\n"
"s_waitcnt lgkmcnt(14)\n"
"ds_read_b128 v[176:179], v101 offset:16384\n"
"s_waitcnt lgkmcnt(14)\n"
"ds_read_b128 v[180:183], v101 offset:17408\n"
"s_waitcnt lgkmcnt(14)\n"
"ds_read_b128 v[184:187], v101 offset:18432\n"
"s_waitcnt lgkmcnt(14)\n"
"ds_read_b128 v[188:191], v101 offset:19456\n"
"s_waitcnt lgkmcnt(14)\n"
"ds_read_b128 v[192:195], v101 offset:20480\n"
"s_waitcnt lgkmcnt(14)\n"
"ds_read_b128 v[196:199], v101 offset:21504\n"
"s_waitcnt lgkmcnt(14)\n"
"ds_read_b128 v[200:203], v101 offset:22528\n"
"v_pk_add_f32 v[0:1], v[0:1], v[112:113]\n"
"v_pk_add_f32 v[2:3], v[2:3], v[114:115]\n"
"v_pk_add_f32 v[4:5], v[4:5], v[116:117]\n"
"v_pk_add_f32 v[6:7], v[6:7], v[118:119]\n"
"v_cvt_pk_bf16_f32 v104, v0, v1\n"
"v_cvt_pk_bf16_f32 v105, v2, v3\n"
"v_cvt_pk_bf16_f32 v106, v4, v5\n"
"v_cvt_pk_bf16_f32 v107, v6, v7\n"
"s_add_u32 s72, s78, 0\n"
"buffer_store_dwordx4 v[104:107], v102, s[32:35], s72 offen sc1\n"
"v_pk_add_f32 v[8:9], v[8:9], v[120:121]\n"
"v_pk_add_f32 v[10:11], v[10:11], v[122:123]\n"
"v_pk_add_f32 v[12:13], v[12:13], v[124:125]\n"
"v_pk_add_f32 v[14:15], v[14:15], v[126:127]\n"
"v_cvt_pk_bf16_f32 v108, v8, v9\n"
"v_cvt_pk_bf16_f32 v109, v10, v11\n"
"v_cvt_pk_bf16_f32 v110, v12, v13\n"
"v_cvt_pk_bf16_f32 v111, v14, v15\n"
"s_add_u32 s72, s78, 1024\n"
"buffer_store_dwordx4 v[108:111], v102, s[32:35], s72 offen sc1\n"
"v_pk_add_f32 v[16:17], v[16:17], v[128:129]\n"
"v_pk_add_f32 v[18:19], v[18:19], v[130:131]\n"
"v_pk_add_f32 v[20:21], v[20:21], v[132:133]\n"
"v_pk_add_f32 v[22:23], v[22:23], v[134:135]\n"
"v_cvt_pk_bf16_f32 v104, v16, v17\n"
"v_cvt_pk_bf16_f32 v105, v18, v19\n"
"v_cvt_pk_bf16_f32 v106, v20, v21\n"
"v_cvt_pk_bf16_f32 v107, v22, v23\n"
"s_add_u32 s72, s78, 2048\n"
"buffer_store_dwordx4 v[104:107], v102, s[32:35], s72 offen sc1\n"
"v_pk_add_f32 v[24:25], v[24:25], v[136:137]\n"
"v_pk_add_f32 v[26:27], v[26:27], v[138:139]\n"
"v_pk_add_f32 v[28:29], v[28:29], v[140:141]\n"
"v_pk_add_f32 v[30:31], v[30:31], v[142:143]\n"
"v_cvt_pk_bf16_f32 v108, v24, v25\n"
"v_cvt_pk_bf16_f32 v109, v26, v27\n"
"v_cvt_pk_bf16_f32 v110, v28, v29\n"
"v_cvt_pk_bf16_f32 v111, v30, v31\n"
"s_add_u32 s72, s78, 3072\n"
"buffer_store_dwordx4 v[108:111], v102, s[32:35], s72 offen sc1\n"
"s_waitcnt lgkmcnt(14)\n"
"v_pk_add_f32 v[32:33], v[32:33], v[144:145]\n"
"v_pk_add_f32 v[34:35], v[34:35], v[146:147]\n"
"s_waitcnt lgkmcnt(13)\n"
"v_pk_add_f32 v[36:37], v[36:37], v[148:149]\n"
"v_pk_add_f32 v[38:39], v[38:39], v[150:151]\n"
"v_cvt_pk_bf16_f32 v104, v32, v33\n"
"v_cvt_pk_bf16_f32 v105, v34, v35\n"
"v_cvt_pk_bf16_f32 v106, v36, v37\n"
"v_cvt_pk_bf16_f32 v107, v38, v39\n"
"s_add_u32 s72, s78, 4096\n"
"buffer_store_dwordx4 v[104:107], v102, s[32:35], s72 offen sc1\n"
"s_waitcnt lgkmcnt(12)\n"
"v_pk_add_f32 v[40:41], v[40:41], v[152:153]\n"
"v_pk_add_f32 v[42:43], v[42:43], v[154:155]\n"
"s_waitcnt lgkmcnt(11)\n"
"v_pk_add_f32 v[44:45], v[44:45], v[156:157]\n"
"v_pk_add_f32 v[46:47], v[46:47], v[158:159]\n"
"v_cvt_pk_bf16_f32 v108, v40, v41\n"
"v_cvt_pk_bf16_f32 v109, v42, v43\n"
"v_cvt_pk_bf16_f32 v110, v44, v45\n"
"v_cvt_pk_bf16_f32 v111, v46, v47\n"
"s_add_u32 s72, s79, 5120\n"
"buffer_store_dwordx4 v[108:111], v102, s[32:35], s72 offen sc1\n"
"s_waitcnt lgkmcnt(10)\n"
"v_pk_add_f32 v[48:49], v[48:49], v[160:161]\n"
"v_pk_add_f32 v[50:51], v[50:51], v[162:163]\n"
"s_waitcnt lgkmcnt(9)\n"
"v_pk_add_f32 v[52:53], v[52:53], v[164:165]\n"
"v_pk_add_f32 v[54:55], v[54:55], v[166:167]\n"
"v_cvt_pk_bf16_f32 v104, v48, v49\n"
"v_cvt_pk_bf16_f32 v105, v50, v51\n"
"v_cvt_pk_bf16_f32 v106, v52, v53\n"
"v_cvt_pk_bf16_f32 v107, v54, v55\n"
"s_add_u32 s72, s79, 6144\n"
"buffer_store_dwordx4 v[104:107], v102, s[32:35], s72 offen sc1\n"
"s_waitcnt lgkmcnt(8)\n"
"v_pk_add_f32 v[56:57], v[56:57], v[168:169]\n"
"v_pk_add_f32 v[58:59], v[58:59], v[170:171]\n"
"s_waitcnt lgkmcnt(7)\n"
"v_pk_add_f32 v[60:61], v[60:61], v[172:173]\n"
"v_pk_add_f32 v[62:63], v[62:63], v[174:175]\n"
"v_cvt_pk_bf16_f32 v108, v56, v57\n"
"v_cvt_pk_bf16_f32 v109, v58, v59\n"
"v_cvt_pk_bf16_f32 v110, v60, v61\n"
"v_cvt_pk_bf16_f32 v111, v62, v63\n"
"s_add_u32 s72, s79, 7168\n"
"buffer_store_dwordx4 v[108:111], v102, s[32:35], s72 offen sc1\n"
"s_waitcnt lgkmcnt(6)\n"
"v_pk_add_f32 v[64:65], v[64:65], v[176:177]\n"
"v_pk_add_f32 v[66:67], v[66:67], v[178:179]\n"
"s_waitcnt lgkmcnt(5)\n"
"v_pk_add_f32 v[68:69], v[68:69], v[180:181]\n"
"v_pk_add_f32 v[70:71], v[70:71], v[182:183]\n"
"v_cvt_pk_bf16_f32 v104, v64, v65\n"
"v_cvt_pk_bf16_f32 v105, v66, v67\n"
"v_cvt_pk_bf16_f32 v106, v68, v69\n"
"v_cvt_pk_bf16_f32 v107, v70, v71\n"
"s_add_u32 s72, s79, 8192\n"
"buffer_store_dwordx4 v[104:107], v102, s[32:35], s72 offen sc1\n"
"s_waitcnt lgkmcnt(4)\n"
"v_pk_add_f32 v[72:73], v[72:73], v[184:185]\n"
"v_pk_add_f32 v[74:75], v[74:75], v[186:187]\n"
"s_waitcnt lgkmcnt(3)\n"
"v_pk_add_f32 v[76:77], v[76:77], v[188:189]\n"
"v_pk_add_f32 v[78:79], v[78:79], v[190:191]\n"
"v_cvt_pk_bf16_f32 v108, v72, v73\n"
"v_cvt_pk_bf16_f32 v109, v74, v75\n"
"v_cvt_pk_bf16_f32 v110, v76, v77\n"
"v_cvt_pk_bf16_f32 v111, v78, v79\n"
"s_add_u32 s72, s79, 9216\n"
"buffer_store_dwordx4 v[108:111], v102, s[32:35], s72 offen sc1\n"
"s_waitcnt lgkmcnt(0)\n"
"v_pk_add_f32 v[80:81], v[80:81], v[192:193]\n"
"v_pk_add_f32 v[82:83], v[82:83], v[194:195]\n"
"v_cvt_pk_bf16_f32 v96, v80, v81\n"
"v_cvt_pk_bf16_f32 v97, v82, v83\n"
"s_mov_b32 s72, 0xec00\n"
"s_cmp_eq_u32 s17, 0\n"
"s_cselect_b32 s72, 0x2800, s72\n"
"s_add_u32 s21, s74, s81\n"
"s_mul_i32 s21, s21, 12288\n"
"s_add_u32 s72, s72, s21\n"
"buffer_store_dwordx2 v[96:97], v103, s[32:35], s72 offen sc1\n"
"v_pk_add_f32 v[84:85], v[84:85], v[196:197]\n"
"v_pk_add_f32 v[86:87], v[86:87], v[198:199]\n"
"v_cvt_pk_bf16_f32 v98, v84, v85\n"
"v_cvt_pk_bf16_f32 v99, v86, v87\n"
"s_mov_b32 s72, 0x1a800\n"
"s_cmp_eq_u32 s17, 0\n"
"s_cselect_b32 s72, 0x2a00, s72\n"
"s_add_u32 s21, s74, s81\n"
"s_mul_i32 s21, s21, 12288\n"
"s_add_u32 s72, s72, s21\n"
"buffer_store_dwordx2 v[98:99], v103, s[32:35], s72 offen sc1\n"
"v_pk_add_f32 v[88:89], v[88:89], v[200:201]\n"
"v_pk_add_f32 v[90:91], v[90:91], v[202:203]\n"
"v_cvt_pk_bf16_f32 v100, v88, v89\n"
"v_cvt_pk_bf16_f32 v101, v90, v91\n"
"s_mov_b32 s72, 0x1aa00\n"
"s_cmp_eq_u32 s17, 0\n"
"s_cselect_b32 s72, 0x2c00, s72\n"
"s_add_u32 s21, s74, s81\n"
"s_mul_i32 s21, s21, 12288\n"
"s_add_u32 s72, s72, s21\n"
"buffer_store_dwordx2 v[100:101], v103, s[32:35], s72 offen sc1\n"
"Lr1b_%=:\n"
"s_waitcnt vmcnt(0) lgkmcnt(0)\n"
"s_barrier\n"
"s_cmp_lg_u32 s15, 0\n"
"s_cbranch_scc1 Lpub2_%=\n"
"s_lshl_b32 s21, s18, 3\n"
"s_add_u32 s21, s21, s19\n"
"s_lshl_b32 s21, s21, 4\n"
"s_add_u32 s21, s21, s20\n"
"s_lshl_b32 s21, s21, 3\n"
"s_add_u32 s21, s21, 2048\n"
"v_mov_b32 v218, s21\n"
"v_mov_b32 v220, s92\n"
"v_mov_b32 v221, s93\n"
"s_mov_b64 exec, 1\n"
"global_store_dwordx2 v218, v[220:221], s[94:95] sc1\n"
"s_mov_b64 exec, -1\n"
"Lpub2_%=:\n"
"s_cmp_lg_u32 s14, 255\n"
"s_cbranch_scc1 Lnoep_%=\n"
"s_cmp_lg_u32 s15, 3\n"
"s_cbranch_scc1 Lnoep_%=\n"
"v_lshlrev_b32 v200, 3, v247\n"
"v_lshlrev_b32 v201, 3, v247\n"
"v_add_u32 v201, 512, v201\n"
"v_lshlrev_b32 v202, 3, v247\n"
"v_add_u32 v202, 1024, v202\n"
"v_lshlrev_b32 v203, 3, v247\n"
"v_add_u32 v203, 1536, v203\n"
"s_mov_b32 s73, 131072\n"
"Lep_%=:\n"
"global_load_dwordx2 v[204:205], v200, s[94:95] sc1\n"
"global_load_dwordx2 v[206:207], v201, s[94:95] sc1\n"
"global_load_dwordx2 v[208:209], v202, s[94:95] sc1\n"
"global_load_dwordx2 v[210:211], v203, s[94:95] sc1\n"
"s_waitcnt vmcnt(0)\n"
"v_cmp_eq_u64 vcc, s[92:93], v[204:205]\n"
"s_nop 1\n"
"s_mov_b64 s[90:91], vcc\n"
"v_cmp_eq_u64 vcc, s[92:93], v[206:207]\n"
"s_nop 1\n"
"s_and_b64 s[90:91], s[90:91], vcc\n"
"v_cmp_eq_u64 vcc, s[92:93], v[208:209]\n"
"s_nop 1\n"
"s_and_b64 s[90:91], s[90:91], vcc\n"
"v_cmp_eq_u64 vcc, s[92:93], v[210:211]\n"
"s_nop 1\n"
"s_and_b64 s[90:91], s[90:91], vcc\n"
"s_cmp_eq_u64 s[90:91], exec\n"
"s_cbranch_scc1 Lepd_%=\n"
"s_sleep 2\n"
"s_sub_u32 s73, s73, 1\n"
"s_cmp_lg_u32 s73, 0\n"
"s_cbranch_scc1 Lep_%=\n"
"Lepd_%=:\n"
"v_mov_b32 v218, 0x1000\n"
"v_mov_b32 v220, s92\n"
"v_mov_b32 v221, s93\n"
"s_mov_b64 exec, 1\n"
"global_store_dwordx2 v218, v[220:221], s[94:95] sc1\n"
"s_mov_b64 exec, -1\n"
"Lnoep_%=:\n"
"s_lshl_b32 s21, s18, 3\n"
"s_add_u32 s21, s21, s19\n"
"s_lshl_b32 s21, s21, 4\n"
"s_add_u32 s21, s21, s80\n"
"s_lshl_b32 s21, s21, 3\n"
"s_add_u32 s21, s21, 2048\n"
"v_and_b32 v218, 3, v247\n"
"v_lshlrev_b32 v218, 5, v218\n"
"v_add_u32 v218, s21, v218\n"
"s_mov_b32 s73, 131072\n"
"Lp2_%=:\n"
"global_load_dwordx2 v[220:221], v218, s[94:95] sc1\n"
"s_waitcnt vmcnt(0)\n"
"v_cmp_eq_u64 vcc, s[92:93], v[220:221]\n"
"s_nop 1\n"
"s_mov_b64 s[90:91], vcc\n"
"s_cmp_eq_u64 s[90:91], exec\n"
"s_cbranch_scc1 Lp2d_%=\n"
"s_sleep 2\n"
"s_sub_u32 s73, s73, 1\n"
"s_cmp_lg_u32 s73, 0\n"
"s_cbranch_scc1 Lp2_%=\n"
"Lp2d_%=:\n"
"s_lshl_b32 s21, s81, 2\n"
"s_add_u32 s21, s21, s74\n"
"s_mul_i32 s77, s21, 12288\n"
"s_add_u32 s83, s15, 0\n"
"s_min_u32 s83, s83, 9\n"
"s_lshl_b32 s72, s83, 10\n"
"s_add_u32 s72, s72, s77\n"
"buffer_load_dwordx4 v[0:3], v102, s[32:35], s72 offen sc1\n"
"s_add_u32 s72, s72, 12288\n"
"buffer_load_dwordx4 v[4:7], v102, s[32:35], s72 offen sc1\n"
"s_add_u32 s72, s72, 12288\n"
"buffer_load_dwordx4 v[8:11], v102, s[32:35], s72 offen sc1\n"
"s_add_u32 s72, s72, 12288\n"
"buffer_load_dwordx4 v[12:15], v102, s[32:35], s72 offen sc1\n"
"s_add_u32 s84, s15, 4\n"
"s_min_u32 s84, s84, 9\n"
"s_lshl_b32 s72, s84, 10\n"
"s_add_u32 s72, s72, s77\n"
"buffer_load_dwordx4 v[16:19], v102, s[32:35], s72 offen sc1\n"
"s_add_u32 s72, s72, 12288\n"
"buffer_load_dwordx4 v[20:23], v102, s[32:35], s72 offen sc1\n"
"s_add_u32 s72, s72, 12288\n"
"buffer_load_dwordx4 v[24:27], v102, s[32:35], s72 offen sc1\n"
"s_add_u32 s72, s72, 12288\n"
"buffer_load_dwordx4 v[28:31], v102, s[32:35], s72 offen sc1\n"
"s_add_u32 s85, s15, 8\n"
"s_min_u32 s85, s85, 9\n"
"s_lshl_b32 s72, s85, 10\n"
"s_add_u32 s72, s72, s77\n"
"buffer_load_dwordx4 v[32:35], v102, s[32:35], s72 offen sc1\n"
"s_add_u32 s72, s72, 12288\n"
"buffer_load_dwordx4 v[36:39], v102, s[32:35], s72 offen sc1\n"
"s_add_u32 s72, s72, 12288\n"
"buffer_load_dwordx4 v[40:43], v102, s[32:35], s72 offen sc1\n"
"s_add_u32 s72, s72, 12288\n"
"buffer_load_dwordx4 v[44:47], v102, s[32:35], s72 offen sc1\n"
"s_cmp_lt_u32 s81, 2\n"
"s_cselect_b32 s21, 2, 1\n"
"s_min_u32 s21, s21, s15\n"
"s_mul_i32 s22, s81, 3\n"
"s_cmp_eq_u32 s81, 3\n"
"s_cselect_b32 s22, 8, s22\n"
"s_add_u32 s86, s22, s21\n"
"s_lshl_b32 s72, s21, 9\n"
"s_add_u32 s72, s72, s77\n"
"s_add_u32 s72, s72, 10240\n"
"buffer_load_dwordx2 v[48:49], v103, s[32:35], s72 offen sc1\n"
"s_add_u32 s72, s72, 12288\n"
"buffer_load_dwordx2 v[50:51], v103, s[32:35], s72 offen sc1\n"
"s_add_u32 s72, s72, 12288\n"
"buffer_load_dwordx2 v[52:53], v103, s[32:35], s72 offen sc1\n"
"s_add_u32 s72, s72, 12288\n"
"buffer_load_dwordx2 v[54:55], v103, s[32:35], s72 offen sc1\n"
"v_and_b32 v104, 0xf, v247\n"
"v_mul_u32_u24 v104, 0xa00, v104\n"
"v_lshrrev_b32 v253, 4, v247\n"
"v_lshlrev_b32 v253, 3, v253\n"
"v_add_u32 v104, v253, v104\n"
"s_mul_i32 s21, s19, 144\n"
"s_mul_i32 s21, s21, 2560\n"
"s_mul_i32 s22, s18, 1280\n"
"s_add_u32 s21, s21, s22\n"
"s_mul_i32 s22, s80, 320\n"
"s_add_u32 s21, s21, s22\n"
"s_mul_i32 s22, s81, 81920\n"
"s_add_u32 s22, s22, s21\n"
"s_waitcnt vmcnt(15)\n"
"v_lshlrev_b32 v64, 16, v0\n"
"v_and_b32 v65, 0xffff0000, v0\n"
"s_waitcnt vmcnt(14)\n"
"v_lshlrev_b32 v72, 16, v4\n"
"v_and_b32 v73, 0xffff0000, v4\n"
"v_add_f32 v64, v64, v72\n"
"v_add_f32 v65, v65, v73\n"
"s_waitcnt vmcnt(13)\n"
"v_lshlrev_b32 v72, 16, v8\n"
"v_and_b32 v73, 0xffff0000, v8\n"
"v_add_f32 v64, v64, v72\n"
"v_add_f32 v65, v65, v73\n"
"s_waitcnt vmcnt(12)\n"
"v_lshlrev_b32 v72, 16, v12\n"
"v_and_b32 v73, 0xffff0000, v12\n"
"v_add_f32 v64, v64, v72\n"
"v_add_f32 v65, v65, v73\n"
"v_lshlrev_b32 v66, 16, v1\n"
"v_and_b32 v67, 0xffff0000, v1\n"
"v_lshlrev_b32 v72, 16, v5\n"
"v_and_b32 v73, 0xffff0000, v5\n"
"v_add_f32 v66, v66, v72\n"
"v_add_f32 v67, v67, v73\n"
"v_lshlrev_b32 v72, 16, v9\n"
"v_and_b32 v73, 0xffff0000, v9\n"
"v_add_f32 v66, v66, v72\n"
"v_add_f32 v67, v67, v73\n"
"v_lshlrev_b32 v72, 16, v13\n"
"v_and_b32 v73, 0xffff0000, v13\n"
"v_add_f32 v66, v66, v72\n"
"v_add_f32 v67, v67, v73\n"
"v_lshlrev_b32 v68, 16, v2\n"
"v_and_b32 v69, 0xffff0000, v2\n"
"v_lshlrev_b32 v72, 16, v6\n"
"v_and_b32 v73, 0xffff0000, v6\n"
"v_add_f32 v68, v68, v72\n"
"v_add_f32 v69, v69, v73\n"
"v_lshlrev_b32 v72, 16, v10\n"
"v_and_b32 v73, 0xffff0000, v10\n"
"v_add_f32 v68, v68, v72\n"
"v_add_f32 v69, v69, v73\n"
"v_lshlrev_b32 v72, 16, v14\n"
"v_and_b32 v73, 0xffff0000, v14\n"
"v_add_f32 v68, v68, v72\n"
"v_add_f32 v69, v69, v73\n"
"v_lshlrev_b32 v70, 16, v3\n"
"v_and_b32 v71, 0xffff0000, v3\n"
"v_lshlrev_b32 v72, 16, v7\n"
"v_and_b32 v73, 0xffff0000, v7\n"
"v_add_f32 v70, v70, v72\n"
"v_add_f32 v71, v71, v73\n"
"v_lshlrev_b32 v72, 16, v11\n"
"v_and_b32 v73, 0xffff0000, v11\n"
"v_add_f32 v70, v70, v72\n"
"v_add_f32 v71, v71, v73\n"
"v_lshlrev_b32 v72, 16, v15\n"
"v_and_b32 v73, 0xffff0000, v15\n"
"v_add_f32 v70, v70, v72\n"
"v_add_f32 v71, v71, v73\n"
"v_cvt_pk_bf16_f32 v76, v64, v65\n"
"v_cvt_pk_bf16_f32 v77, v66, v67\n"
"v_cvt_pk_bf16_f32 v78, v68, v69\n"
"v_cvt_pk_bf16_f32 v79, v70, v71\n"
"s_lshl_b32 s23, s83, 5\n"
"s_add_u32 s23, s23, s22\n"
"buffer_store_dwordx2 v[76:77], v104, s[28:31], s23 offen\n"
"s_add_u32 s23, s23, 40960\n"
"buffer_store_dwordx2 v[78:79], v104, s[28:31], s23 offen\n"
"s_waitcnt vmcnt(13)\n"
"v_lshlrev_b32 v64, 16, v16\n"
"v_and_b32 v65, 0xffff0000, v16\n"
"s_waitcnt vmcnt(12)\n"
"v_lshlrev_b32 v72, 16, v20\n"
"v_and_b32 v73, 0xffff0000, v20\n"
"v_add_f32 v64, v64, v72\n"
"v_add_f32 v65, v65, v73\n"
"s_waitcnt vmcnt(11)\n"
"v_lshlrev_b32 v72, 16, v24\n"
"v_and_b32 v73, 0xffff0000, v24\n"
"v_add_f32 v64, v64, v72\n"
"v_add_f32 v65, v65, v73\n"
"s_waitcnt vmcnt(10)\n"
"v_lshlrev_b32 v72, 16, v28\n"
"v_and_b32 v73, 0xffff0000, v28\n"
"v_add_f32 v64, v64, v72\n"
"v_add_f32 v65, v65, v73\n"
"v_lshlrev_b32 v66, 16, v17\n"
"v_and_b32 v67, 0xffff0000, v17\n"
"v_lshlrev_b32 v72, 16, v21\n"
"v_and_b32 v73, 0xffff0000, v21\n"
"v_add_f32 v66, v66, v72\n"
"v_add_f32 v67, v67, v73\n"
"v_lshlrev_b32 v72, 16, v25\n"
"v_and_b32 v73, 0xffff0000, v25\n"
"v_add_f32 v66, v66, v72\n"
"v_add_f32 v67, v67, v73\n"
"v_lshlrev_b32 v72, 16, v29\n"
"v_and_b32 v73, 0xffff0000, v29\n"
"v_add_f32 v66, v66, v72\n"
"v_add_f32 v67, v67, v73\n"
"v_lshlrev_b32 v68, 16, v18\n"
"v_and_b32 v69, 0xffff0000, v18\n"
"v_lshlrev_b32 v72, 16, v22\n"
"v_and_b32 v73, 0xffff0000, v22\n"
"v_add_f32 v68, v68, v72\n"
"v_add_f32 v69, v69, v73\n"
"v_lshlrev_b32 v72, 16, v26\n"
"v_and_b32 v73, 0xffff0000, v26\n"
"v_add_f32 v68, v68, v72\n"
"v_add_f32 v69, v69, v73\n"
"v_lshlrev_b32 v72, 16, v30\n"
"v_and_b32 v73, 0xffff0000, v30\n"
"v_add_f32 v68, v68, v72\n"
"v_add_f32 v69, v69, v73\n"
"v_lshlrev_b32 v70, 16, v19\n"
"v_and_b32 v71, 0xffff0000, v19\n"
"v_lshlrev_b32 v72, 16, v23\n"
"v_and_b32 v73, 0xffff0000, v23\n"
"v_add_f32 v70, v70, v72\n"
"v_add_f32 v71, v71, v73\n"
"v_lshlrev_b32 v72, 16, v27\n"
"v_and_b32 v73, 0xffff0000, v27\n"
"v_add_f32 v70, v70, v72\n"
"v_add_f32 v71, v71, v73\n"
"v_lshlrev_b32 v72, 16, v31\n"
"v_and_b32 v73, 0xffff0000, v31\n"
"v_add_f32 v70, v70, v72\n"
"v_add_f32 v71, v71, v73\n"
"v_cvt_pk_bf16_f32 v76, v64, v65\n"
"v_cvt_pk_bf16_f32 v77, v66, v67\n"
"v_cvt_pk_bf16_f32 v78, v68, v69\n"
"v_cvt_pk_bf16_f32 v79, v70, v71\n"
"s_lshl_b32 s23, s84, 5\n"
"s_add_u32 s23, s23, s22\n"
"buffer_store_dwordx2 v[76:77], v104, s[28:31], s23 offen\n"
"s_add_u32 s23, s23, 40960\n"
"buffer_store_dwordx2 v[78:79], v104, s[28:31], s23 offen\n"
"s_waitcnt vmcnt(11)\n"
"v_lshlrev_b32 v64, 16, v32\n"
"v_and_b32 v65, 0xffff0000, v32\n"
"s_waitcnt vmcnt(10)\n"
"v_lshlrev_b32 v72, 16, v36\n"
"v_and_b32 v73, 0xffff0000, v36\n"
"v_add_f32 v64, v64, v72\n"
"v_add_f32 v65, v65, v73\n"
"s_waitcnt vmcnt(9)\n"
"v_lshlrev_b32 v72, 16, v40\n"
"v_and_b32 v73, 0xffff0000, v40\n"
"v_add_f32 v64, v64, v72\n"
"v_add_f32 v65, v65, v73\n"
"s_waitcnt vmcnt(8)\n"
"v_lshlrev_b32 v72, 16, v44\n"
"v_and_b32 v73, 0xffff0000, v44\n"
"v_add_f32 v64, v64, v72\n"
"v_add_f32 v65, v65, v73\n"
"v_lshlrev_b32 v66, 16, v33\n"
"v_and_b32 v67, 0xffff0000, v33\n"
"v_lshlrev_b32 v72, 16, v37\n"
"v_and_b32 v73, 0xffff0000, v37\n"
"v_add_f32 v66, v66, v72\n"
"v_add_f32 v67, v67, v73\n"
"v_lshlrev_b32 v72, 16, v41\n"
"v_and_b32 v73, 0xffff0000, v41\n"
"v_add_f32 v66, v66, v72\n"
"v_add_f32 v67, v67, v73\n"
"v_lshlrev_b32 v72, 16, v45\n"
"v_and_b32 v73, 0xffff0000, v45\n"
"v_add_f32 v66, v66, v72\n"
"v_add_f32 v67, v67, v73\n"
"v_lshlrev_b32 v68, 16, v34\n"
"v_and_b32 v69, 0xffff0000, v34\n"
"v_lshlrev_b32 v72, 16, v38\n"
"v_and_b32 v73, 0xffff0000, v38\n"
"v_add_f32 v68, v68, v72\n"
"v_add_f32 v69, v69, v73\n"
"v_lshlrev_b32 v72, 16, v42\n"
"v_and_b32 v73, 0xffff0000, v42\n"
"v_add_f32 v68, v68, v72\n"
"v_add_f32 v69, v69, v73\n"
"v_lshlrev_b32 v72, 16, v46\n"
"v_and_b32 v73, 0xffff0000, v46\n"
"v_add_f32 v68, v68, v72\n"
"v_add_f32 v69, v69, v73\n"
"v_lshlrev_b32 v70, 16, v35\n"
"v_and_b32 v71, 0xffff0000, v35\n"
"v_lshlrev_b32 v72, 16, v39\n"
"v_and_b32 v73, 0xffff0000, v39\n"
"v_add_f32 v70, v70, v72\n"
"v_add_f32 v71, v71, v73\n"
"v_lshlrev_b32 v72, 16, v43\n"
"v_and_b32 v73, 0xffff0000, v43\n"
"v_add_f32 v70, v70, v72\n"
"v_add_f32 v71, v71, v73\n"
"v_lshlrev_b32 v72, 16, v47\n"
"v_and_b32 v73, 0xffff0000, v47\n"
"v_add_f32 v70, v70, v72\n"
"v_add_f32 v71, v71, v73\n"
"v_cvt_pk_bf16_f32 v76, v64, v65\n"
"v_cvt_pk_bf16_f32 v77, v66, v67\n"
"v_cvt_pk_bf16_f32 v78, v68, v69\n"
"v_cvt_pk_bf16_f32 v79, v70, v71\n"
"s_lshl_b32 s23, s85, 5\n"
"s_add_u32 s23, s23, s22\n"
"buffer_store_dwordx2 v[76:77], v104, s[28:31], s23 offen\n"
"s_add_u32 s23, s23, 40960\n"
"buffer_store_dwordx2 v[78:79], v104, s[28:31], s23 offen\n"
"s_waitcnt vmcnt(9)\n"
"v_lshlrev_b32 v64, 16, v48\n"
"v_and_b32 v65, 0xffff0000, v48\n"
"s_waitcnt vmcnt(8)\n"
"v_lshlrev_b32 v72, 16, v50\n"
"v_and_b32 v73, 0xffff0000, v50\n"
"v_add_f32 v64, v64, v72\n"
"v_add_f32 v65, v65, v73\n"
"s_waitcnt vmcnt(7)\n"
"v_lshlrev_b32 v72, 16, v52\n"
"v_and_b32 v73, 0xffff0000, v52\n"
"v_add_f32 v64, v64, v72\n"
"v_add_f32 v65, v65, v73\n"
"s_waitcnt vmcnt(6)\n"
"v_lshlrev_b32 v72, 16, v54\n"
"v_and_b32 v73, 0xffff0000, v54\n"
"v_add_f32 v64, v64, v72\n"
"v_add_f32 v65, v65, v73\n"
"v_lshlrev_b32 v66, 16, v49\n"
"v_and_b32 v67, 0xffff0000, v49\n"
"v_lshlrev_b32 v72, 16, v51\n"
"v_and_b32 v73, 0xffff0000, v51\n"
"v_add_f32 v66, v66, v72\n"
"v_add_f32 v67, v67, v73\n"
"v_lshlrev_b32 v72, 16, v53\n"
"v_and_b32 v73, 0xffff0000, v53\n"
"v_add_f32 v66, v66, v72\n"
"v_add_f32 v67, v67, v73\n"
"v_lshlrev_b32 v72, 16, v55\n"
"v_and_b32 v73, 0xffff0000, v55\n"
"v_add_f32 v66, v66, v72\n"
"v_add_f32 v67, v67, v73\n"
"v_cvt_pk_bf16_f32 v80, v64, v65\n"
"v_cvt_pk_bf16_f32 v81, v66, v67\n"
"s_lshl_b32 s23, s86, 5\n"
"s_add_u32 s23, s23, s21\n"
"s_add_u32 s23, s23, 327680\n"
"buffer_store_dwordx2 v[80:81], v104, s[28:31], s23 offen\n"
"s_waitcnt vmcnt(0)\n"
"s_endpgm\n"
"Lcode1_%=:\n"
"Lslow0_%=:\n"
"s_nop 15\n"
"s_nop 7\n"
"v_max3_f32 v248, v80, v81, v82\n"
"v_max3_f32 v248, v248, v83, v84\n"
"v_max3_f32 v248, v248, v85, v86\n"
"v_max3_f32 v248, v248, v87, v88\n"
"v_max3_f32 v248, v248, v89, v90\n"
"v_max3_f32 v248, v248, v91, v92\n"
"v_max3_f32 v248, v248, v93, v94\n"
"v_max_f32 v248, v248, v95\n"
"v_max3_f32 v249, v96, v97, v98\n"
"v_max3_f32 v249, v249, v99, v100\n"
"v_max3_f32 v249, v249, v101, v102\n"
"v_max3_f32 v249, v249, v103, v104\n"
"v_max3_f32 v249, v249, v105, v106\n"
"v_max3_f32 v249, v249, v107, v108\n"
"v_max3_f32 v249, v249, v109, v110\n"
"v_max_f32 v249, v249, v111\n"
"v_max3_f32 v250, v112, v113, v114\n"
"v_max_f32 v250, v250, v115\n"
"v_mov_b32 v251, v248\n"
"s_nop 1\n"
"v_permlane32_swap_b32 v248, v251\n"
"v_max_f32 v248, v248, v251\n"
"v_mov_b32 v251, v249\n"
"s_nop 1\n"
"v_permlane32_swap_b32 v249, v251\n"
"v_max_f32 v249, v249, v251\n"
"v_mov_b32 v251, v250\n"
"s_nop 1\n"
"v_permlane32_swap_b32 v250, v251\n"
"v_max_f32 v250, v250, v251\n"
"v_mov_b32 v251, v250\n"
"s_nop 1\n"
"v_permlane16_swap_b32 v250, v251\n"
"v_max_f32 v250, v250, v251\n"
"v_max_f32 v248, 0, v248\n"
"v_max_f32 v249, 0, v249\n"
"v_max_f32 v250, 0, v250\n"
"v_exp_f32 v252, -v248\n"
"v_exp_f32 v253, -v249\n"
"v_exp_f32 v254, -v250\n"
"v_mul_f32 v235, v235, v252\n"
"v_mul_f32 v236, v236, v253\n"
"v_mul_f32 v237, v237, v254\n"
"v_sub_f32 v80, v80, v248\n"
"v_sub_f32 v81, v81, v248\n"
"v_sub_f32 v82, v82, v248\n"
"v_sub_f32 v83, v83, v248\n"
"v_sub_f32 v84, v84, v248\n"
"v_sub_f32 v85, v85, v248\n"
"v_sub_f32 v86, v86, v248\n"
"v_sub_f32 v87, v87, v248\n"
"v_sub_f32 v88, v88, v248\n"
"v_sub_f32 v89, v89, v248\n"
"v_sub_f32 v90, v90, v248\n"
"v_sub_f32 v91, v91, v248\n"
"v_sub_f32 v92, v92, v248\n"
"v_sub_f32 v93, v93, v248\n"
"v_sub_f32 v94, v94, v248\n"
"v_sub_f32 v95, v95, v248\n"
"v_sub_f32 v152, v152, v248\n"
"v_sub_f32 v153, v153, v248\n"
"v_sub_f32 v154, v154, v248\n"
"v_sub_f32 v155, v155, v248\n"
"v_sub_f32 v156, v156, v248\n"
"v_sub_f32 v157, v157, v248\n"
"v_sub_f32 v158, v158, v248\n"
"v_sub_f32 v159, v159, v248\n"
"v_sub_f32 v160, v160, v248\n"
"v_sub_f32 v161, v161, v248\n"
"v_sub_f32 v162, v162, v248\n"
"v_sub_f32 v163, v163, v248\n"
"v_sub_f32 v164, v164, v248\n"
"v_sub_f32 v165, v165, v248\n"
"v_sub_f32 v166, v166, v248\n"
"v_sub_f32 v167, v167, v248\n"
"v_sub_f32 v96, v96, v249\n"
"v_sub_f32 v97, v97, v249\n"
"v_sub_f32 v98, v98, v249\n"
"v_sub_f32 v99, v99, v249\n"
"v_sub_f32 v100, v100, v249\n"
"v_sub_f32 v101, v101, v249\n"
"v_sub_f32 v102, v102, v249\n"
"v_sub_f32 v103, v103, v249\n"
"v_sub_f32 v104, v104, v249\n"
"v_sub_f32 v105, v105, v249\n"
"v_sub_f32 v106, v106, v249\n"
"v_sub_f32 v107, v107, v249\n"
"v_sub_f32 v108, v108, v249\n"
"v_sub_f32 v109, v109, v249\n"
"v_sub_f32 v110, v110, v249\n"
"v_sub_f32 v111, v111, v249\n"
"v_sub_f32 v168, v168, v249\n"
"v_sub_f32 v169, v169, v249\n"
"v_sub_f32 v170, v170, v249\n"
"v_sub_f32 v171, v171, v249\n"
"v_sub_f32 v172, v172, v249\n"
"v_sub_f32 v173, v173, v249\n"
"v_sub_f32 v174, v174, v249\n"
"v_sub_f32 v175, v175, v249\n"
"v_sub_f32 v176, v176, v249\n"
"v_sub_f32 v177, v177, v249\n"
"v_sub_f32 v178, v178, v249\n"
"v_sub_f32 v179, v179, v249\n"
"v_sub_f32 v180, v180, v249\n"
"v_sub_f32 v181, v181, v249\n"
"v_sub_f32 v182, v182, v249\n"
"v_sub_f32 v183, v183, v249\n"
"v_sub_f32 v112, v112, v250\n"
"v_sub_f32 v113, v113, v250\n"
"v_sub_f32 v114, v114, v250\n"
"v_sub_f32 v115, v115, v250\n"
"v_sub_f32 v184, v184, v250\n"
"v_sub_f32 v185, v185, v250\n"
"v_sub_f32 v186, v186, v250\n"
"v_sub_f32 v187, v187, v250\n"
"v_sub_f32 v116, v116, v248\n"
"v_sub_f32 v117, v117, v248\n"
"v_sub_f32 v118, v118, v248\n"
"v_sub_f32 v119, v119, v248\n"
"v_sub_f32 v120, v120, v248\n"
"v_sub_f32 v121, v121, v248\n"
"v_sub_f32 v122, v122, v248\n"
"v_sub_f32 v123, v123, v248\n"
"v_sub_f32 v124, v124, v248\n"
"v_sub_f32 v125, v125, v248\n"
"v_sub_f32 v126, v126, v248\n"
"v_sub_f32 v127, v127, v248\n"
"v_sub_f32 v128, v128, v248\n"
"v_sub_f32 v129, v129, v248\n"
"v_sub_f32 v130, v130, v248\n"
"v_sub_f32 v131, v131, v248\n"
"v_sub_f32 v132, v132, v249\n"
"v_sub_f32 v133, v133, v249\n"
"v_sub_f32 v134, v134, v249\n"
"v_sub_f32 v135, v135, v249\n"
"v_sub_f32 v136, v136, v249\n"
"v_sub_f32 v137, v137, v249\n"
"v_sub_f32 v138, v138, v249\n"
"v_sub_f32 v139, v139, v249\n"
"v_sub_f32 v140, v140, v249\n"
"v_sub_f32 v141, v141, v249\n"
"v_sub_f32 v142, v142, v249\n"
"v_sub_f32 v143, v143, v249\n"
"v_sub_f32 v144, v144, v249\n"
"v_sub_f32 v145, v145, v249\n"
"v_sub_f32 v146, v146, v249\n"
"v_sub_f32 v147, v147, v249\n"
"v_sub_f32 v148, v148, v250\n"
"v_sub_f32 v149, v149, v250\n"
"v_sub_f32 v150, v150, v250\n"
"v_sub_f32 v151, v151, v250\n"
"Lslown0_%=:\n"
"v_accvgpr_read_b32 v224, a0\n"
"v_mul_f32 v224, v224, v252\n"
"v_accvgpr_write_b32 a0, v224\n"
"v_accvgpr_read_b32 v225, a1\n"
"v_mul_f32 v225, v225, v252\n"
"v_accvgpr_write_b32 a1, v225\n"
"v_accvgpr_read_b32 v226, a2\n"
"v_mul_f32 v226, v226, v252\n"
"v_accvgpr_write_b32 a2, v226\n"
"v_accvgpr_read_b32 v227, a3\n"
"v_mul_f32 v227, v227, v252\n"
"v_accvgpr_write_b32 a3, v227\n"
"v_accvgpr_read_b32 v228, a4\n"
"v_mul_f32 v228, v228, v252\n"
"v_accvgpr_write_b32 a4, v228\n"
"v_accvgpr_read_b32 v229, a5\n"
"v_mul_f32 v229, v229, v252\n"
"v_accvgpr_write_b32 a5, v229\n"
"v_accvgpr_read_b32 v230, a6\n"
"v_mul_f32 v230, v230, v252\n"
"v_accvgpr_write_b32 a6, v230\n"
"v_accvgpr_read_b32 v231, a7\n"
"v_mul_f32 v231, v231, v252\n"
"v_accvgpr_write_b32 a7, v231\n"
"v_accvgpr_read_b32 v224, a8\n"
"v_mul_f32 v224, v224, v252\n"
"v_accvgpr_write_b32 a8, v224\n"
"v_accvgpr_read_b32 v225, a9\n"
"v_mul_f32 v225, v225, v252\n"
"v_accvgpr_write_b32 a9, v225\n"
"v_accvgpr_read_b32 v226, a10\n"
"v_mul_f32 v226, v226, v252\n"
"v_accvgpr_write_b32 a10, v226\n"
"v_accvgpr_read_b32 v227, a11\n"
"v_mul_f32 v227, v227, v252\n"
"v_accvgpr_write_b32 a11, v227\n"
"v_accvgpr_read_b32 v228, a12\n"
"v_mul_f32 v228, v228, v252\n"
"v_accvgpr_write_b32 a12, v228\n"
"v_accvgpr_read_b32 v229, a13\n"
"v_mul_f32 v229, v229, v252\n"
"v_accvgpr_write_b32 a13, v229\n"
"v_accvgpr_read_b32 v230, a14\n"
"v_mul_f32 v230, v230, v252\n"
"v_accvgpr_write_b32 a14, v230\n"
"v_accvgpr_read_b32 v231, a15\n"
"v_mul_f32 v231, v231, v252\n"
"v_accvgpr_write_b32 a15, v231\n"
"v_accvgpr_read_b32 v224, a16\n"
"v_mul_f32 v224, v224, v253\n"
"v_accvgpr_write_b32 a16, v224\n"
"v_accvgpr_read_b32 v225, a17\n"
"v_mul_f32 v225, v225, v253\n"
"v_accvgpr_write_b32 a17, v225\n"
"v_accvgpr_read_b32 v226, a18\n"
"v_mul_f32 v226, v226, v253\n"
"v_accvgpr_write_b32 a18, v226\n"
"v_accvgpr_read_b32 v227, a19\n"
"v_mul_f32 v227, v227, v253\n"
"v_accvgpr_write_b32 a19, v227\n"
"v_accvgpr_read_b32 v228, a20\n"
"v_mul_f32 v228, v228, v253\n"
"v_accvgpr_write_b32 a20, v228\n"
"v_accvgpr_read_b32 v229, a21\n"
"v_mul_f32 v229, v229, v253\n"
"v_accvgpr_write_b32 a21, v229\n"
"v_accvgpr_read_b32 v230, a22\n"
"v_mul_f32 v230, v230, v253\n"
"v_accvgpr_write_b32 a22, v230\n"
"v_accvgpr_read_b32 v231, a23\n"
"v_mul_f32 v231, v231, v253\n"
"v_accvgpr_write_b32 a23, v231\n"
"v_accvgpr_read_b32 v224, a24\n"
"v_mul_f32 v224, v224, v253\n"
"v_accvgpr_write_b32 a24, v224\n"
"v_accvgpr_read_b32 v225, a25\n"
"v_mul_f32 v225, v225, v253\n"
"v_accvgpr_write_b32 a25, v225\n"
"v_accvgpr_read_b32 v226, a26\n"
"v_mul_f32 v226, v226, v253\n"
"v_accvgpr_write_b32 a26, v226\n"
"v_accvgpr_read_b32 v227, a27\n"
"v_mul_f32 v227, v227, v253\n"
"v_accvgpr_write_b32 a27, v227\n"
"v_accvgpr_read_b32 v228, a28\n"
"v_mul_f32 v228, v228, v253\n"
"v_accvgpr_write_b32 a28, v228\n"
"v_accvgpr_read_b32 v229, a29\n"
"v_mul_f32 v229, v229, v253\n"
"v_accvgpr_write_b32 a29, v229\n"
"v_accvgpr_read_b32 v230, a30\n"
"v_mul_f32 v230, v230, v253\n"
"v_accvgpr_write_b32 a30, v230\n"
"v_accvgpr_read_b32 v231, a31\n"
"v_mul_f32 v231, v231, v253\n"
"v_accvgpr_write_b32 a31, v231\n"
"v_accvgpr_read_b32 v224, a32\n"
"v_mul_f32 v224, v224, v252\n"
"v_accvgpr_write_b32 a32, v224\n"
"v_accvgpr_read_b32 v225, a33\n"
"v_mul_f32 v225, v225, v252\n"
"v_accvgpr_write_b32 a33, v225\n"
"v_accvgpr_read_b32 v226, a34\n"
"v_mul_f32 v226, v226, v252\n"
"v_accvgpr_write_b32 a34, v226\n"
"v_accvgpr_read_b32 v227, a35\n"
"v_mul_f32 v227, v227, v252\n"
"v_accvgpr_write_b32 a35, v227\n"
"v_accvgpr_read_b32 v228, a36\n"
"v_mul_f32 v228, v228, v252\n"
"v_accvgpr_write_b32 a36, v228\n"
"v_accvgpr_read_b32 v229, a37\n"
"v_mul_f32 v229, v229, v252\n"
"v_accvgpr_write_b32 a37, v229\n"
"v_accvgpr_read_b32 v230, a38\n"
"v_mul_f32 v230, v230, v252\n"
"v_accvgpr_write_b32 a38, v230\n"
"v_accvgpr_read_b32 v231, a39\n"
"v_mul_f32 v231, v231, v252\n"
"v_accvgpr_write_b32 a39, v231\n"
"v_accvgpr_read_b32 v224, a40\n"
"v_mul_f32 v224, v224, v252\n"
"v_accvgpr_write_b32 a40, v224\n"
"v_accvgpr_read_b32 v225, a41\n"
"v_mul_f32 v225, v225, v252\n"
"v_accvgpr_write_b32 a41, v225\n"
"v_accvgpr_read_b32 v226, a42\n"
"v_mul_f32 v226, v226, v252\n"
"v_accvgpr_write_b32 a42, v226\n"
"v_accvgpr_read_b32 v227, a43\n"
"v_mul_f32 v227, v227, v252\n"
"v_accvgpr_write_b32 a43, v227\n"
"v_accvgpr_read_b32 v228, a44\n"
"v_mul_f32 v228, v228, v252\n"
"v_accvgpr_write_b32 a44, v228\n"
"v_accvgpr_read_b32 v229, a45\n"
"v_mul_f32 v229, v229, v252\n"
"v_accvgpr_write_b32 a45, v229\n"
"v_accvgpr_read_b32 v230, a46\n"
"v_mul_f32 v230, v230, v252\n"
"v_accvgpr_write_b32 a46, v230\n"
"v_accvgpr_read_b32 v231, a47\n"
"v_mul_f32 v231, v231, v252\n"
"v_accvgpr_write_b32 a47, v231\n"
"v_accvgpr_read_b32 v224, a48\n"
"v_mul_f32 v224, v224, v253\n"
"v_accvgpr_write_b32 a48, v224\n"
"v_accvgpr_read_b32 v225, a49\n"
"v_mul_f32 v225, v225, v253\n"
"v_accvgpr_write_b32 a49, v225\n"
"v_accvgpr_read_b32 v226, a50\n"
"v_mul_f32 v226, v226, v253\n"
"v_accvgpr_write_b32 a50, v226\n"
"v_accvgpr_read_b32 v227, a51\n"
"v_mul_f32 v227, v227, v253\n"
"v_accvgpr_write_b32 a51, v227\n"
"v_accvgpr_read_b32 v228, a52\n"
"v_mul_f32 v228, v228, v253\n"
"v_accvgpr_write_b32 a52, v228\n"
"v_accvgpr_read_b32 v229, a53\n"
"v_mul_f32 v229, v229, v253\n"
"v_accvgpr_write_b32 a53, v229\n"
"v_accvgpr_read_b32 v230, a54\n"
"v_mul_f32 v230, v230, v253\n"
"v_accvgpr_write_b32 a54, v230\n"
"v_accvgpr_read_b32 v231, a55\n"
"v_mul_f32 v231, v231, v253\n"
"v_accvgpr_write_b32 a55, v231\n"
"v_accvgpr_read_b32 v224, a56\n"
"v_mul_f32 v224, v224, v253\n"
"v_accvgpr_write_b32 a56, v224\n"
"v_accvgpr_read_b32 v225, a57\n"
"v_mul_f32 v225, v225, v253\n"
"v_accvgpr_write_b32 a57, v225\n"
"v_accvgpr_read_b32 v226, a58\n"
"v_mul_f32 v226, v226, v253\n"
"v_accvgpr_write_b32 a58, v226\n"
"v_accvgpr_read_b32 v227, a59\n"
"v_mul_f32 v227, v227, v253\n"
"v_accvgpr_write_b32 a59, v227\n"
"v_accvgpr_read_b32 v228, a60\n"
"v_mul_f32 v228, v228, v253\n"
"v_accvgpr_write_b32 a60, v228\n"
"v_accvgpr_read_b32 v229, a61\n"
"v_mul_f32 v229, v229, v253\n"
"v_accvgpr_write_b32 a61, v229\n"
"v_accvgpr_read_b32 v230, a62\n"
"v_mul_f32 v230, v230, v253\n"
"v_accvgpr_write_b32 a62, v230\n"
"v_accvgpr_read_b32 v231, a63\n"
"v_mul_f32 v231, v231, v253\n"
"v_accvgpr_write_b32 a63, v231\n"
"v_accvgpr_read_b32 v224, a64\n"
"v_mul_f32 v224, v224, v252\n"
"v_accvgpr_write_b32 a64, v224\n"
"v_accvgpr_read_b32 v225, a65\n"
"v_mul_f32 v225, v225, v252\n"
"v_accvgpr_write_b32 a65, v225\n"
"v_accvgpr_read_b32 v226, a66\n"
"v_mul_f32 v226, v226, v252\n"
"v_accvgpr_write_b32 a66, v226\n"
"v_accvgpr_read_b32 v227, a67\n"
"v_mul_f32 v227, v227, v252\n"
"v_accvgpr_write_b32 a67, v227\n"
"v_accvgpr_read_b32 v228, a68\n"
"v_mul_f32 v228, v228, v252\n"
"v_accvgpr_write_b32 a68, v228\n"
"v_accvgpr_read_b32 v229, a69\n"
"v_mul_f32 v229, v229, v252\n"
"v_accvgpr_write_b32 a69, v229\n"
"v_accvgpr_read_b32 v230, a70\n"
"v_mul_f32 v230, v230, v252\n"
"v_accvgpr_write_b32 a70, v230\n"
"v_accvgpr_read_b32 v231, a71\n"
"v_mul_f32 v231, v231, v252\n"
"v_accvgpr_write_b32 a71, v231\n"
"v_accvgpr_read_b32 v224, a72\n"
"v_mul_f32 v224, v224, v252\n"
"v_accvgpr_write_b32 a72, v224\n"
"v_accvgpr_read_b32 v225, a73\n"
"v_mul_f32 v225, v225, v252\n"
"v_accvgpr_write_b32 a73, v225\n"
"v_accvgpr_read_b32 v226, a74\n"
"v_mul_f32 v226, v226, v252\n"
"v_accvgpr_write_b32 a74, v226\n"
"v_accvgpr_read_b32 v227, a75\n"
"v_mul_f32 v227, v227, v252\n"
"v_accvgpr_write_b32 a75, v227\n"
"v_accvgpr_read_b32 v228, a76\n"
"v_mul_f32 v228, v228, v252\n"
"v_accvgpr_write_b32 a76, v228\n"
"v_accvgpr_read_b32 v229, a77\n"
"v_mul_f32 v229, v229, v252\n"
"v_accvgpr_write_b32 a77, v229\n"
"v_accvgpr_read_b32 v230, a78\n"
"v_mul_f32 v230, v230, v252\n"
"v_accvgpr_write_b32 a78, v230\n"
"v_accvgpr_read_b32 v231, a79\n"
"v_mul_f32 v231, v231, v252\n"
"v_accvgpr_write_b32 a79, v231\n"
"v_accvgpr_read_b32 v224, a80\n"
"v_mul_f32 v224, v224, v253\n"
"v_accvgpr_write_b32 a80, v224\n"
"v_accvgpr_read_b32 v225, a81\n"
"v_mul_f32 v225, v225, v253\n"
"v_accvgpr_write_b32 a81, v225\n"
"v_accvgpr_read_b32 v226, a82\n"
"v_mul_f32 v226, v226, v253\n"
"v_accvgpr_write_b32 a82, v226\n"
"v_accvgpr_read_b32 v227, a83\n"
"v_mul_f32 v227, v227, v253\n"
"v_accvgpr_write_b32 a83, v227\n"
"v_accvgpr_read_b32 v228, a84\n"
"v_mul_f32 v228, v228, v253\n"
"v_accvgpr_write_b32 a84, v228\n"
"v_accvgpr_read_b32 v229, a85\n"
"v_mul_f32 v229, v229, v253\n"
"v_accvgpr_write_b32 a85, v229\n"
"v_accvgpr_read_b32 v230, a86\n"
"v_mul_f32 v230, v230, v253\n"
"v_accvgpr_write_b32 a86, v230\n"
"v_accvgpr_read_b32 v231, a87\n"
"v_mul_f32 v231, v231, v253\n"
"v_accvgpr_write_b32 a87, v231\n"
"v_accvgpr_read_b32 v224, a88\n"
"v_mul_f32 v224, v224, v253\n"
"v_accvgpr_write_b32 a88, v224\n"
"v_accvgpr_read_b32 v225, a89\n"
"v_mul_f32 v225, v225, v253\n"
"v_accvgpr_write_b32 a89, v225\n"
"v_accvgpr_read_b32 v226, a90\n"
"v_mul_f32 v226, v226, v253\n"
"v_accvgpr_write_b32 a90, v226\n"
"v_accvgpr_read_b32 v227, a91\n"
"v_mul_f32 v227, v227, v253\n"
"v_accvgpr_write_b32 a91, v227\n"
"v_accvgpr_read_b32 v228, a92\n"
"v_mul_f32 v228, v228, v253\n"
"v_accvgpr_write_b32 a92, v228\n"
"v_accvgpr_read_b32 v229, a93\n"
"v_mul_f32 v229, v229, v253\n"
"v_accvgpr_write_b32 a93, v229\n"
"v_accvgpr_read_b32 v230, a94\n"
"v_mul_f32 v230, v230, v253\n"
"v_accvgpr_write_b32 a94, v230\n"
"v_accvgpr_read_b32 v231, a95\n"
"v_mul_f32 v231, v231, v253\n"
"v_accvgpr_write_b32 a95, v231\n"
"v_accvgpr_read_b32 v224, a96\n"
"v_mul_f32 v224, v224, v252\n"
"v_accvgpr_write_b32 a96, v224\n"
"v_accvgpr_read_b32 v225, a97\n"
"v_mul_f32 v225, v225, v252\n"
"v_accvgpr_write_b32 a97, v225\n"
"v_accvgpr_read_b32 v226, a98\n"
"v_mul_f32 v226, v226, v252\n"
"v_accvgpr_write_b32 a98, v226\n"
"v_accvgpr_read_b32 v227, a99\n"
"v_mul_f32 v227, v227, v252\n"
"v_accvgpr_write_b32 a99, v227\n"
"v_accvgpr_read_b32 v228, a100\n"
"v_mul_f32 v228, v228, v252\n"
"v_accvgpr_write_b32 a100, v228\n"
"v_accvgpr_read_b32 v229, a101\n"
"v_mul_f32 v229, v229, v252\n"
"v_accvgpr_write_b32 a101, v229\n"
"v_accvgpr_read_b32 v230, a102\n"
"v_mul_f32 v230, v230, v252\n"
"v_accvgpr_write_b32 a102, v230\n"
"v_accvgpr_read_b32 v231, a103\n"
"v_mul_f32 v231, v231, v252\n"
"v_accvgpr_write_b32 a103, v231\n"
"v_accvgpr_read_b32 v224, a104\n"
"v_mul_f32 v224, v224, v252\n"
"v_accvgpr_write_b32 a104, v224\n"
"v_accvgpr_read_b32 v225, a105\n"
"v_mul_f32 v225, v225, v252\n"
"v_accvgpr_write_b32 a105, v225\n"
"v_accvgpr_read_b32 v226, a106\n"
"v_mul_f32 v226, v226, v252\n"
"v_accvgpr_write_b32 a106, v226\n"
"v_accvgpr_read_b32 v227, a107\n"
"v_mul_f32 v227, v227, v252\n"
"v_accvgpr_write_b32 a107, v227\n"
"v_accvgpr_read_b32 v228, a108\n"
"v_mul_f32 v228, v228, v252\n"
"v_accvgpr_write_b32 a108, v228\n"
"v_accvgpr_read_b32 v229, a109\n"
"v_mul_f32 v229, v229, v252\n"
"v_accvgpr_write_b32 a109, v229\n"
"v_accvgpr_read_b32 v230, a110\n"
"v_mul_f32 v230, v230, v252\n"
"v_accvgpr_write_b32 a110, v230\n"
"v_accvgpr_read_b32 v231, a111\n"
"v_mul_f32 v231, v231, v252\n"
"v_accvgpr_write_b32 a111, v231\n"
"v_accvgpr_read_b32 v224, a112\n"
"v_mul_f32 v224, v224, v253\n"
"v_accvgpr_write_b32 a112, v224\n"
"v_accvgpr_read_b32 v225, a113\n"
"v_mul_f32 v225, v225, v253\n"
"v_accvgpr_write_b32 a113, v225\n"
"v_accvgpr_read_b32 v226, a114\n"
"v_mul_f32 v226, v226, v253\n"
"v_accvgpr_write_b32 a114, v226\n"
"v_accvgpr_read_b32 v227, a115\n"
"v_mul_f32 v227, v227, v253\n"
"v_accvgpr_write_b32 a115, v227\n"
"v_accvgpr_read_b32 v228, a116\n"
"v_mul_f32 v228, v228, v253\n"
"v_accvgpr_write_b32 a116, v228\n"
"v_accvgpr_read_b32 v229, a117\n"
"v_mul_f32 v229, v229, v253\n"
"v_accvgpr_write_b32 a117, v229\n"
"v_accvgpr_read_b32 v230, a118\n"
"v_mul_f32 v230, v230, v253\n"
"v_accvgpr_write_b32 a118, v230\n"
"v_accvgpr_read_b32 v231, a119\n"
"v_mul_f32 v231, v231, v253\n"
"v_accvgpr_write_b32 a119, v231\n"
"v_accvgpr_read_b32 v224, a120\n"
"v_mul_f32 v224, v224, v253\n"
"v_accvgpr_write_b32 a120, v224\n"
"v_accvgpr_read_b32 v225, a121\n"
"v_mul_f32 v225, v225, v253\n"
"v_accvgpr_write_b32 a121, v225\n"
"v_accvgpr_read_b32 v226, a122\n"
"v_mul_f32 v226, v226, v253\n"
"v_accvgpr_write_b32 a122, v226\n"
"v_accvgpr_read_b32 v227, a123\n"
"v_mul_f32 v227, v227, v253\n"
"v_accvgpr_write_b32 a123, v227\n"
"v_accvgpr_read_b32 v228, a124\n"
"v_mul_f32 v228, v228, v253\n"
"v_accvgpr_write_b32 a124, v228\n"
"v_accvgpr_read_b32 v229, a125\n"
"v_mul_f32 v229, v229, v253\n"
"v_accvgpr_write_b32 a125, v229\n"
"v_accvgpr_read_b32 v230, a126\n"
"v_mul_f32 v230, v230, v253\n"
"v_accvgpr_write_b32 a126, v230\n"
"v_accvgpr_read_b32 v231, a127\n"
"v_mul_f32 v231, v231, v253\n"
"v_accvgpr_write_b32 a127, v231\n"
"v_accvgpr_read_b32 v224, a128\n"
"v_mul_f32 v224, v224, v254\n"
"v_accvgpr_write_b32 a128, v224\n"
"v_accvgpr_read_b32 v225, a129\n"
"v_mul_f32 v225, v225, v254\n"
"v_accvgpr_write_b32 a129, v225\n"
"v_accvgpr_read_b32 v226, a130\n"
"v_mul_f32 v226, v226, v254\n"
"v_accvgpr_write_b32 a130, v226\n"
"v_accvgpr_read_b32 v227, a131\n"
"v_mul_f32 v227, v227, v254\n"
"v_accvgpr_write_b32 a131, v227\n"
"v_accvgpr_read_b32 v228, a132\n"
"v_mul_f32 v228, v228, v254\n"
"v_accvgpr_write_b32 a132, v228\n"
"v_accvgpr_read_b32 v229, a133\n"
"v_mul_f32 v229, v229, v254\n"
"v_accvgpr_write_b32 a133, v229\n"
"v_accvgpr_read_b32 v230, a134\n"
"v_mul_f32 v230, v230, v254\n"
"v_accvgpr_write_b32 a134, v230\n"
"v_accvgpr_read_b32 v231, a135\n"
"v_mul_f32 v231, v231, v254\n"
"v_accvgpr_write_b32 a135, v231\n"
"v_accvgpr_read_b32 v224, a136\n"
"v_mul_f32 v224, v224, v254\n"
"v_accvgpr_write_b32 a136, v224\n"
"v_accvgpr_read_b32 v225, a137\n"
"v_mul_f32 v225, v225, v254\n"
"v_accvgpr_write_b32 a137, v225\n"
"v_accvgpr_read_b32 v226, a138\n"
"v_mul_f32 v226, v226, v254\n"
"v_accvgpr_write_b32 a138, v226\n"
"v_accvgpr_read_b32 v227, a139\n"
"v_mul_f32 v227, v227, v254\n"
"v_accvgpr_write_b32 a139, v227\n"
"v_accvgpr_read_b32 v228, a140\n"
"v_mul_f32 v228, v228, v254\n"
"v_accvgpr_write_b32 a140, v228\n"
"v_accvgpr_read_b32 v229, a141\n"
"v_mul_f32 v229, v229, v254\n"
"v_accvgpr_write_b32 a141, v229\n"
"v_accvgpr_read_b32 v230, a142\n"
"v_mul_f32 v230, v230, v254\n"
"v_accvgpr_write_b32 a142, v230\n"
"v_accvgpr_read_b32 v231, a143\n"
"v_mul_f32 v231, v231, v254\n"
"v_accvgpr_write_b32 a143, v231\n"
"v_accvgpr_read_b32 v224, a144\n"
"v_mul_f32 v224, v224, v254\n"
"v_accvgpr_write_b32 a144, v224\n"
"v_accvgpr_read_b32 v225, a145\n"
"v_mul_f32 v225, v225, v254\n"
"v_accvgpr_write_b32 a145, v225\n"
"v_accvgpr_read_b32 v226, a146\n"
"v_mul_f32 v226, v226, v254\n"
"v_accvgpr_write_b32 a146, v226\n"
"v_accvgpr_read_b32 v227, a147\n"
"v_mul_f32 v227, v227, v254\n"
"v_accvgpr_write_b32 a147, v227\n"
"v_accvgpr_read_b32 v228, a148\n"
"v_mul_f32 v228, v228, v254\n"
"v_accvgpr_write_b32 a148, v228\n"
"v_accvgpr_read_b32 v229, a149\n"
"v_mul_f32 v229, v229, v254\n"
"v_accvgpr_write_b32 a149, v229\n"
"v_accvgpr_read_b32 v230, a150\n"
"v_mul_f32 v230, v230, v254\n"
"v_accvgpr_write_b32 a150, v230\n"
"v_accvgpr_read_b32 v231, a151\n"
"v_mul_f32 v231, v231, v254\n"
"v_accvgpr_write_b32 a151, v231\n"
"v_accvgpr_read_b32 v224, a152\n"
"v_mul_f32 v224, v224, v254\n"
"v_accvgpr_write_b32 a152, v224\n"
"v_accvgpr_read_b32 v225, a153\n"
"v_mul_f32 v225, v225, v254\n"
"v_accvgpr_write_b32 a153, v225\n"
"v_accvgpr_read_b32 v226, a154\n"
"v_mul_f32 v226, v226, v254\n"
"v_accvgpr_write_b32 a154, v226\n"
"v_accvgpr_read_b32 v227, a155\n"
"v_mul_f32 v227, v227, v254\n"
"v_accvgpr_write_b32 a155, v227\n"
"v_accvgpr_read_b32 v228, a156\n"
"v_mul_f32 v228, v228, v254\n"
"v_accvgpr_write_b32 a156, v228\n"
"v_accvgpr_read_b32 v229, a157\n"
"v_mul_f32 v229, v229, v254\n"
"v_accvgpr_write_b32 a157, v229\n"
"v_accvgpr_read_b32 v230, a158\n"
"v_mul_f32 v230, v230, v254\n"
"v_accvgpr_write_b32 a158, v230\n"
"v_accvgpr_read_b32 v231, a159\n"
"v_mul_f32 v231, v231, v254\n"
"v_accvgpr_write_b32 a159, v231\n"
"v_exp_f32 v224, v80\n"
"v_exp_f32 v225, v81\n"
"v_exp_f32 v226, v82\n"
"v_exp_f32 v227, v83\n"
"v_add_f32 v232, v224, v225\n"
"v_cvt_pk_bf16_f32 v188, v224, v225\n"
"v_exp_f32 v228, v84\n"
"v_exp_f32 v229, v85\n"
"v_add_f32 v232, v232, v226\n"
"v_add_f32 v232, v232, v227\n"
"v_cvt_pk_bf16_f32 v189, v226, v227\n"
"v_exp_f32 v230, v86\n"
"v_exp_f32 v231, v87\n"
"v_add_f32 v232, v232, v228\n"
"v_add_f32 v232, v232, v229\n"
"v_cvt_pk_bf16_f32 v190, v228, v229\n"
"v_exp_f32 v224, v88\n"
"v_exp_f32 v225, v89\n"
"v_add_f32 v232, v232, v230\n"
"v_add_f32 v232, v232, v231\n"
"v_cvt_pk_bf16_f32 v191, v230, v231\n"
"v_exp_f32 v226, v90\n"
"v_exp_f32 v227, v91\n"
"v_add_f32 v232, v232, v224\n"
"v_add_f32 v232, v232, v225\n"
"v_cvt_pk_bf16_f32 v192, v224, v225\n"
"v_exp_f32 v228, v92\n"
"v_exp_f32 v229, v93\n"
"v_add_f32 v232, v232, v226\n"
"v_add_f32 v232, v232, v227\n"
"v_cvt_pk_bf16_f32 v193, v226, v227\n"
"v_exp_f32 v230, v94\n"
"v_exp_f32 v231, v95\n"
"v_add_f32 v232, v232, v228\n"
"v_add_f32 v232, v232, v229\n"
"v_cvt_pk_bf16_f32 v194, v228, v229\n"
"v_exp_f32 v224, v96\n"
"v_exp_f32 v225, v97\n"
"v_add_f32 v232, v232, v230\n"
"v_add_f32 v232, v232, v231\n"
"v_cvt_pk_bf16_f32 v195, v230, v231\n"
"v_exp_f32 v226, v98\n"
"v_exp_f32 v227, v99\n"
"v_add_f32 v233, v224, v225\n"
"v_cvt_pk_bf16_f32 v196, v224, v225\n"
"v_exp_f32 v228, v100\n"
"v_exp_f32 v229, v101\n"
"v_add_f32 v233, v233, v226\n"
"v_add_f32 v233, v233, v227\n"
"v_cvt_pk_bf16_f32 v197, v226, v227\n"
"v_exp_f32 v230, v102\n"
"v_exp_f32 v231, v103\n"
"v_add_f32 v233, v233, v228\n"
"v_add_f32 v233, v233, v229\n"
"v_cvt_pk_bf16_f32 v198, v228, v229\n"
"v_exp_f32 v224, v104\n"
"v_exp_f32 v225, v105\n"
"v_add_f32 v233, v233, v230\n"
"v_add_f32 v233, v233, v231\n"
"v_cvt_pk_bf16_f32 v199, v230, v231\n"
"v_exp_f32 v226, v106\n"
"v_exp_f32 v227, v107\n"
"v_add_f32 v233, v233, v224\n"
"v_add_f32 v233, v233, v225\n"
"v_cvt_pk_bf16_f32 v200, v224, v225\n"
"v_exp_f32 v228, v108\n"
"v_exp_f32 v229, v109\n"
"v_add_f32 v233, v233, v226\n"
"v_add_f32 v233, v233, v227\n"
"v_cvt_pk_bf16_f32 v201, v226, v227\n"
"v_exp_f32 v230, v110\n"
"v_exp_f32 v231, v111\n"
"v_add_f32 v233, v233, v228\n"
"v_add_f32 v233, v233, v229\n"
"v_cvt_pk_bf16_f32 v202, v228, v229\n"
"v_exp_f32 v224, v112\n"
"v_exp_f32 v225, v113\n"
"v_add_f32 v233, v233, v230\n"
"v_add_f32 v233, v233, v231\n"
"v_cvt_pk_bf16_f32 v203, v230, v231\n"
"v_exp_f32 v226, v114\n"
"v_exp_f32 v227, v115\n"
"v_add_f32 v234, v224, v225\n"
"v_cvt_pk_bf16_f32 v204, v224, v225\n"
"v_add_f32 v234, v234, v226\n"
"v_add_f32 v234, v234, v227\n"
"v_cvt_pk_bf16_f32 v205, v226, v227\n"
"s_nop 1\n"
"s_cmp_eq_u32 s89, 0\n"
"s_cbranch_scc1 Lbacki0_%=\n"
"s_branch Lbackev_%=\n"
"Lstubi0_%=:\n"
"s_mov_b32 s89, 0\n"
"s_branch Lslow0_%=\n"
"Lstubev_%=:\n"
"s_mov_b32 s89, 1\n"
"s_branch Lslow0_%=\n"
"Lslow1_%=:\n"
"s_nop 15\n"
"s_nop 7\n"
"v_max3_f32 v248, v116, v117, v118\n"
"v_max3_f32 v248, v248, v119, v120\n"
"v_max3_f32 v248, v248, v121, v122\n"
"v_max3_f32 v248, v248, v123, v124\n"
"v_max3_f32 v248, v248, v125, v126\n"
"v_max3_f32 v248, v248, v127, v128\n"
"v_max3_f32 v248, v248, v129, v130\n"
"v_max_f32 v248, v248, v131\n"
"v_max3_f32 v249, v132, v133, v134\n"
"v_max3_f32 v249, v249, v135, v136\n"
"v_max3_f32 v249, v249, v137, v138\n"
"v_max3_f32 v249, v249, v139, v140\n"
"v_max3_f32 v249, v249, v141, v142\n"
"v_max3_f32 v249, v249, v143, v144\n"
"v_max3_f32 v249, v249, v145, v146\n"
"v_max_f32 v249, v249, v147\n"
"v_max3_f32 v250, v148, v149, v150\n"
"v_max_f32 v250, v250, v151\n"
"v_mov_b32 v251, v248\n"
"s_nop 1\n"
"v_permlane32_swap_b32 v248, v251\n"
"v_max_f32 v248, v248, v251\n"
"v_mov_b32 v251, v249\n"
"s_nop 1\n"
"v_permlane32_swap_b32 v249, v251\n"
"v_max_f32 v249, v249, v251\n"
"v_mov_b32 v251, v250\n"
"s_nop 1\n"
"v_permlane32_swap_b32 v250, v251\n"
"v_max_f32 v250, v250, v251\n"
"v_mov_b32 v251, v250\n"
"s_nop 1\n"
"v_permlane16_swap_b32 v250, v251\n"
"v_max_f32 v250, v250, v251\n"
"v_max_f32 v248, 0, v248\n"
"v_max_f32 v249, 0, v249\n"
"v_max_f32 v250, 0, v250\n"
"v_exp_f32 v252, -v248\n"
"v_exp_f32 v253, -v249\n"
"v_exp_f32 v254, -v250\n"
"v_mul_f32 v235, v235, v252\n"
"v_mul_f32 v236, v236, v253\n"
"v_mul_f32 v237, v237, v254\n"
"v_sub_f32 v116, v116, v248\n"
"v_sub_f32 v117, v117, v248\n"
"v_sub_f32 v118, v118, v248\n"
"v_sub_f32 v119, v119, v248\n"
"v_sub_f32 v120, v120, v248\n"
"v_sub_f32 v121, v121, v248\n"
"v_sub_f32 v122, v122, v248\n"
"v_sub_f32 v123, v123, v248\n"
"v_sub_f32 v124, v124, v248\n"
"v_sub_f32 v125, v125, v248\n"
"v_sub_f32 v126, v126, v248\n"
"v_sub_f32 v127, v127, v248\n"
"v_sub_f32 v128, v128, v248\n"
"v_sub_f32 v129, v129, v248\n"
"v_sub_f32 v130, v130, v248\n"
"v_sub_f32 v131, v131, v248\n"
"v_sub_f32 v152, v152, v248\n"
"v_sub_f32 v153, v153, v248\n"
"v_sub_f32 v154, v154, v248\n"
"v_sub_f32 v155, v155, v248\n"
"v_sub_f32 v156, v156, v248\n"
"v_sub_f32 v157, v157, v248\n"
"v_sub_f32 v158, v158, v248\n"
"v_sub_f32 v159, v159, v248\n"
"v_sub_f32 v160, v160, v248\n"
"v_sub_f32 v161, v161, v248\n"
"v_sub_f32 v162, v162, v248\n"
"v_sub_f32 v163, v163, v248\n"
"v_sub_f32 v164, v164, v248\n"
"v_sub_f32 v165, v165, v248\n"
"v_sub_f32 v166, v166, v248\n"
"v_sub_f32 v167, v167, v248\n"
"v_sub_f32 v132, v132, v249\n"
"v_sub_f32 v133, v133, v249\n"
"v_sub_f32 v134, v134, v249\n"
"v_sub_f32 v135, v135, v249\n"
"v_sub_f32 v136, v136, v249\n"
"v_sub_f32 v137, v137, v249\n"
"v_sub_f32 v138, v138, v249\n"
"v_sub_f32 v139, v139, v249\n"
"v_sub_f32 v140, v140, v249\n"
"v_sub_f32 v141, v141, v249\n"
"v_sub_f32 v142, v142, v249\n"
"v_sub_f32 v143, v143, v249\n"
"v_sub_f32 v144, v144, v249\n"
"v_sub_f32 v145, v145, v249\n"
"v_sub_f32 v146, v146, v249\n"
"v_sub_f32 v147, v147, v249\n"
"v_sub_f32 v168, v168, v249\n"
"v_sub_f32 v169, v169, v249\n"
"v_sub_f32 v170, v170, v249\n"
"v_sub_f32 v171, v171, v249\n"
"v_sub_f32 v172, v172, v249\n"
"v_sub_f32 v173, v173, v249\n"
"v_sub_f32 v174, v174, v249\n"
"v_sub_f32 v175, v175, v249\n"
"v_sub_f32 v176, v176, v249\n"
"v_sub_f32 v177, v177, v249\n"
"v_sub_f32 v178, v178, v249\n"
"v_sub_f32 v179, v179, v249\n"
"v_sub_f32 v180, v180, v249\n"
"v_sub_f32 v181, v181, v249\n"
"v_sub_f32 v182, v182, v249\n"
"v_sub_f32 v183, v183, v249\n"
"v_sub_f32 v148, v148, v250\n"
"v_sub_f32 v149, v149, v250\n"
"v_sub_f32 v150, v150, v250\n"
"v_sub_f32 v151, v151, v250\n"
"v_sub_f32 v184, v184, v250\n"
"v_sub_f32 v185, v185, v250\n"
"v_sub_f32 v186, v186, v250\n"
"v_sub_f32 v187, v187, v250\n"
"s_cmp_eq_u32 s89, 1\n"
"s_cbranch_scc1 Lslown1_%=\n"
"v_sub_f32 v80, v80, v248\n"
"v_sub_f32 v81, v81, v248\n"
"v_sub_f32 v82, v82, v248\n"
"v_sub_f32 v83, v83, v248\n"
"v_sub_f32 v84, v84, v248\n"
"v_sub_f32 v85, v85, v248\n"
"v_sub_f32 v86, v86, v248\n"
"v_sub_f32 v87, v87, v248\n"
"v_sub_f32 v88, v88, v248\n"
"v_sub_f32 v89, v89, v248\n"
"v_sub_f32 v90, v90, v248\n"
"v_sub_f32 v91, v91, v248\n"
"v_sub_f32 v92, v92, v248\n"
"v_sub_f32 v93, v93, v248\n"
"v_sub_f32 v94, v94, v248\n"
"v_sub_f32 v95, v95, v248\n"
"v_sub_f32 v96, v96, v249\n"
"v_sub_f32 v97, v97, v249\n"
"v_sub_f32 v98, v98, v249\n"
"v_sub_f32 v99, v99, v249\n"
"v_sub_f32 v100, v100, v249\n"
"v_sub_f32 v101, v101, v249\n"
"v_sub_f32 v102, v102, v249\n"
"v_sub_f32 v103, v103, v249\n"
"v_sub_f32 v104, v104, v249\n"
"v_sub_f32 v105, v105, v249\n"
"v_sub_f32 v106, v106, v249\n"
"v_sub_f32 v107, v107, v249\n"
"v_sub_f32 v108, v108, v249\n"
"v_sub_f32 v109, v109, v249\n"
"v_sub_f32 v110, v110, v249\n"
"v_sub_f32 v111, v111, v249\n"
"v_sub_f32 v112, v112, v250\n"
"v_sub_f32 v113, v113, v250\n"
"v_sub_f32 v114, v114, v250\n"
"v_sub_f32 v115, v115, v250\n"
"Lslown1_%=:\n"
"v_accvgpr_read_b32 v224, a0\n"
"v_mul_f32 v224, v224, v252\n"
"v_accvgpr_write_b32 a0, v224\n"
"v_accvgpr_read_b32 v225, a1\n"
"v_mul_f32 v225, v225, v252\n"
"v_accvgpr_write_b32 a1, v225\n"
"v_accvgpr_read_b32 v226, a2\n"
"v_mul_f32 v226, v226, v252\n"
"v_accvgpr_write_b32 a2, v226\n"
"v_accvgpr_read_b32 v227, a3\n"
"v_mul_f32 v227, v227, v252\n"
"v_accvgpr_write_b32 a3, v227\n"
"v_accvgpr_read_b32 v228, a4\n"
"v_mul_f32 v228, v228, v252\n"
"v_accvgpr_write_b32 a4, v228\n"
"v_accvgpr_read_b32 v229, a5\n"
"v_mul_f32 v229, v229, v252\n"
"v_accvgpr_write_b32 a5, v229\n"
"v_accvgpr_read_b32 v230, a6\n"
"v_mul_f32 v230, v230, v252\n"
"v_accvgpr_write_b32 a6, v230\n"
"v_accvgpr_read_b32 v231, a7\n"
"v_mul_f32 v231, v231, v252\n"
"v_accvgpr_write_b32 a7, v231\n"
"v_accvgpr_read_b32 v224, a8\n"
"v_mul_f32 v224, v224, v252\n"
"v_accvgpr_write_b32 a8, v224\n"
"v_accvgpr_read_b32 v225, a9\n"
"v_mul_f32 v225, v225, v252\n"
"v_accvgpr_write_b32 a9, v225\n"
"v_accvgpr_read_b32 v226, a10\n"
"v_mul_f32 v226, v226, v252\n"
"v_accvgpr_write_b32 a10, v226\n"
"v_accvgpr_read_b32 v227, a11\n"
"v_mul_f32 v227, v227, v252\n"
"v_accvgpr_write_b32 a11, v227\n"
"v_accvgpr_read_b32 v228, a12\n"
"v_mul_f32 v228, v228, v252\n"
"v_accvgpr_write_b32 a12, v228\n"
"v_accvgpr_read_b32 v229, a13\n"
"v_mul_f32 v229, v229, v252\n"
"v_accvgpr_write_b32 a13, v229\n"
"v_accvgpr_read_b32 v230, a14\n"
"v_mul_f32 v230, v230, v252\n"
"v_accvgpr_write_b32 a14, v230\n"
"v_accvgpr_read_b32 v231, a15\n"
"v_mul_f32 v231, v231, v252\n"
"v_accvgpr_write_b32 a15, v231\n"
"v_accvgpr_read_b32 v224, a16\n"
"v_mul_f32 v224, v224, v253\n"
"v_accvgpr_write_b32 a16, v224\n"
"v_accvgpr_read_b32 v225, a17\n"
"v_mul_f32 v225, v225, v253\n"
"v_accvgpr_write_b32 a17, v225\n"
"v_accvgpr_read_b32 v226, a18\n"
"v_mul_f32 v226, v226, v253\n"
"v_accvgpr_write_b32 a18, v226\n"
"v_accvgpr_read_b32 v227, a19\n"
"v_mul_f32 v227, v227, v253\n"
"v_accvgpr_write_b32 a19, v227\n"
"v_accvgpr_read_b32 v228, a20\n"
"v_mul_f32 v228, v228, v253\n"
"v_accvgpr_write_b32 a20, v228\n"
"v_accvgpr_read_b32 v229, a21\n"
"v_mul_f32 v229, v229, v253\n"
"v_accvgpr_write_b32 a21, v229\n"
"v_accvgpr_read_b32 v230, a22\n"
"v_mul_f32 v230, v230, v253\n"
"v_accvgpr_write_b32 a22, v230\n"
"v_accvgpr_read_b32 v231, a23\n"
"v_mul_f32 v231, v231, v253\n"
"v_accvgpr_write_b32 a23, v231\n"
"v_accvgpr_read_b32 v224, a24\n"
"v_mul_f32 v224, v224, v253\n"
"v_accvgpr_write_b32 a24, v224\n"
"v_accvgpr_read_b32 v225, a25\n"
"v_mul_f32 v225, v225, v253\n"
"v_accvgpr_write_b32 a25, v225\n"
"v_accvgpr_read_b32 v226, a26\n"
"v_mul_f32 v226, v226, v253\n"
"v_accvgpr_write_b32 a26, v226\n"
"v_accvgpr_read_b32 v227, a27\n"
"v_mul_f32 v227, v227, v253\n"
"v_accvgpr_write_b32 a27, v227\n"
"v_accvgpr_read_b32 v228, a28\n"
"v_mul_f32 v228, v228, v253\n"
"v_accvgpr_write_b32 a28, v228\n"
"v_accvgpr_read_b32 v229, a29\n"
"v_mul_f32 v229, v229, v253\n"
"v_accvgpr_write_b32 a29, v229\n"
"v_accvgpr_read_b32 v230, a30\n"
"v_mul_f32 v230, v230, v253\n"
"v_accvgpr_write_b32 a30, v230\n"
"v_accvgpr_read_b32 v231, a31\n"
"v_mul_f32 v231, v231, v253\n"
"v_accvgpr_write_b32 a31, v231\n"
"v_accvgpr_read_b32 v224, a32\n"
"v_mul_f32 v224, v224, v252\n"
"v_accvgpr_write_b32 a32, v224\n"
"v_accvgpr_read_b32 v225, a33\n"
"v_mul_f32 v225, v225, v252\n"
"v_accvgpr_write_b32 a33, v225\n"
"v_accvgpr_read_b32 v226, a34\n"
"v_mul_f32 v226, v226, v252\n"
"v_accvgpr_write_b32 a34, v226\n"
"v_accvgpr_read_b32 v227, a35\n"
"v_mul_f32 v227, v227, v252\n"
"v_accvgpr_write_b32 a35, v227\n"
"v_accvgpr_read_b32 v228, a36\n"
"v_mul_f32 v228, v228, v252\n"
"v_accvgpr_write_b32 a36, v228\n"
"v_accvgpr_read_b32 v229, a37\n"
"v_mul_f32 v229, v229, v252\n"
"v_accvgpr_write_b32 a37, v229\n"
"v_accvgpr_read_b32 v230, a38\n"
"v_mul_f32 v230, v230, v252\n"
"v_accvgpr_write_b32 a38, v230\n"
"v_accvgpr_read_b32 v231, a39\n"
"v_mul_f32 v231, v231, v252\n"
"v_accvgpr_write_b32 a39, v231\n"
"v_accvgpr_read_b32 v224, a40\n"
"v_mul_f32 v224, v224, v252\n"
"v_accvgpr_write_b32 a40, v224\n"
"v_accvgpr_read_b32 v225, a41\n"
"v_mul_f32 v225, v225, v252\n"
"v_accvgpr_write_b32 a41, v225\n"
"v_accvgpr_read_b32 v226, a42\n"
"v_mul_f32 v226, v226, v252\n"
"v_accvgpr_write_b32 a42, v226\n"
"v_accvgpr_read_b32 v227, a43\n"
"v_mul_f32 v227, v227, v252\n"
"v_accvgpr_write_b32 a43, v227\n"
"v_accvgpr_read_b32 v228, a44\n"
"v_mul_f32 v228, v228, v252\n"
"v_accvgpr_write_b32 a44, v228\n"
"v_accvgpr_read_b32 v229, a45\n"
"v_mul_f32 v229, v229, v252\n"
"v_accvgpr_write_b32 a45, v229\n"
"v_accvgpr_read_b32 v230, a46\n"
"v_mul_f32 v230, v230, v252\n"
"v_accvgpr_write_b32 a46, v230\n"
"v_accvgpr_read_b32 v231, a47\n"
"v_mul_f32 v231, v231, v252\n"
"v_accvgpr_write_b32 a47, v231\n"
"v_accvgpr_read_b32 v224, a48\n"
"v_mul_f32 v224, v224, v253\n"
"v_accvgpr_write_b32 a48, v224\n"
"v_accvgpr_read_b32 v225, a49\n"
"v_mul_f32 v225, v225, v253\n"
"v_accvgpr_write_b32 a49, v225\n"
"v_accvgpr_read_b32 v226, a50\n"
"v_mul_f32 v226, v226, v253\n"
"v_accvgpr_write_b32 a50, v226\n"
"v_accvgpr_read_b32 v227, a51\n"
"v_mul_f32 v227, v227, v253\n"
"v_accvgpr_write_b32 a51, v227\n"
"v_accvgpr_read_b32 v228, a52\n"
"v_mul_f32 v228, v228, v253\n"
"v_accvgpr_write_b32 a52, v228\n"
"v_accvgpr_read_b32 v229, a53\n"
"v_mul_f32 v229, v229, v253\n"
"v_accvgpr_write_b32 a53, v229\n"
"v_accvgpr_read_b32 v230, a54\n"
"v_mul_f32 v230, v230, v253\n"
"v_accvgpr_write_b32 a54, v230\n"
"v_accvgpr_read_b32 v231, a55\n"
"v_mul_f32 v231, v231, v253\n"
"v_accvgpr_write_b32 a55, v231\n"
"v_accvgpr_read_b32 v224, a56\n"
"v_mul_f32 v224, v224, v253\n"
"v_accvgpr_write_b32 a56, v224\n"
"v_accvgpr_read_b32 v225, a57\n"
"v_mul_f32 v225, v225, v253\n"
"v_accvgpr_write_b32 a57, v225\n"
"v_accvgpr_read_b32 v226, a58\n"
"v_mul_f32 v226, v226, v253\n"
"v_accvgpr_write_b32 a58, v226\n"
"v_accvgpr_read_b32 v227, a59\n"
"v_mul_f32 v227, v227, v253\n"
"v_accvgpr_write_b32 a59, v227\n"
"v_accvgpr_read_b32 v228, a60\n"
"v_mul_f32 v228, v228, v253\n"
"v_accvgpr_write_b32 a60, v228\n"
"v_accvgpr_read_b32 v229, a61\n"
"v_mul_f32 v229, v229, v253\n"
"v_accvgpr_write_b32 a61, v229\n"
"v_accvgpr_read_b32 v230, a62\n"
"v_mul_f32 v230, v230, v253\n"
"v_accvgpr_write_b32 a62, v230\n"
"v_accvgpr_read_b32 v231, a63\n"
"v_mul_f32 v231, v231, v253\n"
"v_accvgpr_write_b32 a63, v231\n"
"v_accvgpr_read_b32 v224, a64\n"
"v_mul_f32 v224, v224, v252\n"
"v_accvgpr_write_b32 a64, v224\n"
"v_accvgpr_read_b32 v225, a65\n"
"v_mul_f32 v225, v225, v252\n"
"v_accvgpr_write_b32 a65, v225\n"
"v_accvgpr_read_b32 v226, a66\n"
"v_mul_f32 v226, v226, v252\n"
"v_accvgpr_write_b32 a66, v226\n"
"v_accvgpr_read_b32 v227, a67\n"
"v_mul_f32 v227, v227, v252\n"
"v_accvgpr_write_b32 a67, v227\n"
"v_accvgpr_read_b32 v228, a68\n"
"v_mul_f32 v228, v228, v252\n"
"v_accvgpr_write_b32 a68, v228\n"
"v_accvgpr_read_b32 v229, a69\n"
"v_mul_f32 v229, v229, v252\n"
"v_accvgpr_write_b32 a69, v229\n"
"v_accvgpr_read_b32 v230, a70\n"
"v_mul_f32 v230, v230, v252\n"
"v_accvgpr_write_b32 a70, v230\n"
"v_accvgpr_read_b32 v231, a71\n"
"v_mul_f32 v231, v231, v252\n"
"v_accvgpr_write_b32 a71, v231\n"
"v_accvgpr_read_b32 v224, a72\n"
"v_mul_f32 v224, v224, v252\n"
"v_accvgpr_write_b32 a72, v224\n"
"v_accvgpr_read_b32 v225, a73\n"
"v_mul_f32 v225, v225, v252\n"
"v_accvgpr_write_b32 a73, v225\n"
"v_accvgpr_read_b32 v226, a74\n"
"v_mul_f32 v226, v226, v252\n"
"v_accvgpr_write_b32 a74, v226\n"
"v_accvgpr_read_b32 v227, a75\n"
"v_mul_f32 v227, v227, v252\n"
"v_accvgpr_write_b32 a75, v227\n"
"v_accvgpr_read_b32 v228, a76\n"
"v_mul_f32 v228, v228, v252\n"
"v_accvgpr_write_b32 a76, v228\n"
"v_accvgpr_read_b32 v229, a77\n"
"v_mul_f32 v229, v229, v252\n"
"v_accvgpr_write_b32 a77, v229\n"
"v_accvgpr_read_b32 v230, a78\n"
"v_mul_f32 v230, v230, v252\n"
"v_accvgpr_write_b32 a78, v230\n"
"v_accvgpr_read_b32 v231, a79\n"
"v_mul_f32 v231, v231, v252\n"
"v_accvgpr_write_b32 a79, v231\n"
"v_accvgpr_read_b32 v224, a80\n"
"v_mul_f32 v224, v224, v253\n"
"v_accvgpr_write_b32 a80, v224\n"
"v_accvgpr_read_b32 v225, a81\n"
"v_mul_f32 v225, v225, v253\n"
"v_accvgpr_write_b32 a81, v225\n"
"v_accvgpr_read_b32 v226, a82\n"
"v_mul_f32 v226, v226, v253\n"
"v_accvgpr_write_b32 a82, v226\n"
"v_accvgpr_read_b32 v227, a83\n"
"v_mul_f32 v227, v227, v253\n"
"v_accvgpr_write_b32 a83, v227\n"
"v_accvgpr_read_b32 v228, a84\n"
"v_mul_f32 v228, v228, v253\n"
"v_accvgpr_write_b32 a84, v228\n"
"v_accvgpr_read_b32 v229, a85\n"
"v_mul_f32 v229, v229, v253\n"
"v_accvgpr_write_b32 a85, v229\n"
"v_accvgpr_read_b32 v230, a86\n"
"v_mul_f32 v230, v230, v253\n"
"v_accvgpr_write_b32 a86, v230\n"
"v_accvgpr_read_b32 v231, a87\n"
"v_mul_f32 v231, v231, v253\n"
"v_accvgpr_write_b32 a87, v231\n"
"v_accvgpr_read_b32 v224, a88\n"
"v_mul_f32 v224, v224, v253\n"
"v_accvgpr_write_b32 a88, v224\n"
"v_accvgpr_read_b32 v225, a89\n"
"v_mul_f32 v225, v225, v253\n"
"v_accvgpr_write_b32 a89, v225\n"
"v_accvgpr_read_b32 v226, a90\n"
"v_mul_f32 v226, v226, v253\n"
"v_accvgpr_write_b32 a90, v226\n"
"v_accvgpr_read_b32 v227, a91\n"
"v_mul_f32 v227, v227, v253\n"
"v_accvgpr_write_b32 a91, v227\n"
"v_accvgpr_read_b32 v228, a92\n"
"v_mul_f32 v228, v228, v253\n"
"v_accvgpr_write_b32 a92, v228\n"
"v_accvgpr_read_b32 v229, a93\n"
"v_mul_f32 v229, v229, v253\n"
"v_accvgpr_write_b32 a93, v229\n"
"v_accvgpr_read_b32 v230, a94\n"
"v_mul_f32 v230, v230, v253\n"
"v_accvgpr_write_b32 a94, v230\n"
"v_accvgpr_read_b32 v231, a95\n"
"v_mul_f32 v231, v231, v253\n"
"v_accvgpr_write_b32 a95, v231\n"
"v_accvgpr_read_b32 v224, a96\n"
"v_mul_f32 v224, v224, v252\n"
"v_accvgpr_write_b32 a96, v224\n"
"v_accvgpr_read_b32 v225, a97\n"
"v_mul_f32 v225, v225, v252\n"
"v_accvgpr_write_b32 a97, v225\n"
"v_accvgpr_read_b32 v226, a98\n"
"v_mul_f32 v226, v226, v252\n"
"v_accvgpr_write_b32 a98, v226\n"
"v_accvgpr_read_b32 v227, a99\n"
"v_mul_f32 v227, v227, v252\n"
"v_accvgpr_write_b32 a99, v227\n"
"v_accvgpr_read_b32 v228, a100\n"
"v_mul_f32 v228, v228, v252\n"
"v_accvgpr_write_b32 a100, v228\n"
"v_accvgpr_read_b32 v229, a101\n"
"v_mul_f32 v229, v229, v252\n"
"v_accvgpr_write_b32 a101, v229\n"
"v_accvgpr_read_b32 v230, a102\n"
"v_mul_f32 v230, v230, v252\n"
"v_accvgpr_write_b32 a102, v230\n"
"v_accvgpr_read_b32 v231, a103\n"
"v_mul_f32 v231, v231, v252\n"
"v_accvgpr_write_b32 a103, v231\n"
"v_accvgpr_read_b32 v224, a104\n"
"v_mul_f32 v224, v224, v252\n"
"v_accvgpr_write_b32 a104, v224\n"
"v_accvgpr_read_b32 v225, a105\n"
"v_mul_f32 v225, v225, v252\n"
"v_accvgpr_write_b32 a105, v225\n"
"v_accvgpr_read_b32 v226, a106\n"
"v_mul_f32 v226, v226, v252\n"
"v_accvgpr_write_b32 a106, v226\n"
"v_accvgpr_read_b32 v227, a107\n"
"v_mul_f32 v227, v227, v252\n"
"v_accvgpr_write_b32 a107, v227\n"
"v_accvgpr_read_b32 v228, a108\n"
"v_mul_f32 v228, v228, v252\n"
"v_accvgpr_write_b32 a108, v228\n"
"v_accvgpr_read_b32 v229, a109\n"
"v_mul_f32 v229, v229, v252\n"
"v_accvgpr_write_b32 a109, v229\n"
"v_accvgpr_read_b32 v230, a110\n"
"v_mul_f32 v230, v230, v252\n"
"v_accvgpr_write_b32 a110, v230\n"
"v_accvgpr_read_b32 v231, a111\n"
"v_mul_f32 v231, v231, v252\n"
"v_accvgpr_write_b32 a111, v231\n"
"v_accvgpr_read_b32 v224, a112\n"
"v_mul_f32 v224, v224, v253\n"
"v_accvgpr_write_b32 a112, v224\n"
"v_accvgpr_read_b32 v225, a113\n"
"v_mul_f32 v225, v225, v253\n"
"v_accvgpr_write_b32 a113, v225\n"
"v_accvgpr_read_b32 v226, a114\n"
"v_mul_f32 v226, v226, v253\n"
"v_accvgpr_write_b32 a114, v226\n"
"v_accvgpr_read_b32 v227, a115\n"
"v_mul_f32 v227, v227, v253\n"
"v_accvgpr_write_b32 a115, v227\n"
"v_accvgpr_read_b32 v228, a116\n"
"v_mul_f32 v228, v228, v253\n"
"v_accvgpr_write_b32 a116, v228\n"
"v_accvgpr_read_b32 v229, a117\n"
"v_mul_f32 v229, v229, v253\n"
"v_accvgpr_write_b32 a117, v229\n"
"v_accvgpr_read_b32 v230, a118\n"
"v_mul_f32 v230, v230, v253\n"
"v_accvgpr_write_b32 a118, v230\n"
"v_accvgpr_read_b32 v231, a119\n"
"v_mul_f32 v231, v231, v253\n"
"v_accvgpr_write_b32 a119, v231\n"
"v_accvgpr_read_b32 v224, a120\n"
"v_mul_f32 v224, v224, v253\n"
"v_accvgpr_write_b32 a120, v224\n"
"v_accvgpr_read_b32 v225, a121\n"
"v_mul_f32 v225, v225, v253\n"
"v_accvgpr_write_b32 a121, v225\n"
"v_accvgpr_read_b32 v226, a122\n"
"v_mul_f32 v226, v226, v253\n"
"v_accvgpr_write_b32 a122, v226\n"
"v_accvgpr_read_b32 v227, a123\n"
"v_mul_f32 v227, v227, v253\n"
"v_accvgpr_write_b32 a123, v227\n"
"v_accvgpr_read_b32 v228, a124\n"
"v_mul_f32 v228, v228, v253\n"
"v_accvgpr_write_b32 a124, v228\n"
"v_accvgpr_read_b32 v229, a125\n"
"v_mul_f32 v229, v229, v253\n"
"v_accvgpr_write_b32 a125, v229\n"
"v_accvgpr_read_b32 v230, a126\n"
"v_mul_f32 v230, v230, v253\n"
"v_accvgpr_write_b32 a126, v230\n"
"v_accvgpr_read_b32 v231, a127\n"
"v_mul_f32 v231, v231, v253\n"
"v_accvgpr_write_b32 a127, v231\n"
"v_accvgpr_read_b32 v224, a128\n"
"v_mul_f32 v224, v224, v254\n"
"v_accvgpr_write_b32 a128, v224\n"
"v_accvgpr_read_b32 v225, a129\n"
"v_mul_f32 v225, v225, v254\n"
"v_accvgpr_write_b32 a129, v225\n"
"v_accvgpr_read_b32 v226, a130\n"
"v_mul_f32 v226, v226, v254\n"
"v_accvgpr_write_b32 a130, v226\n"
"v_accvgpr_read_b32 v227, a131\n"
"v_mul_f32 v227, v227, v254\n"
"v_accvgpr_write_b32 a131, v227\n"
"v_accvgpr_read_b32 v228, a132\n"
"v_mul_f32 v228, v228, v254\n"
"v_accvgpr_write_b32 a132, v228\n"
"v_accvgpr_read_b32 v229, a133\n"
"v_mul_f32 v229, v229, v254\n"
"v_accvgpr_write_b32 a133, v229\n"
"v_accvgpr_read_b32 v230, a134\n"
"v_mul_f32 v230, v230, v254\n"
"v_accvgpr_write_b32 a134, v230\n"
"v_accvgpr_read_b32 v231, a135\n"
"v_mul_f32 v231, v231, v254\n"
"v_accvgpr_write_b32 a135, v231\n"
"v_accvgpr_read_b32 v224, a136\n"
"v_mul_f32 v224, v224, v254\n"
"v_accvgpr_write_b32 a136, v224\n"
"v_accvgpr_read_b32 v225, a137\n"
"v_mul_f32 v225, v225, v254\n"
"v_accvgpr_write_b32 a137, v225\n"
"v_accvgpr_read_b32 v226, a138\n"
"v_mul_f32 v226, v226, v254\n"
"v_accvgpr_write_b32 a138, v226\n"
"v_accvgpr_read_b32 v227, a139\n"
"v_mul_f32 v227, v227, v254\n"
"v_accvgpr_write_b32 a139, v227\n"
"v_accvgpr_read_b32 v228, a140\n"
"v_mul_f32 v228, v228, v254\n"
"v_accvgpr_write_b32 a140, v228\n"
"v_accvgpr_read_b32 v229, a141\n"
"v_mul_f32 v229, v229, v254\n"
"v_accvgpr_write_b32 a141, v229\n"
"v_accvgpr_read_b32 v230, a142\n"
"v_mul_f32 v230, v230, v254\n"
"v_accvgpr_write_b32 a142, v230\n"
"v_accvgpr_read_b32 v231, a143\n"
"v_mul_f32 v231, v231, v254\n"
"v_accvgpr_write_b32 a143, v231\n"
"v_accvgpr_read_b32 v224, a144\n"
"v_mul_f32 v224, v224, v254\n"
"v_accvgpr_write_b32 a144, v224\n"
"v_accvgpr_read_b32 v225, a145\n"
"v_mul_f32 v225, v225, v254\n"
"v_accvgpr_write_b32 a145, v225\n"
"v_accvgpr_read_b32 v226, a146\n"
"v_mul_f32 v226, v226, v254\n"
"v_accvgpr_write_b32 a146, v226\n"
"v_accvgpr_read_b32 v227, a147\n"
"v_mul_f32 v227, v227, v254\n"
"v_accvgpr_write_b32 a147, v227\n"
"v_accvgpr_read_b32 v228, a148\n"
"v_mul_f32 v228, v228, v254\n"
"v_accvgpr_write_b32 a148, v228\n"
"v_accvgpr_read_b32 v229, a149\n"
"v_mul_f32 v229, v229, v254\n"
"v_accvgpr_write_b32 a149, v229\n"
"v_accvgpr_read_b32 v230, a150\n"
"v_mul_f32 v230, v230, v254\n"
"v_accvgpr_write_b32 a150, v230\n"
"v_accvgpr_read_b32 v231, a151\n"
"v_mul_f32 v231, v231, v254\n"
"v_accvgpr_write_b32 a151, v231\n"
"v_accvgpr_read_b32 v224, a152\n"
"v_mul_f32 v224, v224, v254\n"
"v_accvgpr_write_b32 a152, v224\n"
"v_accvgpr_read_b32 v225, a153\n"
"v_mul_f32 v225, v225, v254\n"
"v_accvgpr_write_b32 a153, v225\n"
"v_accvgpr_read_b32 v226, a154\n"
"v_mul_f32 v226, v226, v254\n"
"v_accvgpr_write_b32 a154, v226\n"
"v_accvgpr_read_b32 v227, a155\n"
"v_mul_f32 v227, v227, v254\n"
"v_accvgpr_write_b32 a155, v227\n"
"v_accvgpr_read_b32 v228, a156\n"
"v_mul_f32 v228, v228, v254\n"
"v_accvgpr_write_b32 a156, v228\n"
"v_accvgpr_read_b32 v229, a157\n"
"v_mul_f32 v229, v229, v254\n"
"v_accvgpr_write_b32 a157, v229\n"
"v_accvgpr_read_b32 v230, a158\n"
"v_mul_f32 v230, v230, v254\n"
"v_accvgpr_write_b32 a158, v230\n"
"v_accvgpr_read_b32 v231, a159\n"
"v_mul_f32 v231, v231, v254\n"
"v_accvgpr_write_b32 a159, v231\n"
"v_exp_f32 v224, v116\n"
"v_exp_f32 v225, v117\n"
"v_exp_f32 v226, v118\n"
"v_exp_f32 v227, v119\n"
"v_add_f32 v232, v224, v225\n"
"v_cvt_pk_bf16_f32 v206, v224, v225\n"
"v_exp_f32 v228, v120\n"
"v_exp_f32 v229, v121\n"
"v_add_f32 v232, v232, v226\n"
"v_add_f32 v232, v232, v227\n"
"v_cvt_pk_bf16_f32 v207, v226, v227\n"
"v_exp_f32 v230, v122\n"
"v_exp_f32 v231, v123\n"
"v_add_f32 v232, v232, v228\n"
"v_add_f32 v232, v232, v229\n"
"v_cvt_pk_bf16_f32 v208, v228, v229\n"
"v_exp_f32 v224, v124\n"
"v_exp_f32 v225, v125\n"
"v_add_f32 v232, v232, v230\n"
"v_add_f32 v232, v232, v231\n"
"v_cvt_pk_bf16_f32 v209, v230, v231\n"
"v_exp_f32 v226, v126\n"
"v_exp_f32 v227, v127\n"
"v_add_f32 v232, v232, v224\n"
"v_add_f32 v232, v232, v225\n"
"v_cvt_pk_bf16_f32 v210, v224, v225\n"
"v_exp_f32 v228, v128\n"
"v_exp_f32 v229, v129\n"
"v_add_f32 v232, v232, v226\n"
"v_add_f32 v232, v232, v227\n"
"v_cvt_pk_bf16_f32 v211, v226, v227\n"
"v_exp_f32 v230, v130\n"
"v_exp_f32 v231, v131\n"
"v_add_f32 v232, v232, v228\n"
"v_add_f32 v232, v232, v229\n"
"v_cvt_pk_bf16_f32 v212, v228, v229\n"
"v_exp_f32 v224, v132\n"
"v_exp_f32 v225, v133\n"
"v_add_f32 v232, v232, v230\n"
"v_add_f32 v232, v232, v231\n"
"v_cvt_pk_bf16_f32 v213, v230, v231\n"
"v_exp_f32 v226, v134\n"
"v_exp_f32 v227, v135\n"
"v_add_f32 v233, v224, v225\n"
"v_cvt_pk_bf16_f32 v214, v224, v225\n"
"v_exp_f32 v228, v136\n"
"v_exp_f32 v229, v137\n"
"v_add_f32 v233, v233, v226\n"
"v_add_f32 v233, v233, v227\n"
"v_cvt_pk_bf16_f32 v215, v226, v227\n"
"v_exp_f32 v230, v138\n"
"v_exp_f32 v231, v139\n"
"v_add_f32 v233, v233, v228\n"
"v_add_f32 v233, v233, v229\n"
"v_cvt_pk_bf16_f32 v216, v228, v229\n"
"v_exp_f32 v224, v140\n"
"v_exp_f32 v225, v141\n"
"v_add_f32 v233, v233, v230\n"
"v_add_f32 v233, v233, v231\n"
"v_cvt_pk_bf16_f32 v217, v230, v231\n"
"v_exp_f32 v226, v142\n"
"v_exp_f32 v227, v143\n"
"v_add_f32 v233, v233, v224\n"
"v_add_f32 v233, v233, v225\n"
"v_cvt_pk_bf16_f32 v218, v224, v225\n"
"v_exp_f32 v228, v144\n"
"v_exp_f32 v229, v145\n"
"v_add_f32 v233, v233, v226\n"
"v_add_f32 v233, v233, v227\n"
"v_cvt_pk_bf16_f32 v219, v226, v227\n"
"v_exp_f32 v230, v146\n"
"v_exp_f32 v231, v147\n"
"v_add_f32 v233, v233, v228\n"
"v_add_f32 v233, v233, v229\n"
"v_cvt_pk_bf16_f32 v220, v228, v229\n"
"v_exp_f32 v224, v148\n"
"v_exp_f32 v225, v149\n"
"v_add_f32 v233, v233, v230\n"
"v_add_f32 v233, v233, v231\n"
"v_cvt_pk_bf16_f32 v221, v230, v231\n"
"v_exp_f32 v226, v150\n"
"v_exp_f32 v227, v151\n"
"v_add_f32 v234, v224, v225\n"
"v_cvt_pk_bf16_f32 v222, v224, v225\n"
"v_add_f32 v234, v234, v226\n"
"v_add_f32 v234, v234, v227\n"
"v_cvt_pk_bf16_f32 v223, v226, v227\n"
"s_nop 1\n"
"s_cmp_eq_u32 s89, 0\n"
"s_cbranch_scc1 Lbackod_%=\n"
"s_branch Lbackil_%=\n"
"Lstubod_%=:\n"
"s_mov_b32 s89, 0\n"
"s_branch Lslow1_%=\n"
"Lstubil_%=:\n"
"s_mov_b32 s89, 1\n"
"s_branch Lslow1_%=\n"
  :: "s"(__builtin_amdgcn_kernarg_segment_ptr()), "s"(blockIdx.x), "v"(threadIdx.x), "s"((unsigned)(size_t)lds)
  : "v0", "v1", "v2", "v3", "v4", "v5", "v6", "v7", "v8", "v9", "v10", "v11", "v12", "v13", "v14", "v15", "v16", "v17", "v18", "v19", "v20", "v21", "v22", "v23", "v24", "v25", "v26", "v27", "v28", "v29", "v30", "v31", "v32", "v33", "v34", "v35", "v36", "v37", "v38", "v39", "v40", "v41", "v42", "v43", "v44", "v45", "v46", "v47", "v48", "v49", "v50", "v51", "v52", "v53", "v54", "v55", "v56", "v57", "v58", "v59", "v60", "v61", "v62", "v63", "v64", "v65", "v66", "v67", "v68", "v69", "v70", "v71", "v72", "v73", "v74", "v75", "v76", "v77", "v78", "v79", "v80", "v81", "v82", "v83", "v84", "v85", "v86", "v87", "v88", "v89", "v90", "v91", "v92", "v93", "v94", "v95", "v96", "v97", "v98", "v99", "v100", "v101", "v102", "v103", "v104", "v105", "v106", "v107", "v108", "v109", "v110", "v111", "v112", "v113", "v114", "v115", "v116", "v117", "v118", "v119", "v120", "v121", "v122", "v123", "v124", "v125", "v126", "v127", "v128", "v129", "v130", "v131", "v132", "v133", "v134", "v135", "v136", "v137", "v138", "v139", "v140", "v141", "v142", "v143", "v144", "v145", "v146", "v147", "v148", "v149", "v150", "v151", "v152", "v153", "v154", "v155", "v156", "v157", "v158", "v159", "v160", "v161", "v162", "v163", "v164", "v165", "v166", "v167", "v168", "v169", "v170", "v171", "v172", "v173", "v174", "v175", "v176", "v177", "v178", "v179", "v180", "v181", "v182", "v183", "v184", "v185", "v186", "v187", "v188", "v189", "v190", "v191", "v192", "v193", "v194", "v195", "v196", "v197", "v198", "v199", "v200", "v201", "v202", "v203", "v204", "v205", "v206", "v207", "v208", "v209", "v210", "v211", "v212", "v213", "v214", "v215", "v216", "v217", "v218", "v219", "v220", "v221", "v222", "v223", "v224", "v225", "v226", "v227", "v228", "v229", "v230", "v231", "v232", "v233", "v234", "v235", "v236", "v237", "v238", "v239", "v240", "v241", "v242", "v243", "v244", "v245", "v246", "v247", "v248", "v249", "v250", "v251", "v252", "v253", "v254", "a0", "a1", "a2", "a3", "a4", "a5", "a6", "a7", "a8", "a9", "a10", "a11", "a12", "a13", "a14", "a15", "a16", "a17", "a18", "a19", "a20", "a21", "a22", "a23", "a24", "a25", "a26", "a27", "a28", "a29", "a30", "a31", "a32", "a33", "a34", "a35", "a36", "a37", "a38", "a39", "a40", "a41", "a42", "a43", "a44", "a45", "a46", "a47", "a48", "a49", "a50", "a51", "a52", "a53", "a54", "a55", "a56", "a57", "a58", "a59", "a60", "a61", "a62", "a63", "a64", "a65", "a66", "a67", "a68", "a69", "a70", "a71", "a72", "a73", "a74", "a75", "a76", "a77", "a78", "a79", "a80", "a81", "a82", "a83", "a84", "a85", "a86", "a87", "a88", "a89", "a90", "a91", "a92", "a93", "a94", "a95", "a96", "a97", "a98", "a99", "a100", "a101", "a102", "a103", "a104", "a105", "a106", "a107", "a108", "a109", "a110", "a111", "a112", "a113", "a114", "a115", "a116", "a117", "a118", "a119", "a120", "a121", "a122", "a123", "a124", "a125", "a126", "a127", "a128", "a129", "a130", "a131", "a132", "a133", "a134", "a135", "a136", "a137", "a138", "a139", "a140", "a141", "a142", "a143", "a144", "a145", "a146", "a147", "a148", "a149", "a150", "a151", "a152", "a153", "a154", "a155", "a156", "a157", "a158", "a159", "a160", "a161", "a162", "a163", "a164", "a165", "a166", "a167", "a168", "a169", "a170", "a171", "a172", "a173", "a174", "a175", "a176", "a177", "a178", "a179", "a180", "a181", "a182", "a183", "a184", "a185", "a186", "a187", "a188", "a189", "a190", "a191", "a192", "a193", "a194", "a195", "a196", "a197", "a198", "a199", "a200", "a201", "a202", "a203", "a204", "a205", "a206", "a207", "a208", "a209", "a210", "a211", "a212", "a213", "a214", "a215", "a216", "a217", "a218", "a219", "a220", "a221", "a222", "a223", "a224", "a225", "a226", "a227", "a228", "a229", "a230", "a231", "a232", "a233", "a234", "a235", "a236", "a237", "a238", "a239", "a240", "a241", "a242", "a243", "a244", "a245", "a246", "a247", "a248", "a249", "a250", "a251", "a252", "a253", "a254", "a255", "s12", "s13", "s14", "s15", "s16", "s17", "s18", "s19", "s20", "s21", "s22", "s23", "s24", "s25", "s26", "s27", "s28", "s29", "s30", "s31", "s33", "s34", "s35", "s36", "s37", "s38", "s39", "s40", "s41", "s42", "s43", "s44", "s45", "s46", "s47", "s48", "s49", "s50", "s51", "s52", "s53", "s54", "s55", "s56", "s57", "s58", "s59", "s60", "s61", "s62", "s63", "s64", "s65", "s66", "s67", "s68", "s69", "s70", "s71", "s72", "s73", "s74", "s75", "s76", "s77", "s78", "s79", "s80", "s81", "s82", "s83", "s84", "s85", "s86", "s87", "s88", "s89", "s90", "s91", "s92", "s93", "s94", "s95", "s96", "s97", "s98", "s99", "vcc", "memory");
}

extern "C" void kernel_launch(void* const* d_in, const int* in_sizes, int n_in, void* d_out, int out_size, void* d_ws, size_t ws_size,
                              hipStream_t stream) {
  fused_mha_block<<<256, 256, 0, stream>>>(d_in[0], d_in[1], d_in[2], d_in[3], d_in[4], d_in[5], d_in[6], d_in[7], d_in[8], d_in[9], d_out, d_ws);
}
```
